```python
import torch

def _softmax(x, axis=-1):
    return torch.softmax(x, dim=axis)
def _gelu(x):
    return torch.nn.functional.gelu(x, approximate="tanh")
def _silu(x):
    return torch.nn.functional.silu(x)
def _sigmoid(x):
    return torch.sigmoid(x)
def _layernorm(x, axis=-1, eps=1e-5):
    m = x.mean(dim=axis, keepdim=True)
    v = x.var(dim=axis, keepdim=True, unbiased=False)
    return (x - m) / torch.sqrt(v + eps)
def _scatter_add(tgt, idx, src, axis=0):
    return tgt.clone().index_add_(axis, idx, src)
def _segment_sum(src, seg, n):
    out = torch.zeros((n,) + src.shape[1:], dtype=src.dtype)
    return out.index_add_(0, seg, src)
def _pad(x, pw):
    flat = tuple(v for pair in reversed(pw) for v in pair)
    return torch.nn.functional.pad(x, flat)

kernel_name = 'sequential_preLN__grouped_kv_mqa__swiglu_ffn__prompt'

def setup_inputs():

    _w = (lambda a: (lambda t: t.to(torch.bfloat16) if t.is_floating_point() else t)(torch.as_tensor(a)))
    def _A_setup():
        B, S, D, H = 1, 2048, 512, 32
        Dh = D // H
        return {
            "x": _w((lambda *s: torch.randn(*s, dtype=torch.bfloat16) * 0.05)(B, S, D)),
            "wq": _w((lambda *s: torch.randn(*s, dtype=torch.bfloat16) * 0.05)(D, H * Dh)),
            "wk": _w((lambda *s: torch.randn(*s, dtype=torch.bfloat16) * 0.05)(D, Dh)),
            "wv": _w((lambda *s: torch.randn(*s, dtype=torch.bfloat16) * 0.05)(D, Dh)),
            "wo": _w((lambda *s: torch.randn(*s, dtype=torch.bfloat16) * 0.05)(H * Dh, D)),
        }
    _A_in = _A_setup()

    def _B_setup():
        B, S, D, F = 1, 2048, 512, 1376
        return {
            "x": _w((lambda *s: torch.randn(*s, dtype=torch.bfloat16) * 0.05)(B, S, D)),
            "w_gate": _w((lambda *s: torch.randn(*s, dtype=torch.bfloat16) * 0.05)(D, F)),
            "w_up": _w((lambda *s: torch.randn(*s, dtype=torch.bfloat16) * 0.05)(D, F)),
            "w_down": _w((lambda *s: torch.randn(*s, dtype=torch.bfloat16) * 0.05)(F, D)),
        }
    _B_in = _B_setup()

    return {
        "A_x": _A_in["x"],
        "A_wq": _A_in["wq"],
        "A_wk": _A_in["wk"],
        "A_wv": _A_in["wv"],
        "A_wo": _A_in["wo"],
        "B_x": _B_in["x"],
        "B_w_gate": _B_in["w_gate"],
        "B_w_up": _B_in["w_up"],
        "B_w_down": _B_in["w_down"],
    }

def reference(A_x, A_wq, A_wk, A_wv, A_wo, B_x, B_w_gate, B_w_up, B_w_down):
    def _A_ref(x, wq, wk, wv, wo):
        B, S, D = x.shape
        H = 32
        Dh = D // H
        q = (lambda x, *d: x.permute(*d))((x @ wq).reshape(B, S, H, Dh), 0, 2, 1, 3)
        k = x @ wk
        v = x @ wv
        scores = torch.einsum("bhqd,bkd->bhqk", q, k) * (Dh ** -0.5)
        p = _softmax(scores, axis=-1)
        o = torch.einsum("bhqk,bkd->bhqd", p, v)
        return (lambda x, *d: x.permute(*d))(o, 0, 2, 1, 3).reshape(B, S, H * Dh) @ wo

    def _B_ref(x, w_gate, w_up, w_down):
        g = _silu(x @ w_gate)
        u = x @ w_up
        return (g * u) @ w_down

    x = A_x
    h = x + _A_ref(_layernorm(x, axis=-1), A_wq, A_wk, A_wv, A_wo)
    h2 = h + _B_ref(_layernorm(h, axis=-1), B_w_gate, B_w_up, B_w_down)
    return h2
```

```cpp
#include <hip/hip_runtime.h>
#include <hip/hip_bf16.h>
#include <stdint.h>

typedef __attribute__((ext_vector_type(8))) short bf16x8;
typedef __attribute__((ext_vector_type(4))) short s16x4;
typedef __attribute__((ext_vector_type(2))) __bf16 bf16x2;
typedef __attribute__((ext_vector_type(4))) unsigned u32x4;
typedef __attribute__((ext_vector_type(2))) unsigned u32x2;
typedef __attribute__((ext_vector_type(16))) float f32x16;
typedef __attribute__((ext_vector_type(4))) float f32x4;
typedef __attribute__((ext_vector_type(2))) float f32x2;
typedef __attribute__((address_space(3))) char* lds_ptr;
typedef unsigned short u16;

#define DI __device__ __forceinline__
#define MFMA32(a, b, c) __builtin_amdgcn_mfma_f32_32x32x16_bf16((a), (b), (c), 0, 0, 0)
#define MFMA16(a, b, c) __builtin_amdgcn_mfma_f32_16x16x32_bf16((a), (b), (c), 0, 0, 0)
#define WAIT_VM_BAR(N) asm volatile("s_waitcnt vmcnt(" #N ") lgkmcnt(0)\n\ts_barrier" ::: "memory")
#define BAR() asm volatile("s_waitcnt lgkmcnt(0)\n\ts_barrier" ::: "memory")

constexpr int SEQ = 2048, DM = 512, FF = 1376, NAT = 86;
constexpr float QSCALE = 0.25f * 1.4426950408889634f;
constexpr float LN_EPS = 1e-5f;

constexpr size_t WS_Q = 0;
constexpr size_t WS_K = WS_Q + (size_t)SEQ * DM * 2;
constexpr size_t WS_V = WS_K + 65536;
constexpr size_t WS_KMAX = WS_V + 65536;
constexpr size_t WS_O = WS_KMAX + 4096;
constexpr size_t WS_H = WS_O + (size_t)SEQ * DM * 2;
constexpr size_t WS_ST = WS_H + (size_t)SEQ * DM * 2;
constexpr size_t WS_ACT = WS_ST + (size_t)SEQ * 16 * 8;

#define STAMP_DECL
#define STAMP(i) do { } while (0)
#define STAMP_FLUSH(kid) do { } while (0)
#define ACC_BEGIN() do { } while (0)
#define ACC_END(i) do { } while (0)
DI void glds16(const void* g, unsigned lds_base) {
  unsigned sv;
  asm volatile("s_mov_b32 %0, m0\n\ts_mov_b32 m0, %2\n\ts_nop 0\n\tglobal_load_lds_dwordx4 %1, off\n\ts_mov_b32 m0, %0"
               : "=&s"(sv) : "v"(g), "s"(lds_base) : "memory");
}
DI void glds4(const void* g, unsigned lds_base) {
  unsigned sv;
  asm volatile("s_mov_b32 %0, m0\n\ts_mov_b32 m0, %2\n\ts_nop 0\n\tglobal_load_lds_dword %1, off\n\ts_mov_b32 m0, %0"
               : "=&s"(sv) : "v"(g), "s"(lds_base) : "memory");
}
DI unsigned rfl(unsigned v) { return (unsigned)__builtin_amdgcn_readfirstlane((int)v); }
DI unsigned pk2(float lo, float hi) {
  bf16x2 v; v[0] = (__bf16)lo; v[1] = (__bf16)hi;
  return __builtin_bit_cast(unsigned, v);
}
DI float bflo(unsigned u) { return __uint_as_float(u << 16); }
DI float bfhi(unsigned u) { return __uint_as_float(u & 0xffff0000u); }
DI float dot2(unsigned a, unsigned b, float c) {
  return __builtin_amdgcn_fdot2_f32_bf16(__builtin_bit_cast(bf16x2, a), __builtin_bit_cast(bf16x2, b), c, false);
}
DI bf16x8 lds_b128(lds_ptr p) { return *(const __attribute__((address_space(3))) bf16x8*)p; }
DI s16x4 lds_tr(lds_ptr p) {
  typedef short v4i16_t __attribute__((ext_vector_type(4)));
  return __builtin_bit_cast(s16x4, __builtin_amdgcn_ds_read_tr16_b64_v4i16((__attribute__((address_space(3))) v4i16_t*)p));
}
DI float swap32_sum(float v) {
  auto r = __builtin_amdgcn_permlane32_swap(__float_as_uint(v), __float_as_uint(v), false, false);
  return __uint_as_float(r[0]) + __uint_as_float(r[1]);
}
DI float swap32_max(float v) {
  auto r = __builtin_amdgcn_permlane32_swap(__float_as_uint(v), __float_as_uint(v), false, false);
  return fmaxf(__uint_as_float(r[0]), __uint_as_float(r[1]));
}
template <int CTRL, int RM>
DI float dpp(float old, float v) { return __uint_as_float((unsigned)__builtin_amdgcn_update_dpp((int)__float_as_uint(old), (int)__float_as_uint(v), CTRL, RM, 0xf, false)); }
DI float wave_sum(float v) {
  v += dpp<0x111, 0xf>(0.f, v); v += dpp<0x112, 0xf>(0.f, v); v += dpp<0x114, 0xf>(0.f, v); v += dpp<0x118, 0xf>(0.f, v);
  v += dpp<0x142, 0xa>(0.f, v); v += dpp<0x143, 0xc>(0.f, v);
  return __uint_as_float((unsigned)__builtin_amdgcn_readlane((int)__float_as_uint(v), 63));
}
DI float wave_max(float v) {
  v = fmaxf(v, dpp<0x111, 0xf>(0.f, v)); v = fmaxf(v, dpp<0x112, 0xf>(0.f, v)); v = fmaxf(v, dpp<0x114, 0xf>(0.f, v)); v = fmaxf(v, dpp<0x118, 0xf>(0.f, v));
  v = fmaxf(v, dpp<0x142, 0xa>(0.f, v)); v = fmaxf(v, dpp<0x143, 0xc>(0.f, v));
  return __uint_as_float((unsigned)__builtin_amdgcn_readlane((int)__float_as_uint(v), 63));
}
DI float quad_sum(float v) { v += dpp<0xb1, 0xf>(0.f, v); v += dpp<0x4e, 0xf>(0.f, v); return v; }

#define SB() __builtin_amdgcn_sched_barrier(0)
#define PIN(x) asm volatile("" : "+v"(x))
#define PINM(x) asm volatile("" : "+v"(x) :: "memory")
#define LDS_F32X4(p) (*(__attribute__((address_space(3))) f32x4*)(p))
#define LDS_F32X2(p) (*(__attribute__((address_space(3))) f32x2*)(p))
#define LDS_F32(p) (*(__attribute__((address_space(3))) float*)(p))
#define LDS_U32X2(p) (*(__attribute__((address_space(3))) u32x2*)(p))
#define LDS_U32X4(p) (*(__attribute__((address_space(3))) u32x4*)(p))
#define LDS_VU32(p) (*(volatile __attribute__((address_space(3))) unsigned*)(p))
#define WAIT_VM(N) asm volatile("s_waitcnt vmcnt(" #N ")" ::: "memory")
#define RAWBAR() asm volatile("s_barrier" ::: "memory")
#define LGKM0() asm volatile("s_waitcnt lgkmcnt(0)" ::: "memory")
DI bf16x8 cat8(s16x4 a, s16x4 b) { return __builtin_shufflevector(a, b, 0, 1, 2, 3, 4, 5, 6, 7); }
constexpr unsigned ONES = 0x3f803f80u;

constexpr int K1_X = 0, K1_W = 65536, K1_KV = 131072, K1_SC = 147456, K1_LDS = K1_SC + 2048 + 8192;

__global__ __launch_bounds__(512) void k1_qkv(const u16* __restrict__ x, const u16* __restrict__ wq, const u16* __restrict__ wk,
                                              const u16* __restrict__ wv, char* __restrict__ ws) {
  extern __shared__ __attribute__((aligned(16))) char smem[];
  const lds_ptr lds = (lds_ptr)smem;
  const unsigned lds0 = (unsigned)(uintptr_t)smem;
  const int tid = threadIdx.x, lane = tid & 63;
  const int w = (int)rfl(tid >> 6);
  const int bid = blockIdx.x;
  const int tb = (bid & 7) * 4 + ((bid >> 3) & 3), fb = bid >> 5;
  const bool kvwg = fb < 2;
  STAMP_DECL STAMP(0);
  if (w >= 4) {
    const int lw = w - 4;
    const u16* xs = x + (size_t)(tb * 64 + lw * 16 + (lane >> 4)) * DM;
    const int xc = (lane & 15) ^ (lane >> 4);
    const int rr = lane >> 3, pos = lane & 7;
    const u16* wsrc = wq + (size_t)(lw * 32 + rr) * DM + fb * 64 + ((pos ^ (((rr >> 1) & 1) << 2)) * 8);
    const u16* kvs = (fb ? wv : wk) + lw * 512 + lane * 8;
#define K1_ISSUE(st, KV) do { _Pragma("unroll") for (int i = 0; i < 4; ++i) { \
      glds16(xs + (size_t)i * 4 * DM + (st) * 128 + ((xc ^ (4 * i)) * 8), lds0 + K1_X + (st) * 16384 + (lw * 4 + i) * 1024); \
      glds16(wsrc + (size_t)((st) * 128 + i * 8) * DM, lds0 + K1_W + (st) * 16384 + (lw * 4 + i) * 1024); } \
    if (KV) glds16(kvs + (st) * 2048, lds0 + K1_KV + (st) * 4096 + lw * 1024); } while (0)
    if (kvwg) {
      K1_ISSUE(0, 1); K1_ISSUE(1, 1);
      WAIT_VM(9); RAWBAR(); K1_ISSUE(2, 1);
      WAIT_VM(9); RAWBAR(); K1_ISSUE(3, 1);
      WAIT_VM(9); RAWBAR();
    } else {
      K1_ISSUE(0, 0); K1_ISSUE(1, 0);
      WAIT_VM(8); RAWBAR(); K1_ISSUE(2, 0);
      WAIT_VM(8); RAWBAR(); K1_ISSUE(3, 0);
      WAIT_VM(8); RAWBAR();
    }
    WAIT_VM(0); RAWBAR();
    return;
  }
  const int r32 = lane & 31, h = lane >> 5;
  const int blk = (lane >> 4) & 1, q = (lane & 15) >> 2, p = lane & 3;
  const int tt = w & 1, ft = w >> 1;
  const int tok = tt * 32 + r32;
  lds_ptr xb[8];
#pragma unroll
  for (int i = 0; i < 8; ++i) xb[i] = lds + K1_X + tok * 256 + (((2 * i + h) ^ (tok & 15)) * 16);
  const lds_ptr wb = lds + K1_W + (8 * h + q) * 128 + (((ft * 4 + blk * 2 + (p >> 1)) ^ ((q >> 1) << 2)) * 16) + 8 * (p & 1);
  const lds_ptr kb = lds + K1_KV + (8 * h + q) * 32 + 8 * p;
  const float piv = __uint_as_float((unsigned)x[(size_t)(tb * 64 + tok) * DM] << 16);
  f32x16 acc = {}, acc2 = {};
  float cs = 0.f, cs2 = 0.f, sx = 0.f, sxx = 0.f, sxb = 0.f, sxxb = 0.f;
  s16x4 a[2][4][2]; bf16x8 b[2][4];
#define K1_STEP(bu, MM, LD, s0, sc0) do { _Pragma("unroll") for (int i = 0; i < 4; ++i) { const int s = (s0) + i; \
      if (MM) { acc = MFMA32(cat8(a[bu][i][0], a[bu][i][1]), b[bu][i], acc); PINM(acc); } \
      if (LD) { a[1 - (bu)][i][0] = lds_tr(wb + s * 2048); a[1 - (bu)][i][1] = lds_tr(wb + s * 2048 + 512); b[1 - (bu)][i] = lds_b128(xb[s & 7] + (s >> 3) * 16384); } SB(); } \
    if (MM) { \
      if (ft == (bu)) { \
        if (kvwg) { bf16x8 ka[4]; \
          _Pragma("unroll") for (int i = 0; i < 4; ++i) ka[i] = cat8(lds_tr(kb + ((sc0) + i) * 512), lds_tr(kb + ((sc0) + i) * 512 + 128)); \
          if (fb == 0) { _Pragma("unroll") for (int i = 0; i < 4; ++i) acc2 = MFMA32(ka[i], b[bu][i], acc2); } \
          else { _Pragma("unroll") for (int i = 0; i < 4; ++i) acc2 = MFMA32(b[bu][i], ka[i], acc2); } \
          PINM(acc2); SB(); \
          _Pragma("unroll") for (int i = 0; i < 4; ++i) { const u32x4 u = __builtin_bit_cast(u32x4, ka[i]); \
            cs2 = dot2(u[0], ONES, cs2); cs2 = dot2(u[1], ONES, cs2); cs2 = dot2(u[2], ONES, cs2); cs2 = dot2(u[3], ONES, cs2); } PIN(cs2); } \
        _Pragma("unroll") for (int i = 0; i < 4; ++i) { const u32x4 u = __builtin_bit_cast(u32x4, b[bu][i]); \
          sx = dot2(u[0], ONES, sx); sxx = dot2(u[0], u[0], sxx); sxb = dot2(u[1], ONES, sxb); sxxb = dot2(u[1], u[1], sxxb); \
          sx = dot2(u[2], ONES, sx); sxx = dot2(u[2], u[2], sxx); sxb = dot2(u[3], ONES, sxb); sxxb = dot2(u[3], u[3], sxxb); } \
        PIN(sx); PIN(sxx); PIN(sxb); PIN(sxxb); } \
      if (tt == (bu)) { _Pragma("unroll") for (int i = 0; i < 4; ++i) { const u32x4 u = __builtin_bit_cast(u32x4, cat8(a[bu][i][0], a[bu][i][1])); \
          cs = dot2(u[0], ONES, cs); cs = dot2(u[1], ONES, cs); cs = dot2(u[2], ONES, cs); cs = dot2(u[3], ONES, cs); } PIN(cs); } \
      SB(); } } while (0)
  STAMP(1);
#pragma unroll
  for (int st = 0; st < 4; ++st) { RAWBAR(); K1_STEP(1, st > 0, true, st * 8, st * 8 - 4); K1_STEP(0, true, true, st * 8 + 4, st * 8); }
  K1_STEP(1, true, false, 0, 28);
  STAMP(2);
  const lds_ptr sc = lds + K1_SC;
  {
    sx += sxb; sxx += sxxb;
    f32x2 d; d[0] = swap32_sum(sx - 128.f * piv); d[1] = swap32_sum(__builtin_fmaf(-2.f * piv, sx, sxx) + 128.f * piv * piv);
    cs = swap32_sum(cs); cs2 = swap32_sum(cs2);
    if (h == 0) { LDS_F32(sc + (ft * 2 + tt) * 128 + r32 * 4) = cs; LDS_F32X2(sc + 512 + (tt * 2 + ft) * 256 + r32 * 8) = d; LDS_F32(sc + 1536 + w * 128 + r32 * 4) = cs2; }
    if (kvwg && ft == 1) {
#pragma unroll
      for (int g = 0; g < 4; ++g) { f32x4 t; t[0] = acc2[4 * g]; t[1] = acc2[4 * g + 1]; t[2] = acc2[4 * g + 2]; t[3] = acc2[4 * g + 3]; LDS_F32X4(sc + 2048 + tt * 4096 + g * 1024 + lane * 16) = t; }
    }
  }
  BAR();
  float mean, rstd;
  {
    const f32x2 d = LDS_F32X2(sc + 512 + tt * 512 + r32 * 8) + LDS_F32X2(sc + 512 + tt * 512 + 256 + r32 * 8);
    const float dmn = d[0] * (1.f / DM);
    mean = piv + dmn; rstd = __builtin_amdgcn_rsqf(fmaxf(d[1] * (1.f / DM) - dmn * dmn, 0.f) + LN_EPS);
  }
  {
    const float scl = rstd * QSCALE;
    float o[16];
#pragma unroll
    for (int g = 0; g < 4; ++g) {
      const f32x4 c4 = LDS_F32X4(sc + ft * 256 + (8 * g + 4 * h) * 4) + LDS_F32X4(sc + ft * 256 + 128 + (8 * g + 4 * h) * 4);
#pragma unroll
      for (int j = 0; j < 4; ++j) o[4 * g + j] = (acc[4 * g + j] - mean * c4[j]) * scl;
    }
    u32x4 p0, p1;
#pragma unroll
    for (int i = 0; i < 4; ++i) { p0[i] = pk2(o[2 * i], o[2 * i + 1]); p1[i] = pk2(o[8 + 2 * i], o[9 + 2 * i]); }
    char* dst = ws + WS_Q + (size_t)(tb * 64 + tok) * 1024 + (fb * 4 + ft * 2) * 32 + h * 16;
    *(u32x4*)dst = p0;
    *(u32x4*)(dst + 32) = p1;
  }
  if (kvwg && ft == 0) {
    const int gt = tb * 2 + tt;
#pragma unroll
    for (int g = 0; g < 4; ++g) { const f32x4 t = LDS_F32X4(sc + 2048 + tt * 4096 + g * 1024 + lane * 16);
#pragma unroll
      for (int j = 0; j < 4; ++j) acc2[4 * g + j] += t[j]; }
    if (fb == 0) {
      float o[8];
#pragma unroll
      for (int g = 0; g < 2; ++g) {
        const f32x4 c4 = LDS_F32X4(sc + 1536 + tt * 128 + (8 * g + 4 * h) * 4) + LDS_F32X4(sc + 1536 + (tt + 2) * 128 + (8 * g + 4 * h) * 4);
#pragma unroll
        for (int j = 0; j < 4; ++j) o[4 * g + j] = (acc2[4 * g + j] - mean * c4[j]) * rstd;
      }
      u32x4 p0;
#pragma unroll
      for (int i = 0; i < 4; ++i) p0[i] = pk2(o[2 * i], o[2 * i + 1]);
      *(u32x4*)(ws + WS_K + (size_t)gt * 1024 + h * 512 + r32 * 16) = p0;
      float n2 = 0.f;
#pragma unroll
      for (int i = 0; i < 4; ++i) n2 = dot2(p0[i], p0[i], n2);
      n2 = wave_max(swap32_sum(n2));
      if (lane == 0) *(float*)(ws + WS_KMAX + gt * 4) = n2;
    } else {
      const float cv = LDS_F32(sc + 1536 + tt * 128 + r32 * 4) + LDS_F32(sc + 1536 + (tt + 2) * 128 + r32 * 4);
      const lds_ptr sp = sc + 2048 + tt * 4096;
      LGKM0();
      if (h == 0) { f32x2 st; st[0] = mean; st[1] = rstd; LDS_F32X2(sp + r32 * 8) = st; }
      float o[16];
#pragma unroll
      for (int g = 0; g < 4; ++g) {
        const f32x4 s01 = LDS_F32X4(sp + (8 * g + 4 * h) * 8), s23 = LDS_F32X4(sp + (8 * g + 4 * h) * 8 + 16);
        o[4 * g + 0] = (acc2[4 * g + 0] - s01[0] * cv) * s01[1];
        o[4 * g + 1] = (acc2[4 * g + 1] - s01[2] * cv) * s01[3];
        o[4 * g + 2] = (acc2[4 * g + 2] - s23[0] * cv) * s23[1];
        o[4 * g + 3] = (acc2[4 * g + 3] - s23[2] * cv) * s23[3];
      }
      u32x4 p0, p1;
#pragma unroll
      for (int i = 0; i < 4; ++i) { p0[i] = pk2(o[2 * i], o[2 * i + 1]); p1[i] = pk2(o[8 + 2 * i], o[9 + 2 * i]); }
      if (r32 < 16) {
        char* dst = ws + WS_V + (size_t)gt * 1024 + h * 256 + r32 * 16;
        *(u32x4*)dst = p0;
        *(u32x4*)(dst + 512) = p1;
      }
    }
  }
  STAMP(3); STAMP_FLUSH(0);
}

constexpr int K2_PSZ = 2176;
constexpr int K2_RED = 0;
constexpr int K2_Q = 64 * K2_PSZ;
constexpr int K2_NM = K2_Q + 8192;
constexpr int K2_JUNK = K2_NM + 8192;
constexpr int K2_FLG = K2_JUNK + 256;
constexpr int K2_LDS = K2_FLG + 64;
constexpr int K2_MX = 0;

#define E_(r) "v_exp_f32 v" #r ", v" #r "\n\t"
#define C_(d, a, b) "v_cvt_pk_bf16_f32 v" #d ", v" #a ", v" #b "\n\t"
#define PVPREV_ "v_mfma_f32_32x32x16_bf16 v[200:215], %8, v[236:239], v[200:215]\n\t"
#define TILE_A1 E_(168) E_(169)
#define TILE_A2 E_(170) E_(171) E_(172) E_(173) "v_mfma_f32_32x32x16_bf16 v[184:199], %6, %7, v[216:231]\n\t" E_(174) E_(175) \
    C_(232, 168, 169) C_(233, 170, 171) C_(234, 172, 173) C_(235, 174, 175) E_(176) E_(177)
#define TILE_A3 E_(178) E_(179) E_(180) E_(181) E_(182) E_(183) C_(236, 176, 177) C_(237, 178, 179) C_(238, 180, 181) C_(239, 182, 183)
#define TILE_B1 E_(184) E_(185)
#define TILE_B2 E_(186) E_(187) E_(188) E_(189) "v_mfma_f32_32x32x16_bf16 v[168:183], %6, %7, v[216:231]\n\t" E_(190) E_(191) \
    C_(232, 184, 185) C_(233, 186, 187) C_(234, 188, 189) C_(235, 190, 191) E_(192) E_(193)
#define TILE_B3 E_(194) E_(195) E_(196) E_(197) E_(198) E_(199) C_(236, 192, 193) C_(237, 194, 195) C_(238, 196, 197) C_(239, 198, 199)
#define PV0_ "v_mfma_f32_32x32x16_bf16 v[200:215], %9, v[232:235], v[200:215]\n\t"
#define PV0Z_ "v_mfma_f32_32x32x16_bf16 v[200:215], %9, v[232:235], 0\n\t"
#define K2_ASM(TXT, KN, QN, VP, V0) asm volatile(TXT : "+{v[168:183]}"(sa), "+{v[184:199]}"(sb), "+{v[200:215]}"(o), "+{v[232:235]}"(p0), "+{v[236:239]}"(p1) \
    : "{v[216:231]}"(negm), "v"(KN), "v"(QN), "v"(VP), "v"(V0))
#define K2_ASM_NOC(TXT, V0) asm volatile(TXT : "+{v[168:183]}"(sa), "+{v[184:199]}"(sb), "+{v[200:215]}"(o), "+{v[232:235]}"(p0), "+{v[236:239]}"(p1) \
    : "v"(V0), "v"(V0), "v"(V0), "v"(V0), "v"(V0))

__global__ __launch_bounds__(512) void k2_attn(const u16* __restrict__ wo, const u16* __restrict__ wg, const u16* __restrict__ wu, char* __restrict__ ws) {
  extern __shared__ __attribute__((aligned(16))) char smem[];
  const lds_ptr lds = (lds_ptr)smem;
  const unsigned lds0 = (unsigned)(uintptr_t)smem;
  const int tid = threadIdx.x, lane = tid & 63;
  const int w = (int)rfl(tid >> 6);
  const int g8 = (blockIdx.x & 7) * 32 + (blockIdx.x >> 3);
  const int r32 = lane & 31, h = lane >> 5;
  STAMP_DECL STAMP(0);
  const u32x4 qown = *(const u32x4*)(ws + WS_Q + (size_t)(g8 * 8 + (r32 & 7)) * 1024 + (w * 4 + (r32 >> 3)) * 32 + h * 16);
  const float km2l = *(const float*)(ws + WS_KMAX + lane * 4);
  bf16x8 kf[8], vf[8][2];
  u32x4 vr[8];
  const char* ksrc = ws + WS_K + (size_t)w * 8192 + lane * 16;
  const char* vsrc = ws + WS_V + (size_t)w * 8192 + (r32 >> 4) * 512 + h * 256 + (lane & 15) * 16;
#define K2_LOADKV(j) do { kf[j] = *(const bf16x8*)(ksrc + (j) * 1024); vr[j] = *(const u32x4*)(vsrc + (j) * 1024); } while (0)
#define K2_SPLITV(j) do { u32x4 a_, b_; _Pragma("unroll") for (int i_ = 0; i_ < 4; ++i_) { \
    const auto r_ = __builtin_amdgcn_permlane16_swap(vr[j][i_], ONES, false, false); a_[i_] = r_[0]; b_[i_] = r_[1]; } \
    vf[j][0] = __builtin_bit_cast(bf16x8, a_); vf[j][1] = __builtin_bit_cast(bf16x8, b_); } while (0)
  K2_LOADKV(0); K2_LOADKV(1);
  {
    float n2 = 0.f;
#pragma unroll
    for (int i = 0; i < 4; ++i) n2 = dot2(qown[i], qown[i], n2);
    const float mref = sqrtf(swap32_sum(n2) * wave_max(km2l));
    const f32x4 nm = {-mref, -mref, -mref, -mref};
    LDS_U32X4(lds + K2_Q + w * 1024 + lane * 16) = qown;
    LDS_F32X4(lds + K2_NM + w * 1024 + lane * 16) = nm;
    LDS_VU32(lds + K2_FLG + w * 4) = __any(!(mref <= 40.f)) ? 1u : 0u;
  }
  STAMP(1);
  BAR();
  STAMP(2);
  K2_LOADKV(2); K2_LOADKV(3); K2_LOADKV(4); K2_LOADKV(5); K2_LOADKV(6); K2_LOADKV(7);
  const lds_ptr qp = lds + K2_Q + lane * 16;
  if (__any(LDS_VU32(lds + K2_FLG + (lane & 7) * 4) != 0u)) {
    const f32x16 zero = {};
    for (int g = 0; g < 8; ++g) {
      const bf16x8 qf = lds_b128(qp + g * 1024);
      float mx = -3.0e38f;
#pragma unroll
      for (int j = 0; j < 8; ++j) {
        const f32x16 s = MFMA32(kf[j], qf, zero);
#pragma unroll
        for (int i = 0; i < 16; ++i) mx = fmaxf(mx, s[i]);
      }
      mx = swap32_max(mx);
      if (h == 0) LDS_F32(lds + K2_MX + (g * 8 + w) * 128 + r32 * 4) = mx;
    }
    BAR();
    float mx = LDS_F32(lds + K2_MX + w * 1024 + r32 * 4);
#pragma unroll
    for (int i = 1; i < 8; ++i) mx = fmaxf(mx, LDS_F32(lds + K2_MX + w * 1024 + i * 128 + r32 * 4));
    const f32x4 nm = {-mx, -mx, -mx, -mx};
    LDS_F32X4(lds + K2_NM + w * 1024 + lane * 16) = nm;
    BAR();
  }
  f32x16 negm;
#define K2_LOAD_NEGM(g) do { const lds_ptr np_ = lds + K2_NM + (g) * 1024 + lane * 16; \
    _Pragma("unroll") for (int i_ = 0; i_ < 4; ++i_) { const f32x4 t_ = *(volatile __attribute__((address_space(3))) f32x4*)(np_); \
      negm[4 * i_] = t_[0]; negm[4 * i_ + 1] = t_[1]; negm[4 * i_ + 2] = t_[2]; negm[4 * i_ + 3] = t_[3]; } } while (0)
  K2_LOAD_NEGM(0);
  f32x16 o = {}, sa, sb = {};
  u32x4 p0 = {}, p1 = {};
  bf16x8 qf = lds_b128(qp);
  sa = MFMA32(kf[0], qf, negm);
  const lds_ptr redw = lds + K2_RED + w * K2_PSZ + lane * 16;
#define K2_WRITE_PARTIAL(g) do { const lds_ptr rp_ = redw + (g) * (8 * K2_PSZ); f32x4 t0_, t1_; \
    t0_[0] = o[0]; t0_[1] = o[1]; t0_[2] = o[2]; t0_[3] = o[3]; t1_[0] = o[4]; t1_[1] = o[5]; t1_[2] = o[6]; t1_[3] = o[7]; \
    LDS_F32X4(rp_) = t0_; LDS_F32X4(rp_ + 1024) = t1_; LDS_F32(rp_ + 2048 - lane * 16 + r32 * 4) = o[8]; } while (0)
#define VMWAIT0() __builtin_amdgcn_s_waitcnt(0x0F70)
#define SPL(j) do { if (FIRST_) { K2_SPLITV(j); asm volatile("s_nop 1" : "+v"(vf[j][0]), "+v"(vf[j][1])); } } while (0)
#define K2_ITER(g, FIRST) do { constexpr bool FIRST_ = FIRST; \
    SPL(0); \
    if (FIRST) K2_ASM(TILE_A1 TILE_A2 PV0Z_ TILE_A3, kf[1], qf, vf[0][0], vf[0][0]); \
    else { K2_ASM(TILE_A1 PVPREV_ TILE_A2, kf[1], qf, vf[7][1], vf[0][0]); K2_WRITE_PARTIAL((g) - 1); K2_ASM(PV0Z_ TILE_A3, kf[1], qf, vf[7][1], vf[0][0]); } \
    const bf16x8 qn = lds_b128(qp + (((g) + 1) & 7) * 1024); \
    SPL(1); K2_ASM(TILE_B1 PVPREV_ TILE_B2 PV0_ TILE_B3, kf[2], qf, vf[0][1], vf[1][0]); \
    SPL(2); K2_ASM(TILE_A1 PVPREV_ TILE_A2 PV0_ TILE_A3, kf[3], qf, vf[1][1], vf[2][0]); \
    SPL(3); K2_ASM(TILE_B1 PVPREV_ TILE_B2 PV0_ TILE_B3, kf[4], qf, vf[2][1], vf[3][0]); \
    SPL(4); K2_ASM(TILE_A1 PVPREV_ TILE_A2 PV0_ TILE_A3, kf[5], qf, vf[3][1], vf[4][0]); \
    SPL(5); K2_ASM(TILE_B1 PVPREV_ TILE_B2 PV0_ TILE_B3, kf[6], qf, vf[4][1], vf[5][0]); \
    SPL(6); K2_ASM(TILE_A1 PVPREV_ TILE_A2, kf[7], qf, vf[5][1], vf[6][0]); \
    K2_LOAD_NEGM(((g) + 1) & 7); \
    K2_ASM_NOC(PV0_ TILE_A3, vf[6][0]); \
    SPL(7); K2_ASM(TILE_B1 PVPREV_ TILE_B2 PV0_ TILE_B3, kf[0], qn, vf[6][1], vf[7][0]); \
    qf = qn; } while (0)

  STAMP(3);
  asm volatile("s_nop 15" : "+{v[168:183]}"(sa));
  K2_ITER(0, true);
  VMWAIT0();
  for (int g = 1; g < 8; ++g) {
    if (g <= 3) {
      const int wi = (blockIdx.x >> 3) * 8 + w;
      const char* src = g == 1 ? (const char*)wo + (wi * 16 + (lane & 15)) * 128 : (const char*)(g == 2 ? wg : wu) + (wi * 43 + (lane < 43 ? lane : 42)) * 128;
      glds4(src, lds0 + K2_JUNK);
    }
    K2_ITER(g, false);
  }
  STAMP(4);
  K2_ASM("s_nop 1\n\t" PVPREV_ "s_nop 15", kf[0], qf, vf[7][1], vf[7][0]);
  K2_WRITE_PARTIAL(7);
  BAR();
  STAMP(5);
  {
    const lds_ptr rp = lds + K2_RED + w * (8 * K2_PSZ) + lane * 16;
    f32x4 t0[8], t1[8]; float l[8];
#pragma unroll
    for (int i = 0; i < 8; ++i) { t0[i] = LDS_F32X4(rp + i * K2_PSZ); t1[i] = LDS_F32X4(rp + i * K2_PSZ + 1024); l[i] = LDS_F32(rp + i * K2_PSZ + 2048 - lane * 16 + r32 * 4); }
    const f32x4 a = ((t0[0] + t0[1]) + (t0[2] + t0[3])) + ((t0[4] + t0[5]) + (t0[6] + t0[7]));
    const f32x4 b = ((t1[0] + t1[1]) + (t1[2] + t1[3])) + ((t1[4] + t1[5]) + (t1[6] + t1[7]));
    const float inv = 1.f / (((l[0] + l[1]) + (l[2] + l[3])) + ((l[4] + l[5]) + (l[6] + l[7])));
    u32x4 ob;
    ob[0] = pk2(a[0] * inv, a[1] * inv); ob[1] = pk2(a[2] * inv, a[3] * inv); ob[2] = pk2(b[0] * inv, b[1] * inv); ob[3] = pk2(b[2] * inv, b[3] * inv);
    const int tok = g8 * 8 + (r32 & 7);
    *(u32x4*)(ws + WS_O + (size_t)(tok >> 5) * 32768 + (w * 4 + (r32 >> 3)) * 1024 + h * 512 + (tok & 31) * 16) = ob;
  }
  STAMP(6); STAMP_FLUSH(1);
}

constexpr int K4_SLOT = 32768, K4_LDS = 5 * K4_SLOT;

template <int MODE>
__global__ __launch_bounds__(512) void k64(const u16* __restrict__ W, const u16* __restrict__ x, char* __restrict__ ws, u16* __restrict__ out) {
  constexpr int NKS = MODE ? NAT : 32;
  extern __shared__ __attribute__((aligned(16))) char smem[];
  const lds_ptr lds = (lds_ptr)smem;
  const unsigned lds0 = (unsigned)(uintptr_t)smem;
  const int tid = threadIdx.x, lane = tid & 63;
  const int w = (int)rfl(tid >> 6);
  const int bid = blockIdx.x;
  const int tb = (bid & 7) * 4 + ((bid >> 3) & 3), nb = bid >> 5;
  const int r32 = lane & 31, h = lane >> 5;
  STAMP_DECL STAMP(0);
  if (w >= 4) {
    const int lw = w - 4;
    const int rr = lane >> 3, pos = lane & 7;
    const char* wsrc = (const char*)W + (size_t)(lw * 32 + rr) * 1024 + nb * 128 + (pos ^ (((rr >> 1) & 1) << 2)) * 16;
    const char* asrc = ws + (MODE ? WS_ACT : WS_O) + ((size_t)(tb * 2 + (lw >> 1)) * NKS + (lw & 1) * 4) * 1024 + lane * 16;
#define K4_ISSUE(st) do { const unsigned sl_ = lds0 + ((st) % 5) * K4_SLOT; _Pragma("unroll") for (int i = 0; i < 4; ++i) { \
    const bool oka_ = (st) < 10 || !((lw & 1) && i >= 2), okw_ = (st) < 10 || lw < 3; \
    glds16(asrc + (oka_ ? (st) * 8192 + i * 1024 : 0), sl_ + 16384 + (lw * 4 + i) * 1024); \
    glds16(wsrc + (okw_ ? (size_t)(st) * 131072 + i * 8192 : (size_t)0), sl_ + (lw * 4 + i) * 1024); } } while (0)
    if (MODE == 0) {
      K4_ISSUE(0); K4_ISSUE(1);
      WAIT_VM(8); RAWBAR(); K4_ISSUE(2);
      WAIT_VM(8); RAWBAR(); K4_ISSUE(3);
      WAIT_VM(8); RAWBAR();
      WAIT_VM(0); RAWBAR();
    } else {
      K4_ISSUE(0); K4_ISSUE(1); K4_ISSUE(2);
      WAIT_VM(16); RAWBAR(); K4_ISSUE(3); K4_ISSUE(4);
      WAIT_VM(24); RAWBAR(); K4_ISSUE(5);
      WAIT_VM(24); RAWBAR(); K4_ISSUE(6);
      WAIT_VM(24); RAWBAR(); K4_ISSUE(7);
      WAIT_VM(24); RAWBAR(); K4_ISSUE(8);
      WAIT_VM(24); RAWBAR(); K4_ISSUE(9);
      WAIT_VM(24); RAWBAR(); K4_ISSUE(10);
      WAIT_VM(24); RAWBAR();
      WAIT_VM(16); RAWBAR();
      WAIT_VM(8); RAWBAR();
      WAIT_VM(0); RAWBAR();
    }
    STAMP(7); STAMP_FLUSH(MODE ? 4 : 2);
    return;
  }
  const int nt = w & 1, tt = w >> 1;
  const int tok = tb * 64 + tt * 32 + r32;
  const int ncol = nb * 64 + nt * 32 + h * 16;
  const int blk = (lane >> 4) & 1, q = (lane & 15) >> 2, p = lane & 3;
  char* hfrag = ws + WS_H + (size_t)(tok >> 5) * 32768 + (ncol >> 3) * 512 + r32 * 16;
  u32x4 h0, h1;
  if (MODE) { h0 = *(const u32x4*)(hfrag); h1 = *(const u32x4*)(hfrag + 512); }
  else { h0 = *(const u32x4*)(x + (size_t)tok * DM + ncol); h1 = *(const u32x4*)(x + (size_t)tok * DM + ncol + 8); }
  f32x16 acc = {};
  const int cbyte = nt * 64 + (16 * (p & 1) + 8 * blk + 4 * (p >> 1)) * 2;
  lds_ptr ap[3], bp[3];
#pragma unroll
  for (int u = 0; u < 3; ++u) { ap[u] = lds + u * 2 * K4_SLOT + (4 * h + q) * 128 + (cbyte ^ ((q >> 1) << 6)); bp[u] = lds + u * 2 * K4_SLOT + 16384 + tt * 8192 + lane * 16; }
  s16x4 fa[2][4][2]; bf16x8 fb_[2][4];
#define K4_U(st) (((st) % 5) >> 1)
#define K4_O(st) ((((st) % 5) & 1) * K4_SLOT)
#define K4_LD1(bu, st, k0, k) do { fa[bu][k][0] = lds_tr(ap[K4_U(st)] + K4_O(st) + ((k0) + (k)) * 2048); fa[bu][k][1] = lds_tr(ap[K4_U(st)] + K4_O(st) + ((k0) + (k)) * 2048 + 1024); \
    fb_[bu][k] = lds_b128(bp[K4_U(st)] + K4_O(st) + ((k0) + (k)) * 1024); } while (0)
#define K4_STEP(bu, NM, NL, st, k0) do { _Pragma("unroll") for (int k = 0; k < 4; ++k) { \
    if (k < (NM)) { acc = MFMA32(cat8(fa[bu][k][0], fa[bu][k][1]), fb_[bu][k], acc); PINM(acc); } \
    if (k < (NL)) K4_LD1(1 - (bu), st, k0, k); SB(); } } while (0)
#define K4_STAGE(st, NK2) do { BAR(); K4_STEP(1, (st) > 0 ? 4 : 0, 4, st, 0); K4_STEP(0, 4, NK2, st, 4); } while (0)
  STAMP(1);
  K4_STAGE(0, 4); STAMP(2); K4_STAGE(1, 4); STAMP(3); K4_STAGE(2, 4); K4_STAGE(3, 4);
  if (MODE) {
    K4_STAGE(4, 4); STAMP(4); K4_STAGE(5, 4); K4_STAGE(6, 4); K4_STAGE(7, 4); K4_STAGE(8, 4); K4_STAGE(9, 4); STAMP(5); K4_STAGE(10, 2);
    K4_STEP(1, 2, 0, 0, 0);
  } else {
    K4_STEP(1, 4, 0, 0, 0);
  }
  STAMP(6);
  u32x4 o0, o1;
#pragma unroll
  for (int i = 0; i < 4; ++i) {
    o0[i] = pk2(acc[2 * i] + bflo(h0[i]), acc[2 * i + 1] + bfhi(h0[i]));
    o1[i] = pk2(acc[8 + 2 * i] + bflo(h1[i]), acc[9 + 2 * i] + bfhi(h1[i]));
  }
  if (MODE) {
    u16* dst = out + (size_t)tok * DM + ncol;
    *(u32x4*)dst = o0;
    *(u32x4*)(dst + 8) = o1;
  } else {
    *(u32x4*)hfrag = o0;
    *(u32x4*)(hfrag + 512) = o1;
    float v[16], s = 0.f;
#pragma unroll
    for (int i = 0; i < 4; ++i) { v[2 * i] = bflo(o0[i]); v[2 * i + 1] = bfhi(o0[i]); v[8 + 2 * i] = bflo(o1[i]); v[9 + 2 * i] = bfhi(o1[i]); }
#pragma unroll
    for (int i = 0; i < 16; ++i) s += v[i];
    const float m32 = swap32_sum(s) * (1.f / 32);
    float ss = 0.f;
#pragma unroll
    for (int i = 0; i < 16; ++i) ss += (v[i] - m32) * (v[i] - m32);
    f32x2 st; st[0] = m32; st[1] = swap32_sum(ss);
    if (h == 0) *(f32x2*)(ws + WS_ST + ((size_t)((tok >> 5) * 16 + nb * 2 + nt) * 32 + r32) * 8) = st;
  }
  STAMP(7); STAMP_FLUSH(MODE ? 4 : 2);
}

constexpr int K3_SLOT = 40960, K3_LDS = 4 * K3_SLOT;

__global__ __launch_bounds__(512) void k3_gateup(const u16* __restrict__ wg, const u16* __restrict__ wu, const u16* __restrict__ wd,
                                                 char* __restrict__ ws) {
  extern __shared__ __attribute__((aligned(16))) char smem[];
  const lds_ptr lds = (lds_ptr)smem;
  const unsigned lds0 = (unsigned)(uintptr_t)smem;
  const int tid = threadIdx.x, lane = tid & 63;
  const int w = (int)rfl(tid >> 6);
  const int bid = blockIdx.x, jx = bid >> 3;
  if (jx >= 30) return;
  const int rb = (bid & 7) * 2 + (jx & 1), cb = jx >> 1;
  const int at0 = cb * 6 > 80 ? 80 : cb * 6;
  STAMP_DECL STAMP(0);
  if (w >= 4) {
    const int lw = w - 4;
    const char* bsrc = ws + WS_H + (size_t)(rb * 4 + lw) * 32768 + lane * 16;
    const char* wsrc[6];
#pragma unroll
    for (int i = 0; i < 6; ++i) {
      const int id = ((lw & 1) * 6 + i) * 64 + lane; const int row = id / 12, ch = id - row * 12;
      wsrc[i] = (const char*)((lw >> 1) ? wu : wg) + (size_t)row * (FF * 2) + at0 * 32 + (ch ^ (lw & 2)) * 16;
    }
#define K3_ISSUE(st) do { const unsigned sl_ = lds0 + ((st) & 3) * K3_SLOT; \
    _Pragma("unroll") for (int i = 0; i < 4; ++i) glds16(bsrc + (st) * 4096 + i * 1024, sl_ + 24576 + (lw * 4 + i) * 1024); \
    _Pragma("unroll") for (int i = 0; i < 6; ++i) glds16(wsrc[i] + (size_t)(st) * (64 * FF * 2), sl_ + (lw * 6 + i) * 1024); } while (0)
    K3_ISSUE(0); K3_ISSUE(1);
    WAIT_VM(10); RAWBAR(); K3_ISSUE(2);
    WAIT_VM(10); RAWBAR(); K3_ISSUE(3); K3_ISSUE(4);
    WAIT_VM(20); RAWBAR(); K3_ISSUE(5);
    WAIT_VM(20); RAWBAR(); K3_ISSUE(6);
    WAIT_VM(20); RAWBAR(); K3_ISSUE(7);
    WAIT_VM(20); RAWBAR();
    WAIT_VM(10); RAWBAR();
    WAIT_VM(0); RAWBAR();
    return;
  }
  const int tp = w & 1, ag = w >> 1;
  const int r32 = lane & 31, h = lane >> 5, blk = (lane >> 4) & 1, q = (lane & 15) >> 2, p = lane & 3;
  f32x2 lp[2][8];
#pragma unroll
  for (int t = 0; t < 2; ++t)
#pragma unroll
    for (int i = 0; i < 8; ++i) lp[t][i] = *(const f32x2*)(ws + WS_ST + ((size_t)((rb * 4 + tp * 2 + t) * 16 + h * 8 + i) * 32 + r32) * 8);
  f32x16 acc[2][3] = {};
  float cs[3] = {0.f, 0.f, 0.f}, csh[3] = {0.f, 0.f, 0.f};
  lds_ptr ap[2][3], bp[2];
#pragma unroll
  for (int u = 0; u < 2; ++u) {
#pragma unroll
    for (int i = 0; i < 3; ++i) ap[u][i] = lds + u * 2 * K3_SLOT + blk * 12288 + (8 * h + q) * 192 + ((ag * 3 + i) ^ blk) * 32 + 8 * p;
    bp[u] = lds + u * 2 * K3_SLOT + 24576 + tp * 8192 + lane * 16;
  }
  s16x4 fa[2][3][2]; bf16x8 fb_[2][2];
#define K3_AO(st, k) (((st) & 1) * K3_SLOT + (k) * 3072)
#define K3_BO(st, k) (((st) & 1) * K3_SLOT + (k) * 1024)
#define K3_U(st) (((st) >> 1) & 1)
#define K3_LD(bu, st, k) do { fb_[bu][0] = lds_b128(bp[K3_U(st)] + K3_BO(st, k)); fb_[bu][1] = lds_b128(bp[K3_U(st)] + K3_BO(st, k) + 4096); \
    _Pragma("unroll") for (int i = 0; i < 3; ++i) { fa[bu][i][0] = lds_tr(ap[K3_U(st)][i] + K3_AO(st, k)); fa[bu][i][1] = lds_tr(ap[K3_U(st)][i] + K3_AO(st, k) + 768); } } while (0)
#define K3_CS(i, u) do { cs[i] += bflo(u); csh[i] += bfhi(u); PIN(cs[i]); PINM(csh[i]); } while (0)
#define K3_STEP(bu, LOAD, st, k) do { _Pragma("unroll") for (int i = 0; i < 3; ++i) { \
    const bf16x8 a_ = cat8(fa[bu][i][0], fa[bu][i][1]); const u32x2 au = __builtin_bit_cast(u32x2, fa[bu][i][TP]); \
    acc[0][i] = MFMA32(a_, fb_[bu][0], acc[0][i]); PINM(acc[0][i]); \
    if (LOAD) { if (i < 2) fb_[1 - (bu)][i] = lds_b128(bp[K3_U(st)] + K3_BO(st, k) + i * 4096); fa[1 - (bu)][i][0] = lds_tr(ap[K3_U(st)][i] + K3_AO(st, k)); } \
    K3_CS(i, au[0]); SB(); \
    acc[1][i] = MFMA32(a_, fb_[bu][1], acc[1][i]); PINM(acc[1][i]); \
    if (LOAD) fa[1 - (bu)][i][1] = lds_tr(ap[K3_U(st)][i] + K3_AO(st, k) + 768); \
    K3_CS(i, au[1]); SB(); } } while (0)
#define K3_STAGE(st) do { BAR(); if ((st) == 0) K3_LD(0, 0, 0); else K3_STEP(1, true, st, 0); K3_STEP(0, true, st, 1); K3_STEP(1, true, st, 2); K3_STEP(0, true, st, 3); } while (0)
#define K3_ALL() do { K3_STAGE(0); STAMP(2); K3_STAGE(1); STAMP(3); K3_STAGE(2); K3_STAGE(3); STAMP(4); K3_STAGE(4); K3_STAGE(5); K3_STAGE(6); STAMP(5); K3_STAGE(7); \
    K3_STEP(1, false, 0, 0); } while (0)
  float mean[2], rstd[2];
#pragma unroll
  for (int t = 0; t < 2; ++t) {
    float s = 0.f;
#pragma unroll
    for (int i = 0; i < 8; ++i) s += lp[t][i][0];
    mean[t] = swap32_sum(s) * (1.f / 16);
    float m2 = 0.f;
#pragma unroll
    for (int i = 0; i < 8; ++i) { const float d = lp[t][i][0] - mean[t]; m2 += lp[t][i][1] + 32.f * d * d; }
    rstd[t] = __builtin_amdgcn_rsqf(swap32_sum(m2) * (1.f / DM) + LN_EPS);
    PIN(mean[t]); PIN(rstd[t]);
  }
  unsigned junk0, junk1;
  {
    const int l0 = ((jx * 8 + w * 2) * 46 + (lane < 46 ? lane : 45)) * 128, l1 = l0 + 46 * 128;
    asm volatile("global_load_dword %0, %2, off\n\tglobal_load_dword %1, %3, off" : "=&v"(junk0), "=&v"(junk1)
                 : "v"((const char*)wd + (l0 < FF * DM * 2 ? l0 : 0)), "v"((const char*)wd + (l1 < FF * DM * 2 ? l1 : 0)) : "memory");
  }
  STAMP(1);
  if (tp == 0) { constexpr int TP = 0; K3_ALL(); } else { constexpr int TP = 1; K3_ALL(); }
  STAMP(6);
  asm volatile("s_waitcnt vmcnt(0)" :: "v"(junk0), "v"(junk1) : "memory");
  const lds_ptr sc = lds + ag * 1024;
#pragma unroll
  for (int i = 0; i < 3; ++i) { const float c = swap32_sum(cs[i] + csh[i]); if (h == 0) LDS_F32(sc + tp * 512 + i * 128 + r32 * 4) = c; }
  BAR();
#pragma unroll
  for (int i = 0; i < 3; ++i) {
    f32x4 c4[4];
#pragma unroll
    for (int g = 0; g < 4; ++g) c4[g] = LDS_F32X4(sc + i * 128 + (8 * g + 4 * h) * 4) + LDS_F32X4(sc + 512 + i * 128 + (8 * g + 4 * h) * 4);
#pragma unroll
    for (int t = 0; t < 2; ++t) {
      const float ke = -1.4426950408889634f * rstd[t], r2 = rstd[t] * rstd[t];
      const f32x2 nm2 = {-mean[t], -mean[t]}, ke2 = {ke, ke}, r22 = {r2, r2}, one2 = {1.f, 1.f};
      f32x2 tg[4], tu[4], e[4];
#pragma unroll
      for (int j = 0; j < 4; ++j) {
        const f32x2 ag2 = {acc[t][i][2 * j], acc[t][i][2 * j + 1]}, au2 = {acc[t][i][8 + 2 * j], acc[t][i][9 + 2 * j]};
        const f32x2 cg2 = {c4[j >> 1][2 * (j & 1)], c4[j >> 1][2 * (j & 1) + 1]}, cu2 = {c4[2 + (j >> 1)][2 * (j & 1)], c4[2 + (j >> 1)][2 * (j & 1) + 1]};
        tg[j] = __builtin_elementwise_fma(nm2, cg2, ag2); tu[j] = __builtin_elementwise_fma(nm2, cu2, au2);
        e[j] = tg[j] * ke2;
      }
#pragma unroll
      for (int j = 0; j < 4; ++j) { e[j][0] = __builtin_amdgcn_exp2f(e[j][0]); e[j][1] = __builtin_amdgcn_exp2f(e[j][1]); tg[j] = (tg[j] * tu[j]) * r22; }
#pragma unroll
      for (int j = 0; j < 4; ++j) e[j] += one2;
#pragma unroll
      for (int j = 0; j < 4; ++j) { e[j][0] = __builtin_amdgcn_rcpf(e[j][0]); e[j][1] = __builtin_amdgcn_rcpf(e[j][1]); }
      u32x4 pk;
#pragma unroll
      for (int j = 0; j < 4; ++j) { const f32x2 a2 = tg[j] * e[j]; pk[j] = pk2(a2[0], a2[1]); }
      *(u32x4*)(ws + WS_ACT + ((size_t)(rb * 4 + tp * 2 + t) * NAT + at0 + ag * 3 + i) * 1024 + lane * 16) = pk;
    }
  }
  STAMP(7); STAMP_FLUSH(3);
}

extern "C" void kernel_launch(void* const* d_in, const int* in_sizes, int n_in, void* d_out, int out_size, void* d_ws, size_t ws_size,
                              hipStream_t stream) {
  const u16* x = (const u16*)d_in[0];
  const u16* wq = (const u16*)d_in[1];
  const u16* wk = (const u16*)d_in[2];
  const u16* wv = (const u16*)d_in[3];
  const u16* wo = (const u16*)d_in[4];
  const u16* wg = (const u16*)d_in[6];
  const u16* wu = (const u16*)d_in[7];
  const u16* wd = (const u16*)d_in[8];
  char* ws = (char*)d_ws;
  (void)hipFuncSetAttribute((const void*)k1_qkv, hipFuncAttributeMaxDynamicSharedMemorySize, K1_LDS);
  (void)hipFuncSetAttribute((const void*)k2_attn, hipFuncAttributeMaxDynamicSharedMemorySize, K2_LDS);
  (void)hipFuncSetAttribute((const void*)k3_gateup, hipFuncAttributeMaxDynamicSharedMemorySize, K3_LDS);
  (void)hipFuncSetAttribute((const void*)k64<0>, hipFuncAttributeMaxDynamicSharedMemorySize, K4_LDS);
  (void)hipFuncSetAttribute((const void*)k64<1>, hipFuncAttributeMaxDynamicSharedMemorySize, K4_LDS);
  k1_qkv<<<256, 512, K1_LDS, stream>>>(x, wq, wk, wv, ws);
  k2_attn<<<256, 512, K2_LDS, stream>>>(wo, wg, wu, ws);
  k64<0><<<256, 512, K4_LDS, stream>>>(wo, x, ws, (u16*)d_out);
  k3_gateup<<<256, 512, K3_LDS, stream>>>(wg, wu, wd, ws);
  k64<1><<<256, 512, K4_LDS, stream>>>(wd, x, ws, (u16*)d_out);
}
```

```cpp
#include <hip/hip_runtime.h>
#include <hip/hip_bf16.h>
#include <stdint.h>

typedef __attribute__((ext_vector_type(8))) short bf16x8;
typedef __attribute__((ext_vector_type(4))) short s16x4;
typedef __attribute__((ext_vector_type(2))) __bf16 bf16x2;
typedef __attribute__((ext_vector_type(4))) unsigned u32x4;
typedef __attribute__((ext_vector_type(2))) unsigned u32x2;
typedef __attribute__((ext_vector_type(16))) float f32x16;
typedef __attribute__((ext_vector_type(4))) float f32x4;
typedef __attribute__((ext_vector_type(2))) float f32x2;
typedef __attribute__((address_space(3))) char* lds_ptr;
typedef unsigned short u16;

#define DI __device__ __forceinline__
#define MFMA32(a, b, c) __builtin_amdgcn_mfma_f32_32x32x16_bf16((a), (b), (c), 0, 0, 0)
#define MFMA16(a, b, c) __builtin_amdgcn_mfma_f32_16x16x32_bf16((a), (b), (c), 0, 0, 0)
#define WAIT_VM_BAR(N) asm volatile("s_waitcnt vmcnt(" #N ") lgkmcnt(0)\n\ts_barrier" ::: "memory")
#define BAR() asm volatile("s_waitcnt lgkmcnt(0)\n\ts_barrier" ::: "memory")

constexpr int SEQ = 2048, DM = 512, FF = 1376, NAT = 86;
constexpr float QSCALE = 0.25f * 1.4426950408889634f;
constexpr float LN_EPS = 1e-5f;

constexpr size_t WS_Q = 0;
constexpr size_t WS_K = WS_Q + (size_t)SEQ * DM * 2;
constexpr size_t WS_V = WS_K + 65536;
constexpr size_t WS_KMAX = WS_V + 65536;
constexpr size_t WS_WO = WS_KMAX + 4096;
constexpr size_t WS_H = WS_WO + (size_t)DM * DM * 2;
constexpr size_t WS_ST = WS_H + (size_t)SEQ * DM * 2;
constexpr size_t WS_ACT = WS_ST + (size_t)SEQ * 16 * 8;

#define STAMP_DECL
#define STAMP(i) do { } while (0)
#define STAMP_FLUSH(kid) do { } while (0)
#define ACC_BEGIN() do { } while (0)
#define ACC_END(i) do { } while (0)
DI void glds16(const void* g, unsigned lds_base) {
  unsigned sv;
  asm volatile("s_mov_b32 %0, m0\n\ts_mov_b32 m0, %2\n\ts_nop 0\n\tglobal_load_lds_dwordx4 %1, off\n\ts_mov_b32 m0, %0"
               : "=&s"(sv) : "v"(g), "s"(lds_base) : "memory");
}
DI void glds4(const void* g, unsigned lds_base) {
  unsigned sv;
  asm volatile("s_mov_b32 %0, m0\n\ts_mov_b32 m0, %2\n\ts_nop 0\n\tglobal_load_lds_dword %1, off\n\ts_mov_b32 m0, %0"
               : "=&s"(sv) : "v"(g), "s"(lds_base) : "memory");
}
DI unsigned rfl(unsigned v) { return (unsigned)__builtin_amdgcn_readfirstlane((int)v); }
DI unsigned pk2(float lo, float hi) {
  bf16x2 v; v[0] = (__bf16)lo; v[1] = (__bf16)hi;
  return __builtin_bit_cast(unsigned, v);
}
DI float bflo(unsigned u) { return __uint_as_float(u << 16); }
DI float bfhi(unsigned u) { return __uint_as_float(u & 0xffff0000u); }
DI float dot2(unsigned a, unsigned b, float c) {
  return __builtin_amdgcn_fdot2_f32_bf16(__builtin_bit_cast(bf16x2, a), __builtin_bit_cast(bf16x2, b), c, false);
}
DI bf16x8 lds_b128(lds_ptr p) { return *(const __attribute__((address_space(3))) bf16x8*)p; }
DI s16x4 lds_tr(lds_ptr p) {
  typedef short v4i16_t __attribute__((ext_vector_type(4)));
  return __builtin_bit_cast(s16x4, __builtin_amdgcn_ds_read_tr16_b64_v4i16((__attribute__((address_space(3))) v4i16_t*)p));
}
DI float swap32_sum(float v) {
  auto r = __builtin_amdgcn_permlane32_swap(__float_as_uint(v), __float_as_uint(v), false, false);
  return __uint_as_float(r[0]) + __uint_as_float(r[1]);
}
DI float swap16_sum(float v) {
  auto r = __builtin_amdgcn_permlane16_swap(__float_as_uint(v), __float_as_uint(v), false, false);
  return __uint_as_float(r[0]) + __uint_as_float(r[1]);
}
DI float swap32_max(float v) {
  auto r = __builtin_amdgcn_permlane32_swap(__float_as_uint(v), __float_as_uint(v), false, false);
  return fmaxf(__uint_as_float(r[0]), __uint_as_float(r[1]));
}
template <int CTRL, int RM>
DI float dpp(float old, float v) { return __uint_as_float((unsigned)__builtin_amdgcn_update_dpp((int)__float_as_uint(old), (int)__float_as_uint(v), CTRL, RM, 0xf, false)); }
DI float wave_sum(float v) {
  v += dpp<0x111, 0xf>(0.f, v); v += dpp<0x112, 0xf>(0.f, v); v += dpp<0x114, 0xf>(0.f, v); v += dpp<0x118, 0xf>(0.f, v);
  v += dpp<0x142, 0xa>(0.f, v); v += dpp<0x143, 0xc>(0.f, v);
  return __uint_as_float((unsigned)__builtin_amdgcn_readlane((int)__float_as_uint(v), 63));
}
DI float wave_max(float v) {
  v = fmaxf(v, dpp<0x111, 0xf>(0.f, v)); v = fmaxf(v, dpp<0x112, 0xf>(0.f, v)); v = fmaxf(v, dpp<0x114, 0xf>(0.f, v)); v = fmaxf(v, dpp<0x118, 0xf>(0.f, v));
  v = fmaxf(v, dpp<0x142, 0xa>(0.f, v)); v = fmaxf(v, dpp<0x143, 0xc>(0.f, v));
  return __uint_as_float((unsigned)__builtin_amdgcn_readlane((int)__float_as_uint(v), 63));
}
DI float quad_sum(float v) { v += dpp<0xb1, 0xf>(0.f, v); v += dpp<0x4e, 0xf>(0.f, v); return v; }

#define SB() __builtin_amdgcn_sched_barrier(0)
#define PIN(x) asm volatile("" : "+v"(x))
#define PINM(x) asm volatile("" : "+v"(x) :: "memory")
#define LDS_F32X4(p) (*(__attribute__((address_space(3))) f32x4*)(p))
#define LDS_F32X2(p) (*(__attribute__((address_space(3))) f32x2*)(p))
#define LDS_F32(p) (*(__attribute__((address_space(3))) float*)(p))
#define LDS_U32X2(p) (*(__attribute__((address_space(3))) u32x2*)(p))
#define LDS_U32X4(p) (*(__attribute__((address_space(3))) u32x4*)(p))
#define LDS_VU32(p) (*(volatile __attribute__((address_space(3))) unsigned*)(p))
#define WAIT_VM(N) asm volatile("s_waitcnt vmcnt(" #N ")" ::: "memory")
#define RAWBAR() asm volatile("s_barrier" ::: "memory")
#define LGKM0() asm volatile("s_waitcnt lgkmcnt(0)" ::: "memory")
DI bf16x8 cat8(s16x4 a, s16x4 b) { return __builtin_shufflevector(a, b, 0, 1, 2, 3, 4, 5, 6, 7); }
constexpr unsigned ONES = 0x3f803f80u;

constexpr int K1_X = 0, K1_W = 65536, K1_KV = 131072, K1_SC = 147456, K1_LDS = K1_SC + 2048 + 8192;

__global__ __launch_bounds__(512) void k1_qkv(const u16* __restrict__ x, const u16* __restrict__ wq, const u16* __restrict__ wk,
                                              const u16* __restrict__ wv, const u16* __restrict__ wo, char* __restrict__ ws) {
  extern __shared__ __attribute__((aligned(16))) char smem[];
  const lds_ptr lds = (lds_ptr)smem;
  const unsigned lds0 = (unsigned)(uintptr_t)smem;
  const int tid = threadIdx.x, lane = tid & 63;
  const int w = (int)rfl(tid >> 6);
  const int bid = blockIdx.x;
  const int tb = (bid & 7) * 4 + ((bid >> 3) & 3), fb = bid >> 5;
  const bool kvwg = fb < 2;
  STAMP_DECL STAMP(0);
  if (w >= 4) {
    const int lw = w - 4;
    const u16* xs = x + (size_t)(tb * 64 + lw * 16 + (lane >> 4)) * DM;
    const int xc = (lane & 15) ^ (lane >> 4);
    const int rr = lane >> 3, pos = lane & 7;
    const u16* wsrc = wq + (size_t)(lw * 32 + rr) * DM + fb * 64 + ((pos ^ (((rr >> 1) & 1) << 2)) * 8);
    const u16* kvs = (fb ? wv : wk) + lw * 512 + lane * 8;
#define K1_ISSUE(st, KV) do { _Pragma("unroll") for (int i = 0; i < 4; ++i) { \
      glds16(xs + (size_t)i * 4 * DM + (st) * 128 + ((xc ^ (4 * i)) * 8), lds0 + K1_X + (st) * 16384 + (lw * 4 + i) * 1024); \
      glds16(wsrc + (size_t)((st) * 128 + i * 8) * DM, lds0 + K1_W + (st) * 16384 + (lw * 4 + i) * 1024); } \
    if (KV) glds16(kvs + (st) * 2048, lds0 + K1_KV + (st) * 4096 + lw * 1024); } while (0)
    if (kvwg) {
      K1_ISSUE(0, 1); K1_ISSUE(1, 1);
      WAIT_VM(9); RAWBAR(); K1_ISSUE(2, 1);
      WAIT_VM(9); RAWBAR(); K1_ISSUE(3, 1);
      WAIT_VM(9); RAWBAR();
    } else {
      K1_ISSUE(0, 0); K1_ISSUE(1, 0);
      WAIT_VM(8); RAWBAR(); K1_ISSUE(2, 0);
      WAIT_VM(8); RAWBAR(); K1_ISSUE(3, 0);
      if (lw == 0 && fb >= 6) {
        const int job = bid - 192;
        const u16* src = wo + (size_t)((job >> 3) * 64 + (job & 1) * 32 + 8 * (lane >> 4)) * DM + ((job >> 1) & 3) * 128 + (lane & 15) * 8;
        u32x4 R[8];
#pragma unroll
        for (int j = 0; j < 8; ++j) R[j] = *(const u32x4*)(src + (size_t)j * DM);
        WAIT_VM(16); RAWBAR();
        WAIT_VM(8); RAWBAR();
        RAWBAR();
        char* dst = ws + WS_WO + (size_t)job * 8192 + lane * 16;
#pragma unroll
        for (int m = 0; m < 4; ++m) {
          u32x4 lo, hi;
#pragma unroll
          for (int i = 0; i < 4; ++i) { lo[i] = __builtin_amdgcn_perm(R[2 * i + 1][m], R[2 * i][m], 0x05040100u); hi[i] = __builtin_amdgcn_perm(R[2 * i + 1][m], R[2 * i][m], 0x07060302u); }
          *(u32x4*)(dst + (2 * m) * 1024) = lo;
          *(u32x4*)(dst + (2 * m + 1) * 1024) = hi;
        }
        return;
      }
      WAIT_VM(8); RAWBAR();
    }
    WAIT_VM(0); RAWBAR();
    return;
  }
  const int r32 = lane & 31, h = lane >> 5;
  const int blk = (lane >> 4) & 1, q = (lane & 15) >> 2, p = lane & 3;
  const int tt = w & 1, ft = w >> 1;
  const int tok = tt * 32 + r32;
  lds_ptr xb[8];
#pragma unroll
  for (int i = 0; i < 8; ++i) xb[i] = lds + K1_X + tok * 256 + (((2 * i + h) ^ (tok & 15)) * 16);
  const lds_ptr wb = lds + K1_W + (8 * h + q) * 128 + (((ft * 4 + blk * 2 + (p >> 1)) ^ ((q >> 1) << 2)) * 16) + 8 * (p & 1);
  const lds_ptr kb = lds + K1_KV + (8 * h + q) * 32 + 8 * p;
  const float piv = __uint_as_float((unsigned)x[(size_t)(tb * 64 + tok) * DM] << 16);
  f32x16 acc = {}, acc2 = {};
  float cs = 0.f, cs2 = 0.f, sx = 0.f, sxx = 0.f, sxb = 0.f, sxxb = 0.f;
  s16x4 a[2][4][2]; bf16x8 b[2][4];
#define K1_STEP(bu, MM, LD, s0, sc0) do { _Pragma("unroll") for (int i = 0; i < 4; ++i) { const int s = (s0) + i; \
      if (MM) { acc = MFMA32(cat8(a[bu][i][0], a[bu][i][1]), b[bu][i], acc); PINM(acc); } \
      if (LD) { a[1 - (bu)][i][0] = lds_tr(wb + s * 2048); a[1 - (bu)][i][1] = lds_tr(wb + s * 2048 + 512); b[1 - (bu)][i] = lds_b128(xb[s & 7] + (s >> 3) * 16384); } SB(); } \
    if (MM) { \
      if (ft == (bu)) { \
        if (kvwg) { bf16x8 ka[4]; \
          _Pragma("unroll") for (int i = 0; i < 4; ++i) ka[i] = cat8(lds_tr(kb + ((sc0) + i) * 512), lds_tr(kb + ((sc0) + i) * 512 + 128)); \
          if (fb == 0) { _Pragma("unroll") for (int i = 0; i < 4; ++i) acc2 = MFMA32(ka[i], b[bu][i], acc2); } \
          else { _Pragma("unroll") for (int i = 0; i < 4; ++i) acc2 = MFMA32(b[bu][i], ka[i], acc2); } \
          PINM(acc2); SB(); \
          _Pragma("unroll") for (int i = 0; i < 4; ++i) { const u32x4 u = __builtin_bit_cast(u32x4, ka[i]); \
            cs2 = dot2(u[0], ONES, cs2); cs2 = dot2(u[1], ONES, cs2); cs2 = dot2(u[2], ONES, cs2); cs2 = dot2(u[3], ONES, cs2); } PIN(cs2); } \
        _Pragma("unroll") for (int i = 0; i < 4; ++i) { const u32x4 u = __builtin_bit_cast(u32x4, b[bu][i]); \
          sx = dot2(u[0], ONES, sx); sxx = dot2(u[0], u[0], sxx); sxb = dot2(u[1], ONES, sxb); sxxb = dot2(u[1], u[1], sxxb); \
          sx = dot2(u[2], ONES, sx); sxx = dot2(u[2], u[2], sxx); sxb = dot2(u[3], ONES, sxb); sxxb = dot2(u[3], u[3], sxxb); } \
        PIN(sx); PIN(sxx); PIN(sxb); PIN(sxxb); } \
      if (tt == (bu)) { _Pragma("unroll") for (int i = 0; i < 4; ++i) { const u32x4 u = __builtin_bit_cast(u32x4, cat8(a[bu][i][0], a[bu][i][1])); \
          cs = dot2(u[0], ONES, cs); cs = dot2(u[1], ONES, cs); cs = dot2(u[2], ONES, cs); cs = dot2(u[3], ONES, cs); } PIN(cs); } \
      SB(); } } while (0)
  STAMP(1);
#pragma unroll
  for (int st = 0; st < 4; ++st) { RAWBAR(); K1_STEP(1, st > 0, true, st * 8, st * 8 - 4); K1_STEP(0, true, true, st * 8 + 4, st * 8); }
  K1_STEP(1, true, false, 0, 28);
  STAMP(2);
  const lds_ptr sc = lds + K1_SC;
  {
    sx += sxb; sxx += sxxb;
    f32x2 d; d[0] = swap32_sum(sx - 128.f * piv); d[1] = swap32_sum(__builtin_fmaf(-2.f * piv, sx, sxx) + 128.f * piv * piv);
    cs = swap32_sum(cs); cs2 = swap32_sum(cs2);
    if (h == 0) { LDS_F32(sc + (ft * 2 + tt) * 128 + r32 * 4) = cs; LDS_F32X2(sc + 512 + (tt * 2 + ft) * 256 + r32 * 8) = d; LDS_F32(sc + 1536 + w * 128 + r32 * 4) = cs2; }
    if (kvwg && ft == 1) {
#pragma unroll
      for (int g = 0; g < 4; ++g) { f32x4 t; t[0] = acc2[4 * g]; t[1] = acc2[4 * g + 1]; t[2] = acc2[4 * g + 2]; t[3] = acc2[4 * g + 3]; LDS_F32X4(sc + 2048 + tt * 4096 + g * 1024 + lane * 16) = t; }
    }
  }
  BAR();
  float mean, rstd;
  {
    const f32x2 d = LDS_F32X2(sc + 512 + tt * 512 + r32 * 8) + LDS_F32X2(sc + 512 + tt * 512 + 256 + r32 * 8);
    const float dmn = d[0] * (1.f / DM);
    mean = piv + dmn; rstd = __builtin_amdgcn_rsqf(fmaxf(d[1] * (1.f / DM) - dmn * dmn, 0.f) + LN_EPS);
  }
  {
    const float scl = rstd * QSCALE;
    float o[16];
#pragma unroll
    for (int g = 0; g < 4; ++g) {
      const f32x4 c4 = LDS_F32X4(sc + ft * 256 + (8 * g + 4 * h) * 4) + LDS_F32X4(sc + ft * 256 + 128 + (8 * g + 4 * h) * 4);
#pragma unroll
      for (int j = 0; j < 4; ++j) o[4 * g + j] = (acc[4 * g + j] - mean * c4[j]) * scl;
    }
    u32x4 p0, p1;
#pragma unroll
    for (int i = 0; i < 4; ++i) { p0[i] = pk2(o[2 * i], o[2 * i + 1]); p1[i] = pk2(o[8 + 2 * i], o[9 + 2 * i]); }
    char* dst = ws + WS_Q + (size_t)(tb * 64 + tok) * 1024 + (fb * 4 + ft * 2) * 32 + h * 16;
    *(u32x4*)dst = p0;
    *(u32x4*)(dst + 32) = p1;
  }
  if (kvwg && ft == 0) {
    const int gt = tb * 2 + tt;
#pragma unroll
    for (int g = 0; g < 4; ++g) { const f32x4 t = LDS_F32X4(sc + 2048 + tt * 4096 + g * 1024 + lane * 16);
#pragma unroll
      for (int j = 0; j < 4; ++j) acc2[4 * g + j] += t[j]; }
    if (fb == 0) {
      float o[8];
#pragma unroll
      for (int g = 0; g < 2; ++g) {
        const f32x4 c4 = LDS_F32X4(sc + 1536 + tt * 128 + (8 * g + 4 * h) * 4) + LDS_F32X4(sc + 1536 + (tt + 2) * 128 + (8 * g + 4 * h) * 4);
#pragma unroll
        for (int j = 0; j < 4; ++j) o[4 * g + j] = (acc2[4 * g + j] - mean * c4[j]) * rstd;
      }
      u32x4 p0;
#pragma unroll
      for (int i = 0; i < 4; ++i) p0[i] = pk2(o[2 * i], o[2 * i + 1]);
      *(u32x4*)(ws + WS_K + (size_t)gt * 1024 + h * 512 + r32 * 16) = p0;
      float n2 = 0.f;
#pragma unroll
      for (int i = 0; i < 4; ++i) n2 = dot2(p0[i], p0[i], n2);
      n2 = wave_max(swap32_sum(n2));
      if (lane == 0) *(float*)(ws + WS_KMAX + gt * 4) = n2;
    } else {
      const float cv = LDS_F32(sc + 1536 + tt * 128 + r32 * 4) + LDS_F32(sc + 1536 + (tt + 2) * 128 + r32 * 4);
      const lds_ptr sp = sc + 2048 + tt * 4096;
      LGKM0();
      if (h == 0) { f32x2 st; st[0] = mean; st[1] = rstd; LDS_F32X2(sp + r32 * 8) = st; }
      float o[16];
#pragma unroll
      for (int g = 0; g < 4; ++g) {
        const f32x4 s01 = LDS_F32X4(sp + (8 * g + 4 * h) * 8), s23 = LDS_F32X4(sp + (8 * g + 4 * h) * 8 + 16);
        o[4 * g + 0] = (acc2[4 * g + 0] - s01[0] * cv) * s01[1];
        o[4 * g + 1] = (acc2[4 * g + 1] - s01[2] * cv) * s01[3];
        o[4 * g + 2] = (acc2[4 * g + 2] - s23[0] * cv) * s23[1];
        o[4 * g + 3] = (acc2[4 * g + 3] - s23[2] * cv) * s23[3];
      }
      u32x4 p0, p1;
#pragma unroll
      for (int i = 0; i < 4; ++i) { p0[i] = pk2(o[2 * i], o[2 * i + 1]); p1[i] = pk2(o[8 + 2 * i], o[9 + 2 * i]); }
      if (r32 < 16) {
        char* dst = ws + WS_V + (size_t)gt * 1024 + h * 256 + r32 * 16;
        *(u32x4*)dst = p0;
        *(u32x4*)(dst + 512) = p1;
      }
    }
  }
  STAMP(3); STAMP_FLUSH(0);
}

constexpr int K2_PSZ = 2176;
constexpr int K2_RED = 0;
constexpr int K2_Q = 64 * K2_PSZ;
constexpr int K2_NM = K2_Q + 8192;
constexpr int K2_JUNK = K2_NM + 8192;
constexpr int K2_FLG = K2_JUNK + 256;
constexpr int K2_OB = K2_FLG + 512;
constexpr int K2_LDS = K2_OB + 2 * 1152;
constexpr int K2_MX = 0;

#define E_(r) "v_exp_f32 v" #r ", v" #r "\n\t"
#define C_(d, a, b) "v_cvt_pk_bf16_f32 v" #d ", v" #a ", v" #b "\n\t"
#define PVPREV_ "v_mfma_f32_32x32x16_bf16 v[200:215], %8, v[236:239], v[200:215]\n\t"
#define TILE_A1 E_(168) E_(169)
#define TILE_A2 E_(170) E_(171) E_(172) E_(173) "v_mfma_f32_32x32x16_bf16 v[184:199], %6, %7, v[216:231]\n\t" E_(174) E_(175) \
    C_(232, 168, 169) C_(233, 170, 171) C_(234, 172, 173) C_(235, 174, 175) E_(176) E_(177)
#define TILE_A3 E_(178) E_(179) E_(180) E_(181) E_(182) E_(183) C_(236, 176, 177) C_(237, 178, 179) C_(238, 180, 181) C_(239, 182, 183)
#define TILE_B1 E_(184) E_(185)
#define TILE_B2 E_(186) E_(187) E_(188) E_(189) "v_mfma_f32_32x32x16_bf16 v[168:183], %6, %7, v[216:231]\n\t" E_(190) E_(191) \
    C_(232, 184, 185) C_(233, 186, 187) C_(234, 188, 189) C_(235, 190, 191) E_(192) E_(193)
#define TILE_B3 E_(194) E_(195) E_(196) E_(197) E_(198) E_(199) C_(236, 192, 193) C_(237, 194, 195) C_(238, 196, 197) C_(239, 198, 199)
#define PV0_ "v_mfma_f32_32x32x16_bf16 v[200:215], %9, v[232:235], v[200:215]\n\t"
#define PV0Z_ "v_mfma_f32_32x32x16_bf16 v[200:215], %9, v[232:235], 0\n\t"
#define K2_ASM(TXT, KN, QN, VP, V0) asm volatile(TXT : "+{v[168:183]}"(sa), "+{v[184:199]}"(sb), "+{v[200:215]}"(o), "+{v[232:235]}"(p0), "+{v[236:239]}"(p1) \
    : "{v[216:231]}"(negm), "v"(KN), "v"(QN), "v"(VP), "v"(V0))
#define K2_ASM_NOC(TXT, V0) asm volatile(TXT : "+{v[168:183]}"(sa), "+{v[184:199]}"(sb), "+{v[200:215]}"(o), "+{v[232:235]}"(p0), "+{v[236:239]}"(p1) \
    : "v"(V0), "v"(V0), "v"(V0), "v"(V0), "v"(V0))

__global__ __launch_bounds__(512) void k2_attn(const u16* __restrict__ x, const u16* __restrict__ wg, const u16* __restrict__ wu,
                                               const u16* __restrict__ wd, char* __restrict__ ws) {
  extern __shared__ __attribute__((aligned(16))) char smem[];
  const lds_ptr lds = (lds_ptr)smem;
  const unsigned lds0 = (unsigned)(uintptr_t)smem;
  const int tid = threadIdx.x, lane = tid & 63;
  const int w = (int)rfl(tid >> 6);
  const int g8 = (blockIdx.x & 7) * 32 + (blockIdx.x >> 3);
  const int r32 = lane & 31, h = lane >> 5;
  STAMP_DECL STAMP(0);
  const u32x4 qown = *(const u32x4*)(ws + WS_Q + (size_t)(g8 * 8 + (r32 & 7)) * 1024 + (w * 4 + (r32 >> 3)) * 32 + h * 16);
  const float km2l = *(const float*)(ws + WS_KMAX + lane * 4);
  bf16x8 kf[8], vf[8][2];
  u32x4 vr[8];
  const char* ksrc = ws + WS_K + (size_t)w * 8192 + lane * 16;
  const char* vsrc = ws + WS_V + (size_t)w * 8192 + (r32 >> 4) * 512 + h * 256 + (lane & 15) * 16;
#define K2_LOADKV(j) do { kf[j] = *(const bf16x8*)(ksrc + (j) * 1024); vr[j] = *(const u32x4*)(vsrc + (j) * 1024); } while (0)
#define K2_SPLITV(j) do { u32x4 a_, b_; _Pragma("unroll") for (int i_ = 0; i_ < 4; ++i_) { \
    const auto r_ = __builtin_amdgcn_permlane16_swap(vr[j][i_], ONES, false, false); a_[i_] = r_[0]; b_[i_] = r_[1]; } \
    vf[j][0] = __builtin_bit_cast(bf16x8, a_); vf[j][1] = __builtin_bit_cast(bf16x8, b_); } while (0)
  K2_LOADKV(0); K2_LOADKV(1);
  if (w < 4) __builtin_amdgcn_s_setprio(2); else __builtin_amdgcn_s_setprio(1);
  if (tid >= 8 && tid < 128) LDS_VU32(lds + K2_FLG + tid * 4) = 0u;
  {
    float n2 = 0.f;
#pragma unroll
    for (int i = 0; i < 4; ++i) n2 = dot2(qown[i], qown[i], n2);
    const float mref = sqrtf(swap32_sum(n2) * wave_max(km2l));
    const f32x4 nm = {-mref, -mref, -mref, -mref};
    LDS_U32X4(lds + K2_Q + w * 1024 + lane * 16) = qown;
    LDS_F32X4(lds + K2_NM + w * 1024 + lane * 16) = nm;
    LDS_VU32(lds + K2_FLG + w * 4) = __any(!(mref <= 40.f)) ? 1u : 0u;
  }
  STAMP(1);
  BAR();
  STAMP(2);
  K2_LOADKV(2); K2_LOADKV(3); K2_LOADKV(4); K2_LOADKV(5); K2_LOADKV(6); K2_LOADKV(7);
  const lds_ptr qp = lds + K2_Q + lane * 16;
  if (__any(LDS_VU32(lds + K2_FLG + (lane & 7) * 4) != 0u)) {
    const f32x16 zero = {};
    for (int g = 0; g < 8; ++g) {
      const bf16x8 qf = lds_b128(qp + g * 1024);
      float mx = -3.0e38f;
#pragma unroll
      for (int j = 0; j < 8; ++j) {
        const f32x16 s = MFMA32(kf[j], qf, zero);
#pragma unroll
        for (int i = 0; i < 16; ++i) mx = fmaxf(mx, s[i]);
      }
      mx = swap32_max(mx);
      if (h == 0) LDS_F32(lds + K2_MX + (g * 8 + w) * 128 + r32 * 4) = mx;
    }
    BAR();
    float mx = LDS_F32(lds + K2_MX + w * 1024 + r32 * 4);
#pragma unroll
    for (int i = 1; i < 8; ++i) mx = fmaxf(mx, LDS_F32(lds + K2_MX + w * 1024 + i * 128 + r32 * 4));
    const f32x4 nm = {-mx, -mx, -mx, -mx};
    LDS_F32X4(lds + K2_NM + w * 1024 + lane * 16) = nm;
    BAR();
  }
  f32x16 negm;
#define K2_LOAD_NEGM(g) do { const lds_ptr np_ = lds + K2_NM + (g) * 1024 + lane * 16; \
    _Pragma("unroll") for (int i_ = 0; i_ < 4; ++i_) { const f32x4 t_ = *(volatile __attribute__((address_space(3))) f32x4*)(np_); \
      negm[4 * i_] = t_[0]; negm[4 * i_ + 1] = t_[1]; negm[4 * i_ + 2] = t_[2]; negm[4 * i_ + 3] = t_[3]; } } while (0)
  K2_LOAD_NEGM(0);
  f32x16 o = {}, sa, sb = {};
  u32x4 p0 = {}, p1 = {};
  bf16x8 qf = lds_b128(qp);
  sa = MFMA32(kf[0], qf, negm);
  const lds_ptr redw = lds + K2_RED + w * K2_PSZ + lane * 16;
#define K2_WRITE_PARTIAL(g) do { const lds_ptr rp_ = redw + (g) * (8 * K2_PSZ); f32x4 t0_, t1_; \
    t0_[0] = o[0]; t0_[1] = o[1]; t0_[2] = o[2]; t0_[3] = o[3]; t1_[0] = o[4]; t1_[1] = o[5]; t1_[2] = o[6]; t1_[3] = o[7]; \
    LDS_F32X4(rp_) = t0_; LDS_F32X4(rp_ + 1024) = t1_; LDS_F32(rp_ + 2048 - lane * 16 + r32 * 4) = o[8]; \
    asm volatile("" ::: "memory"); LDS_VU32(lds + K2_FLG + 32 + ((g) * 8 + w) * 4) = 1u; } while (0)
#define VMWAIT0() __builtin_amdgcn_s_waitcnt(0x0F70)
#define SPL(j) do { if (FIRST_) { K2_SPLITV(j); asm volatile("s_nop 1" : "+v"(vf[j][0]), "+v"(vf[j][1])); } } while (0)
#define K2_ITER(g, FIRST) do { constexpr bool FIRST_ = FIRST; \
    SPL(0); \
    if (FIRST) K2_ASM(TILE_A1 TILE_A2 PV0Z_ TILE_A3, kf[1], qf, vf[0][0], vf[0][0]); \
    else { K2_ASM(TILE_A1 PVPREV_ TILE_A2, kf[1], qf, vf[7][1], vf[0][0]); K2_WRITE_PARTIAL((g) - 1); K2_ASM(PV0Z_ TILE_A3, kf[1], qf, vf[7][1], vf[0][0]); } \
    const bf16x8 qn = lds_b128(qp + (((g) + 1) & 7) * 1024); \
    SPL(1); K2_ASM(TILE_B1 PVPREV_ TILE_B2 PV0_ TILE_B3, kf[2], qf, vf[0][1], vf[1][0]); \
    SPL(2); K2_ASM(TILE_A1 PVPREV_ TILE_A2 PV0_ TILE_A3, kf[3], qf, vf[1][1], vf[2][0]); \
    SPL(3); K2_ASM(TILE_B1 PVPREV_ TILE_B2 PV0_ TILE_B3, kf[4], qf, vf[2][1], vf[3][0]); \
    SPL(4); K2_ASM(TILE_A1 PVPREV_ TILE_A2 PV0_ TILE_A3, kf[5], qf, vf[3][1], vf[4][0]); \
    SPL(5); K2_ASM(TILE_B1 PVPREV_ TILE_B2 PV0_ TILE_B3, kf[6], qf, vf[4][1], vf[5][0]); \
    SPL(6); K2_ASM(TILE_A1 PVPREV_ TILE_A2, kf[7], qf, vf[5][1], vf[6][0]); \
    K2_LOAD_NEGM(((g) + 1) & 7); \
    K2_ASM_NOC(PV0_ TILE_A3, vf[6][0]); \
    SPL(7); K2_ASM(TILE_B1 PVPREV_ TILE_B2 PV0_ TILE_B3, kf[0], qn, vf[6][1], vf[7][0]); \
    qf = qn; } while (0)

  STAMP(3);
  asm volatile("s_nop 15" : "+{v[168:183]}"(sa));
  K2_ITER(0, true);
  VMWAIT0();
  for (int g = 1; g < 8; ++g) {
    if (g <= 4) {
      const int wi = (blockIdx.x >> 3) * 8 + w;
      const char* src = g == 2 ? (const char*)ws + WS_WO + (wi * 16 + (lane & 15)) * 128 : (const char*)(g == 1 ? wd : g == 3 ? wg : wu) + (wi * 43 + (lane < 43 ? lane : 42)) * 128;
      glds4(src, lds0 + K2_JUNK);
    }
    K2_ITER(g, false);
  }
  STAMP(4);
  K2_ASM("s_nop 1\n\t" PVPREV_ "s_nop 15", kf[0], qf, vf[7][1], vf[7][0]);
  K2_WRITE_PARTIAL(7);
  if (w >= 4) { STAMP(5); STAMP_FLUSH(1); return; }
  asm volatile("s_waitcnt vmcnt(0)" ::: "memory");
  VMWAIT0();
  const int g4 = lane >> 4, tok8 = lane & 7;
  const char* wsrc = ws + WS_WO + (size_t)w * 16384 + lane * 16;
  bf16x8 wr[5][8];
#define K2_WLOAD(hh) do { if ((hh) < 16) { _Pragma("unroll") for (int i_ = 0; i_ < 8; ++i_) wr[(hh) % 5][i_] = *(const bf16x8*)(wsrc + (size_t)((hh) >> 1) * 65536 + ((hh) & 1) * 8192 + i_ * 1024); } } while (0)
  f32x4 oacc[8];
  const float piv = __uint_as_float((unsigned)x[(size_t)(g8 * 8 + tok8) * DM + w * 128] << 16);
  {
    u32x4 xr[4];
#pragma unroll
    for (int r = 0; r < 4; ++r) xr[r] = *(const u32x4*)(x + (size_t)(g8 * 8 + tok8) * DM + w * 128 + g4 * 32 + r * 8);
    K2_WLOAD(0); K2_WLOAD(1); K2_WLOAD(2);
#pragma unroll
    for (int r = 0; r < 4; ++r)
#pragma unroll
      for (int i = 0; i < 4; ++i) { oacc[2 * i][r] = bflo(xr[r][i]); oacc[2 * i + 1][r] = bfhi(xr[r][i]); }
  }
#define K2_PROJ(g) do { \
    while (!__all(LDS_VU32(lds + K2_FLG + 32 + ((g) * 8 + tok8) * 4) != 0u)) __builtin_amdgcn_s_sleep(2); \
    asm volatile("" ::: "memory"); \
    { const lds_ptr rp_ = lds + K2_RED + (g) * (8 * K2_PSZ) + (w >> 1) * 1024 + lane * 16 + (w & 1) * 8, lp_ = lds + K2_RED + (g) * (8 * K2_PSZ) + 2048 + r32 * 4; \
      f32x2 t_[8]; float l_[8]; \
      _Pragma("unroll") for (int i_ = 0; i_ < 8; ++i_) { t_[i_] = LDS_F32X2(rp_ + i_ * K2_PSZ); l_[i_] = LDS_F32(lp_ + i_ * K2_PSZ); } \
      SB(); K2_WLOAD(2 * (g) + 3); SB(); \
      const f32x2 ts_ = ((t_[0] + t_[1]) + (t_[2] + t_[3])) + ((t_[4] + t_[5]) + (t_[6] + t_[7])); \
      const float inv_ = __builtin_amdgcn_rcpf(((l_[0] + l_[1]) + (l_[2] + l_[3])) + ((l_[4] + l_[5]) + (l_[6] + l_[7]))); \
      LDS_VU32(lds + K2_OB + ((g) & 1) * 1152 + (r32 & 7) * 144 + (r32 >> 3) * 32 + h * 8 + (w >> 1) * 16 + (w & 1) * 4) = pk2(ts_[0] * inv_, ts_[1] * inv_); \
      LDS_VU32(lds + K2_FLG + 288 + ((g) * 4 + w) * 4) = 1u; } \
    SB(); K2_WLOAD(2 * (g) + 4); SB(); \
    while (!__all(LDS_VU32(lds + K2_FLG + 288 + ((g) * 4 + (lane & 3)) * 4) != 0u)) __builtin_amdgcn_s_sleep(1); \
    asm volatile("" ::: "memory"); \
    const bf16x8 b0_ = lds_b128(lds + K2_OB + ((g) & 1) * 1152 + tok8 * 144 + g4 * 16), b1_ = lds_b128(lds + K2_OB + ((g) & 1) * 1152 + tok8 * 144 + 64 + g4 * 16); \
    _Pragma("unroll") for (int f_ = 0; f_ < 8; ++f_) oacc[f_] = MFMA16(wr[(2 * (g)) % 5][f_], b0_, oacc[f_]); \
    _Pragma("unroll") for (int f_ = 0; f_ < 8; ++f_) { oacc[f_] = MFMA16(wr[(2 * (g) + 1) % 5][f_], b1_, oacc[f_]); PINM(oacc[f_]); } } while (0)
  STAMP(5);
  K2_PROJ(0); K2_PROJ(1); K2_PROJ(2); K2_PROJ(3); K2_PROJ(4); K2_PROJ(5); K2_PROJ(6); K2_PROJ(7);
  STAMP(6);
  {
    const int tok = g8 * 8 + tok8;
    u32x4 hb[4];
#pragma unroll
    for (int r = 0; r < 4; ++r)
#pragma unroll
      for (int i = 0; i < 4; ++i) hb[r][i] = pk2(oacc[2 * i][r], oacc[2 * i + 1][r]);
    if ((lane & 8) == 0) {
#pragma unroll
      for (int r = 0; r < 4; ++r) *(u32x4*)(ws + WS_H + (size_t)(tok >> 5) * 32768 + (w * 16 + g4 * 4 + r) * 512 + (tok & 31) * 16) = hb[r];
    }
    float sx = 0.f, sxx = 0.f;
#pragma unroll
    for (int r = 0; r < 4; ++r)
#pragma unroll
      for (int i = 0; i < 4; ++i) { sx = dot2(hb[r][i], ONES, sx); sxx = dot2(hb[r][i], hb[r][i], sxx); }
    const float d1 = swap32_sum(swap16_sum(sx - 32.f * piv)), d2 = swap32_sum(swap16_sum(__builtin_fmaf(-2.f * piv, sx, sxx) + 32.f * piv * piv));
    f32x2 st; st[0] = piv + d1 * (1.f / 128); st[1] = fmaxf(d2 - d1 * d1 * (1.f / 128), 0.f);
    if (lane < 8) *(f32x2*)(ws + WS_ST + ((size_t)((tok >> 5) * 4 + w) * 32 + (tok & 31)) * 8) = st;
  }
  STAMP(7); STAMP_FLUSH(1);
}

constexpr int K4_SLOT = 32768, K4_LDS = 5 * K4_SLOT;

template <int MODE>
__global__ __launch_bounds__(512) void k64(const u16* __restrict__ W, const u16* __restrict__ x, char* __restrict__ ws, u16* __restrict__ out) {
  constexpr int NKS = MODE ? NAT : 32;
  extern __shared__ __attribute__((aligned(16))) char smem[];
  const lds_ptr lds = (lds_ptr)smem;
  const unsigned lds0 = (unsigned)(uintptr_t)smem;
  const int tid = threadIdx.x, lane = tid & 63;
  const int w = (int)rfl(tid >> 6);
  const int bid = blockIdx.x;
  const int tb = (bid & 7) * 4 + ((bid >> 3) & 3), nb = bid >> 5;
  const int r32 = lane & 31, h = lane >> 5;
  STAMP_DECL STAMP(0);
  if (w >= 4) {
    const int lw = w - 4;
    const int rr = lane >> 3, pos = lane & 7;
    const char* wsrc = (const char*)W + (size_t)(lw * 32 + rr) * 1024 + nb * 128 + (pos ^ (((rr >> 1) & 1) << 2)) * 16;
    const char* asrc = ws + WS_ACT + ((size_t)(tb * 2 + (lw >> 1)) * NKS + (lw & 1) * 4) * 1024 + lane * 16;
#define K4_ISSUE(st) do { const unsigned sl_ = lds0 + ((st) % 5) * K4_SLOT; _Pragma("unroll") for (int i = 0; i < 4; ++i) { \
    const bool oka_ = (st) < 10 || !((lw & 1) && i >= 2), okw_ = (st) < 10 || lw < 3; \
    glds16(asrc + (oka_ ? (st) * 8192 + i * 1024 : 0), sl_ + 16384 + (lw * 4 + i) * 1024); \
    glds16(wsrc + (okw_ ? (size_t)(st) * 131072 + i * 8192 : (size_t)0), sl_ + (lw * 4 + i) * 1024); } } while (0)
    if (MODE == 0) {
      K4_ISSUE(0); K4_ISSUE(1);
      WAIT_VM(8); RAWBAR(); K4_ISSUE(2);
      WAIT_VM(8); RAWBAR(); K4_ISSUE(3);
      WAIT_VM(8); RAWBAR();
      WAIT_VM(0); RAWBAR();
    } else {
      K4_ISSUE(0); K4_ISSUE(1); K4_ISSUE(2);
      WAIT_VM(16); RAWBAR(); K4_ISSUE(3); K4_ISSUE(4);
      WAIT_VM(24); RAWBAR(); K4_ISSUE(5);
      WAIT_VM(24); RAWBAR(); K4_ISSUE(6);
      WAIT_VM(24); RAWBAR(); K4_ISSUE(7);
      WAIT_VM(24); RAWBAR(); K4_ISSUE(8);
      WAIT_VM(24); RAWBAR(); K4_ISSUE(9);
      WAIT_VM(24); RAWBAR(); K4_ISSUE(10);
      WAIT_VM(24); RAWBAR();
      WAIT_VM(16); RAWBAR();
      WAIT_VM(8); RAWBAR();
      WAIT_VM(0); RAWBAR();
    }
    STAMP(7); STAMP_FLUSH(MODE ? 4 : 2);
    return;
  }
  const int nt = w & 1, tt = w >> 1;
  const int tok = tb * 64 + tt * 32 + r32;
  const int ncol = nb * 64 + nt * 32 + h * 16;
  const int blk = (lane >> 4) & 1, q = (lane & 15) >> 2, p = lane & 3;
  char* hfrag = ws + WS_H + (size_t)(tok >> 5) * 32768 + (ncol >> 3) * 512 + r32 * 16;
  u32x4 h0, h1;
  if (MODE) { h0 = *(const u32x4*)(hfrag); h1 = *(const u32x4*)(hfrag + 512); }
  else { h0 = *(const u32x4*)(x + (size_t)tok * DM + ncol); h1 = *(const u32x4*)(x + (size_t)tok * DM + ncol + 8); }
  f32x16 acc = {};
  const int cbyte = nt * 64 + (16 * (p & 1) + 8 * blk + 4 * (p >> 1)) * 2;
  lds_ptr ap[3], bp[3];
#pragma unroll
  for (int u = 0; u < 3; ++u) { ap[u] = lds + u * 2 * K4_SLOT + (4 * h + q) * 128 + (cbyte ^ ((q >> 1) << 6)); bp[u] = lds + u * 2 * K4_SLOT + 16384 + tt * 8192 + lane * 16; }
  s16x4 fa[2][4][2]; bf16x8 fb_[2][4];
#define K4_U(st) (((st) % 5) >> 1)
#define K4_O(st) ((((st) % 5) & 1) * K4_SLOT)
#define K4_LD1(bu, st, k0, k) do { fa[bu][k][0] = lds_tr(ap[K4_U(st)] + K4_O(st) + ((k0) + (k)) * 2048); fa[bu][k][1] = lds_tr(ap[K4_U(st)] + K4_O(st) + ((k0) + (k)) * 2048 + 1024); \
    fb_[bu][k] = lds_b128(bp[K4_U(st)] + K4_O(st) + ((k0) + (k)) * 1024); } while (0)
#define K4_STEP(bu, NM, NL, st, k0) do { _Pragma("unroll") for (int k = 0; k < 4; ++k) { \
    if (k < (NM)) { acc = MFMA32(cat8(fa[bu][k][0], fa[bu][k][1]), fb_[bu][k], acc); PINM(acc); } \
    if (k < (NL)) K4_LD1(1 - (bu), st, k0, k); SB(); } } while (0)
#define K4_STAGE(st, NK2) do { BAR(); K4_STEP(1, (st) > 0 ? 4 : 0, 4, st, 0); K4_STEP(0, 4, NK2, st, 4); } while (0)
  STAMP(1);
  K4_STAGE(0, 4); STAMP(2); K4_STAGE(1, 4); STAMP(3); K4_STAGE(2, 4); K4_STAGE(3, 4);
  if (MODE) {
    K4_STAGE(4, 4); STAMP(4); K4_STAGE(5, 4); K4_STAGE(6, 4); K4_STAGE(7, 4); K4_STAGE(8, 4); K4_STAGE(9, 4); STAMP(5); K4_STAGE(10, 2);
    K4_STEP(1, 2, 0, 0, 0);
  } else {
    K4_STEP(1, 4, 0, 0, 0);
  }
  STAMP(6);
  u32x4 o0, o1;
#pragma unroll
  for (int i = 0; i < 4; ++i) {
    o0[i] = pk2(acc[2 * i] + bflo(h0[i]), acc[2 * i + 1] + bfhi(h0[i]));
    o1[i] = pk2(acc[8 + 2 * i] + bflo(h1[i]), acc[9 + 2 * i] + bfhi(h1[i]));
  }
  if (MODE) {
    u16* dst = out + (size_t)tok * DM + ncol;
    *(u32x4*)dst = o0;
    *(u32x4*)(dst + 8) = o1;
  } else {
    *(u32x4*)hfrag = o0;
    *(u32x4*)(hfrag + 512) = o1;
    float v[16], s = 0.f;
#pragma unroll
    for (int i = 0; i < 4; ++i) { v[2 * i] = bflo(o0[i]); v[2 * i + 1] = bfhi(o0[i]); v[8 + 2 * i] = bflo(o1[i]); v[9 + 2 * i] = bfhi(o1[i]); }
#pragma unroll
    for (int i = 0; i < 16; ++i) s += v[i];
    const float m32 = swap32_sum(s) * (1.f / 32);
    float ss = 0.f;
#pragma unroll
    for (int i = 0; i < 16; ++i) ss += (v[i] - m32) * (v[i] - m32);
    f32x2 st; st[0] = m32; st[1] = swap32_sum(ss);
    if (h == 0) *(f32x2*)(ws + WS_ST + ((size_t)((tok >> 5) * 16 + nb * 2 + nt) * 32 + r32) * 8) = st;
  }
  STAMP(7); STAMP_FLUSH(MODE ? 4 : 2);
}

constexpr int K3_SLOT = 40960, K3_LDS = 4 * K3_SLOT;

__global__ __launch_bounds__(512) void k3_gateup(const u16* __restrict__ wg, const u16* __restrict__ wu, const u16* __restrict__ wd,
                                                 char* __restrict__ ws) {
  extern __shared__ __attribute__((aligned(16))) char smem[];
  const lds_ptr lds = (lds_ptr)smem;
  const unsigned lds0 = (unsigned)(uintptr_t)smem;
  const int tid = threadIdx.x, lane = tid & 63;
  const int w = (int)rfl(tid >> 6);
  const int bid = blockIdx.x, jx = bid >> 3;
  if (jx >= 30) {
#define K3DELAY 0
    __builtin_amdgcn_s_sleep(K3DELAY);
    const char* src = (const char*)wd + (size_t)(((jx & 1) * 8 + w) * 688 + lane) * 128;
#pragma unroll
    for (int i = 0; i < 11; ++i) glds4(src + ((i < 10 || lane < 48) ? i * 8192 : 0), lds0 + w * 256);
    return;
  }
  const int rb = (bid & 7) * 2 + (jx & 1), cb = jx >> 1;
  const int at0 = cb * 6 > 80 ? 80 : cb * 6;
  STAMP_DECL STAMP(0);
  if (w >= 4) {
    const int lw = w - 4;
    const char* bsrc = ws + WS_H + (size_t)(rb * 4 + lw) * 32768 + lane * 16;
    const char* wsrc[6];
#pragma unroll
    for (int i = 0; i < 6; ++i) {
      const int id = ((lw & 1) * 6 + i) * 64 + lane; const int row = id / 12, ch = id - row * 12;
      wsrc[i] = (const char*)((lw >> 1) ? wu : wg) + (size_t)row * (FF * 2) + at0 * 32 + (ch ^ (lw & 2)) * 16;
    }
#define K3_ISSUE(st) do { const unsigned sl_ = lds0 + ((st) & 3) * K3_SLOT; \
    _Pragma("unroll") for (int i = 0; i < 4; ++i) glds16(bsrc + (st) * 4096 + i * 1024, sl_ + 24576 + (lw * 4 + i) * 1024); \
    _Pragma("unroll") for (int i = 0; i < 6; ++i) glds16(wsrc[i] + (size_t)(st) * (64 * FF * 2), sl_ + (lw * 6 + i) * 1024); } while (0)
    K3_ISSUE(0); K3_ISSUE(1);
    WAIT_VM(10); RAWBAR(); K3_ISSUE(2);
    WAIT_VM(10); RAWBAR(); K3_ISSUE(3); K3_ISSUE(4);
    WAIT_VM(20); RAWBAR(); K3_ISSUE(5);
    WAIT_VM(20); RAWBAR(); K3_ISSUE(6);
    WAIT_VM(20); RAWBAR(); K3_ISSUE(7);
    WAIT_VM(20); RAWBAR();
    WAIT_VM(10); RAWBAR();
    WAIT_VM(0); RAWBAR();
    RAWBAR();
    return;
  }
  const int tp = w & 1, ag = w >> 1;
  const int r32 = lane & 31, h = lane >> 5, blk = (lane >> 4) & 1, q = (lane & 15) >> 2, p = lane & 3;
  f32x2 lp[2][2];
#pragma unroll
  for (int t = 0; t < 2; ++t)
#pragma unroll
    for (int i = 0; i < 2; ++i) lp[t][i] = *(const f32x2*)(ws + WS_ST + ((size_t)((rb * 4 + tp * 2 + t) * 4 + h * 2 + i) * 32 + r32) * 8);
  f32x16 acc[2][3] = {};
  float cs[3] = {0.f, 0.f, 0.f}, csh[3] = {0.f, 0.f, 0.f};
  lds_ptr ap[2][3], bp[2];
#pragma unroll
  for (int u = 0; u < 2; ++u) {
#pragma unroll
    for (int i = 0; i < 3; ++i) ap[u][i] = lds + u * 2 * K3_SLOT + blk * 12288 + (8 * h + q) * 192 + ((ag * 3 + i) ^ blk) * 32 + 8 * p;
    bp[u] = lds + u * 2 * K3_SLOT + 24576 + tp * 8192 + lane * 16;
  }
  s16x4 fa[2][3][2]; bf16x8 fb_[2][2];
#define K3_AO(st, k) (((st) & 1) * K3_SLOT + (k) * 3072)
#define K3_BO(st, k) (((st) & 1) * K3_SLOT + (k) * 1024)
#define K3_U(st) (((st) >> 1) & 1)
#define K3_LD(bu, st, k) do { fb_[bu][0] = lds_b128(bp[K3_U(st)] + K3_BO(st, k)); fb_[bu][1] = lds_b128(bp[K3_U(st)] + K3_BO(st, k) + 4096); \
    _Pragma("unroll") for (int i = 0; i < 3; ++i) { fa[bu][i][0] = lds_tr(ap[K3_U(st)][i] + K3_AO(st, k)); fa[bu][i][1] = lds_tr(ap[K3_U(st)][i] + K3_AO(st, k) + 768); } } while (0)
#define K3_CS(i, u) do { cs[i] += bflo(u); csh[i] += bfhi(u); PIN(cs[i]); PINM(csh[i]); } while (0)
#define K3_STEP(bu, LOAD, st, k) do { _Pragma("unroll") for (int i = 0; i < 3; ++i) { \
    const bf16x8 a_ = cat8(fa[bu][i][0], fa[bu][i][1]); const u32x2 au = __builtin_bit_cast(u32x2, fa[bu][i][TP]); \
    acc[0][i] = MFMA32(a_, fb_[bu][0], acc[0][i]); PINM(acc[0][i]); \
    if (LOAD) { if (i < 2) fb_[1 - (bu)][i] = lds_b128(bp[K3_U(st)] + K3_BO(st, k) + i * 4096); fa[1 - (bu)][i][0] = lds_tr(ap[K3_U(st)][i] + K3_AO(st, k)); } \
    K3_CS(i, au[0]); SB(); \
    acc[1][i] = MFMA32(a_, fb_[bu][1], acc[1][i]); PINM(acc[1][i]); \
    if (LOAD) fa[1 - (bu)][i][1] = lds_tr(ap[K3_U(st)][i] + K3_AO(st, k) + 768); \
    K3_CS(i, au[1]); SB(); } } while (0)
#define K3_STAGE(st) do { BAR(); if ((st) == 0) K3_LD(0, 0, 0); else K3_STEP(1, true, st, 0); K3_STEP(0, true, st, 1); K3_STEP(1, true, st, 2); K3_STEP(0, true, st, 3); } while (0)
#define K3_ALL() do { K3_STAGE(0); STAMP(2); K3_STAGE(1); STAMP(3); K3_STAGE(2); K3_STAGE(3); STAMP(4); K3_STAGE(4); K3_STAGE(5); K3_STAGE(6); STAMP(5); K3_STAGE(7); \
    K3_STEP(1, false, 0, 0); } while (0)
  float mean[2], rstd[2];
#pragma unroll
  for (int t = 0; t < 2; ++t) {
    mean[t] = swap32_sum(lp[t][0][0] + lp[t][1][0]) * 0.25f;
    float m2 = 0.f;
#pragma unroll
    for (int i = 0; i < 2; ++i) { const float d = lp[t][i][0] - mean[t]; m2 += lp[t][i][1] + 128.f * d * d; }
    rstd[t] = __builtin_amdgcn_rsqf(swap32_sum(m2) * (1.f / DM) + LN_EPS);
    PIN(mean[t]); PIN(rstd[t]);
  }
  STAMP(1);
  if (tp == 0) { constexpr int TP = 0; K3_ALL(); } else { constexpr int TP = 1; K3_ALL(); }
  STAMP(6);
  const lds_ptr sc = lds + ag * 1024;
#pragma unroll
  for (int i = 0; i < 3; ++i) { const float c = swap32_sum(cs[i] + csh[i]); if (h == 0) LDS_F32(sc + tp * 512 + i * 128 + r32 * 4) = c; }
  BAR();
#pragma unroll
  for (int i = 0; i < 3; ++i) {
    f32x4 c4[4];
#pragma unroll
    for (int g = 0; g < 4; ++g) c4[g] = LDS_F32X4(sc + i * 128 + (8 * g + 4 * h) * 4) + LDS_F32X4(sc + 512 + i * 128 + (8 * g + 4 * h) * 4);
#pragma unroll
    for (int t = 0; t < 2; ++t) {
      const float ke = -1.4426950408889634f * rstd[t], r2 = rstd[t] * rstd[t];
      const f32x2 nm2 = {-mean[t], -mean[t]}, ke2 = {ke, ke}, r22 = {r2, r2}, one2 = {1.f, 1.f};
      f32x2 tg[4], tu[4], e[4];
#pragma unroll
      for (int j = 0; j < 4; ++j) {
        const f32x2 ag2 = {acc[t][i][2 * j], acc[t][i][2 * j + 1]}, au2 = {acc[t][i][8 + 2 * j], acc[t][i][9 + 2 * j]};
        const f32x2 cg2 = {c4[j >> 1][2 * (j & 1)], c4[j >> 1][2 * (j & 1) + 1]}, cu2 = {c4[2 + (j >> 1)][2 * (j & 1)], c4[2 + (j >> 1)][2 * (j & 1) + 1]};
        tg[j] = __builtin_elementwise_fma(nm2, cg2, ag2); tu[j] = __builtin_elementwise_fma(nm2, cu2, au2);
        e[j] = tg[j] * ke2;
      }
#pragma unroll
      for (int j = 0; j < 4; ++j) { e[j][0] = __builtin_amdgcn_exp2f(e[j][0]); e[j][1] = __builtin_amdgcn_exp2f(e[j][1]); tg[j] = (tg[j] * tu[j]) * r22; }
#pragma unroll
      for (int j = 0; j < 4; ++j) e[j] += one2;
#pragma unroll
      for (int j = 0; j < 4; ++j) { e[j][0] = __builtin_amdgcn_rcpf(e[j][0]); e[j][1] = __builtin_amdgcn_rcpf(e[j][1]); }
      u32x4 pk;
#pragma unroll
      for (int j = 0; j < 4; ++j) { const f32x2 a2 = tg[j] * e[j]; pk[j] = pk2(a2[0], a2[1]); }
      *(u32x4*)(ws + WS_ACT + ((size_t)(rb * 4 + tp * 2 + t) * NAT + at0 + ag * 3 + i) * 1024 + lane * 16) = pk;
    }
  }
  STAMP(7); STAMP_FLUSH(3);
}

extern "C" void kernel_launch(void* const* d_in, const int* in_sizes, int n_in, void* d_out, int out_size, void* d_ws, size_t ws_size,
                              hipStream_t stream) {
  const u16* x = (const u16*)d_in[0];
  const u16* wq = (const u16*)d_in[1];
  const u16* wk = (const u16*)d_in[2];
  const u16* wv = (const u16*)d_in[3];
  const u16* wo = (const u16*)d_in[4];
  const u16* wg = (const u16*)d_in[6];
  const u16* wu = (const u16*)d_in[7];
  const u16* wd = (const u16*)d_in[8];
  char* ws = (char*)d_ws;
  (void)hipFuncSetAttribute((const void*)k1_qkv, hipFuncAttributeMaxDynamicSharedMemorySize, K1_LDS);
  (void)hipFuncSetAttribute((const void*)k2_attn, hipFuncAttributeMaxDynamicSharedMemorySize, K2_LDS);
  (void)hipFuncSetAttribute((const void*)k3_gateup, hipFuncAttributeMaxDynamicSharedMemorySize, K3_LDS);
  (void)hipFuncSetAttribute((const void*)k64<1>, hipFuncAttributeMaxDynamicSharedMemorySize, K4_LDS);
  k1_qkv<<<256, 512, K1_LDS, stream>>>(x, wq, wk, wv, wo, ws);
  k2_attn<<<256, 512, K2_LDS, stream>>>(x, wg, wu, wd, ws);
  k3_gateup<<<256, 512, K3_LDS, stream>>>(wg, wu, wd, ws);
  k64<1><<<256, 512, K4_LDS, stream>>>(wd, x, ws, (u16*)d_out);
}
```

```cpp
#include <hip/hip_runtime.h>
#include <hip/hip_bf16.h>
#include <stdint.h>

typedef __attribute__((ext_vector_type(8))) short bf16x8;
typedef __attribute__((ext_vector_type(4))) short s16x4;
typedef __attribute__((ext_vector_type(2))) __bf16 bf16x2;
typedef __attribute__((ext_vector_type(4))) unsigned u32x4;
typedef __attribute__((ext_vector_type(2))) unsigned u32x2;
typedef __attribute__((ext_vector_type(16))) float f32x16;
typedef __attribute__((ext_vector_type(4))) float f32x4;
typedef __attribute__((ext_vector_type(2))) float f32x2;
typedef __attribute__((address_space(3))) char* lds_ptr;
typedef unsigned short u16;

#define DI __device__ __forceinline__
#define MFMA32(a, b, c) __builtin_amdgcn_mfma_f32_32x32x16_bf16((a), (b), (c), 0, 0, 0)
#define MFMA16(a, b, c) __builtin_amdgcn_mfma_f32_16x16x32_bf16((a), (b), (c), 0, 0, 0)
#define WAIT_VM_BAR(N) asm volatile("s_waitcnt vmcnt(" #N ") lgkmcnt(0)\n\ts_barrier" ::: "memory")
#define BAR() asm volatile("s_waitcnt lgkmcnt(0)\n\ts_barrier" ::: "memory")

constexpr int SEQ = 2048, DM = 512, FF = 1376, NAT = 86;
constexpr float QSCALE = 0.25f * 1.4426950408889634f;
constexpr float LN_EPS = 1e-5f;

constexpr size_t WS_Q = 0;
constexpr size_t WS_K = WS_Q + (size_t)SEQ * DM * 2;
constexpr size_t WS_V = WS_K + 65536;
constexpr size_t WS_KMAX = WS_V + 65536;
constexpr size_t WS_WO = WS_KMAX + 4096;
constexpr size_t WS_H = WS_WO + (size_t)DM * DM * 2;
constexpr size_t WS_ST = WS_H + (size_t)SEQ * DM * 2;
constexpr size_t WS_CS = WS_ST + (size_t)SEQ * 16 * 8;
constexpr size_t WS_WGU = WS_CS + 2 * 8 * 1408 * 4;
constexpr size_t WS_ACT = WS_WGU + (size_t)NAT * 32768;

#define STAMP_DECL
#define STAMP(i) do { } while (0)
#define STAMP_FLUSH(kid) do { } while (0)
#define ACC_BEGIN() do { } while (0)
#define ACC_END(i) do { } while (0)
DI void glds16(const void* g, unsigned lds_base) {
  unsigned sv;
  asm volatile("s_mov_b32 %0, m0\n\ts_mov_b32 m0, %2\n\ts_nop 0\n\tglobal_load_lds_dwordx4 %1, off\n\ts_mov_b32 m0, %0"
               : "=&s"(sv) : "v"(g), "s"(lds_base) : "memory");
}
DI void glds4(const void* g, unsigned lds_base) {
  unsigned sv;
  asm volatile("s_mov_b32 %0, m0\n\ts_mov_b32 m0, %2\n\ts_nop 0\n\tglobal_load_lds_dword %1, off\n\ts_mov_b32 m0, %0"
               : "=&s"(sv) : "v"(g), "s"(lds_base) : "memory");
}
DI unsigned rfl(unsigned v) { return (unsigned)__builtin_amdgcn_readfirstlane((int)v); }
DI unsigned pk2(float lo, float hi) {
  bf16x2 v; v[0] = (__bf16)lo; v[1] = (__bf16)hi;
  return __builtin_bit_cast(unsigned, v);
}
DI int ffeat(int at, int rho) { return at < 84 ? 64 * (at >> 2) + 8 * (rho & 7) + 2 * (at & 3) + (rho >> 3) : 1344 + 8 * (rho & 3) + 4 * (at - 84) + (rho >> 2); }
DI void st16_wt(void* p, u32x4 v) { asm volatile("global_store_dwordx4 %0, %1, off sc1\n\ts_nop 1" :: "v"(p), "v"(v) : "memory"); }
DI float rbf(float v) { return (float)(__bf16)v; }
DI float ln_rstd(float var) { return __builtin_amdgcn_rcpf(rbf(__builtin_amdgcn_sqrtf(rbf(rbf(var) + LN_EPS)))); }
DI float bflo(unsigned u) { return __uint_as_float(u << 16); }
DI float bfhi(unsigned u) { return __uint_as_float(u & 0xffff0000u); }
DI float dot2(unsigned a, unsigned b, float c) {
  return __builtin_amdgcn_fdot2_f32_bf16(__builtin_bit_cast(bf16x2, a), __builtin_bit_cast(bf16x2, b), c, false);
}
DI bf16x8 lds_b128(lds_ptr p) { return *(const __attribute__((address_space(3))) bf16x8*)p; }
DI s16x4 lds_tr(lds_ptr p) {
  typedef short v4i16_t __attribute__((ext_vector_type(4)));
  return __builtin_bit_cast(s16x4, __builtin_amdgcn_ds_read_tr16_b64_v4i16((__attribute__((address_space(3))) v4i16_t*)p));
}
DI float swap32_sum(float v) {
  auto r = __builtin_amdgcn_permlane32_swap(__float_as_uint(v), __float_as_uint(v), false, false);
  return __uint_as_float(r[0]) + __uint_as_float(r[1]);
}
DI float swap16_sum(float v) {
  auto r = __builtin_amdgcn_permlane16_swap(__float_as_uint(v), __float_as_uint(v), false, false);
  return __uint_as_float(r[0]) + __uint_as_float(r[1]);
}
DI float swap32_max(float v) {
  auto r = __builtin_amdgcn_permlane32_swap(__float_as_uint(v), __float_as_uint(v), false, false);
  return fmaxf(__uint_as_float(r[0]), __uint_as_float(r[1]));
}
template <int CTRL, int RM>
DI float dpp(float old, float v) { return __uint_as_float((unsigned)__builtin_amdgcn_update_dpp((int)__float_as_uint(old), (int)__float_as_uint(v), CTRL, RM, 0xf, false)); }
DI float wave_sum(float v) {
  v += dpp<0x111, 0xf>(0.f, v); v += dpp<0x112, 0xf>(0.f, v); v += dpp<0x114, 0xf>(0.f, v); v += dpp<0x118, 0xf>(0.f, v);
  v += dpp<0x142, 0xa>(0.f, v); v += dpp<0x143, 0xc>(0.f, v);
  return __uint_as_float((unsigned)__builtin_amdgcn_readlane((int)__float_as_uint(v), 63));
}
DI float wave_max(float v) {
  v = fmaxf(v, dpp<0x111, 0xf>(0.f, v)); v = fmaxf(v, dpp<0x112, 0xf>(0.f, v)); v = fmaxf(v, dpp<0x114, 0xf>(0.f, v)); v = fmaxf(v, dpp<0x118, 0xf>(0.f, v));
  v = fmaxf(v, dpp<0x142, 0xa>(0.f, v)); v = fmaxf(v, dpp<0x143, 0xc>(0.f, v));
  return __uint_as_float((unsigned)__builtin_amdgcn_readlane((int)__float_as_uint(v), 63));
}
DI float quad_sum(float v) { v += dpp<0xb1, 0xf>(0.f, v); v += dpp<0x4e, 0xf>(0.f, v); return v; }

#define SB() __builtin_amdgcn_sched_barrier(0)
#define PIN(x) asm volatile("" : "+v"(x))
#define PINM(x) asm volatile("" : "+v"(x) :: "memory")
#define LDS_F32X4(p) (*(__attribute__((address_space(3))) f32x4*)(p))
#define LDS_F32X2(p) (*(__attribute__((address_space(3))) f32x2*)(p))
#define LDS_F32(p) (*(__attribute__((address_space(3))) float*)(p))
#define LDS_U32X2(p) (*(__attribute__((address_space(3))) u32x2*)(p))
#define LDS_U32X4(p) (*(__attribute__((address_space(3))) u32x4*)(p))
#define LDS_VU32(p) (*(volatile __attribute__((address_space(3))) unsigned*)(p))
#define WAIT_VM(N) asm volatile("s_waitcnt vmcnt(" #N ")" ::: "memory")
#define RAWBAR() asm volatile("s_barrier" ::: "memory")
#define LGKM0() asm volatile("s_waitcnt lgkmcnt(0)" ::: "memory")
DI bf16x8 cat8(s16x4 a, s16x4 b) { return __builtin_shufflevector(a, b, 0, 1, 2, 3, 4, 5, 6, 7); }
constexpr unsigned ONES = 0x3f803f80u;
#define LDS_VU32X4(p) (*(volatile __attribute__((address_space(3))) u32x4*)(p))
DI unsigned min4(u32x4 v) { return min(min(v[0], v[1]), min(v[2], v[3])); }
#define WAIT_CNT(p, n) do { while (rfl(min4(LDS_VU32X4(p))) < (unsigned)(n)) __builtin_amdgcn_s_sleep(1); asm volatile("" ::: "memory"); } while (0)

constexpr int K1_X = 0, K1_W = 65536, K1_KV = 131072, K1_SC = 147456, K1_FLG = K1_SC + 2048 + 8192, K1_LDS = K1_FLG + 32;

__global__ __launch_bounds__(640) void k1_qkv(const u16* __restrict__ x, const u16* __restrict__ wq, const u16* __restrict__ wk,
                                              const u16* __restrict__ wv, const u16* __restrict__ wo, const u16* __restrict__ wg,
                                              const u16* __restrict__ wu, char* __restrict__ ws) {
  extern __shared__ __attribute__((aligned(16))) char smem[];
  const lds_ptr lds = (lds_ptr)smem;
  const unsigned lds0 = (unsigned)(uintptr_t)smem;
  const int tid = threadIdx.x, lane = tid & 63;
  const int w = (int)rfl(tid >> 6);
  const int bid = blockIdx.x;
  const int tb = (bid & 7) * 4 + ((bid >> 3) & 3), fb = bid >> 5;
  const bool kvwg = fb < 2;
  STAMP_DECL STAMP(0);
  if (w >= 8) {
    RAWBAR();
    const int job = (w - 8) * 256 + (255 - bid);
    if (job < 416) {
        const bool isw = job < 64;
        const int jg = job - 64, mat = jg >= 176, jj = jg - mat * 176, ksl = jj & 7, cg = jj >> 3;
        const int col = cg * 64 + (lane & 7) * 8, kc = ksl * 8 + (lane >> 3);
        const u16* src = isw ? wo + (size_t)((job >> 3) * 64 + (job & 1) * 32 + 8 * (lane >> 4)) * DM + ((job >> 1) & 3) * 128 + (lane & 15) * 8
                             : (mat ? wu : wg) + (size_t)kc * 8 * FF + (col < FF ? col : 0);
        const int ld = isw ? DM : FF;
#define K1_SJ 2
        WAIT_CNT(lds + K1_FLG, K1_SJ);
        u32x4 R[8];
#pragma unroll
        for (int j = 0; j < 8; ++j) R[j] = *(const u32x4*)(src + (size_t)j * ld);
        char* dst = isw ? ws + WS_WO + (size_t)job * 8192 + lane * 16
                        : ws + WS_WGU + ((size_t)(cg * 128 + (kc >> 1)) * 64 + (kc & 1) * 32 + mat * 16 + (lane & 7)) * 16;
        float c[8];
#pragma unroll
        for (int m = 0; m < 4; ++m) {
          u32x4 lo, hi;
#pragma unroll
          for (int i = 0; i < 4; ++i) { lo[i] = __builtin_amdgcn_perm(R[2 * i + 1][m], R[2 * i][m], 0x05040100u); hi[i] = __builtin_amdgcn_perm(R[2 * i + 1][m], R[2 * i][m], 0x07060302u); }
          const int o0 = isw ? 2 * m * 1024 : cg < 21 ? m * 32768 : (m >> 1) * 32768 + (m & 1) * 128, o1 = o0 + (isw ? 1024 : cg < 21 ? 128 : 64);
          if (isw || col < FF) { st16_wt(dst + o0, lo); st16_wt(dst + o1, hi); }
          c[2 * m] = dot2(lo[3], ONES, dot2(lo[2], ONES, dot2(lo[1], ONES, dot2(lo[0], ONES, 0.f))));
          c[2 * m + 1] = dot2(hi[3], ONES, dot2(hi[2], ONES, dot2(hi[1], ONES, dot2(hi[0], ONES, 0.f))));
        }
        if (!isw) {
#pragma unroll
          for (int i = 0; i < 8; ++i) { c[i] += dpp<0x128, 0xf>(0.f, c[i]); c[i] = swap32_sum(swap16_sum(c[i])); }
          if (lane < 8) {
            float* cd = (float*)(ws + WS_CS) + (mat * 8 + ksl) * 1408 + cg * 64 + lane * 8;
            *(f32x4*)cd = f32x4{c[0], c[1], c[2], c[3]};
            *(f32x4*)(cd + 4) = f32x4{c[4], c[5], c[6], c[7]};
          }
        }
    }
    return;
  }
  if (w >= 4) {
    const int lw = w - 4;
    const u16* xs = x + (size_t)(tb * 64 + lw * 16 + (lane >> 4)) * DM;
    const int xc = (lane & 15) ^ (lane >> 4);
    const int rr = lane >> 3, pos = lane & 7;
    const u16* wsrc = wq + (size_t)(lw * 32 + rr) * DM + fb * 64 + ((pos ^ (((rr >> 1) & 1) << 2)) * 8);
    const u16* kvs = (fb ? wv : wk) + lw * 512 + lane * 8;
#define K1_ISSUE(st, KV) do { _Pragma("unroll") for (int i = 0; i < 4; ++i) { \
      glds16(xs + (size_t)i * 4 * DM + (st) * 128 + ((xc ^ (4 * i)) * 8), lds0 + K1_X + (st) * 16384 + (lw * 4 + i) * 1024); \
      glds16(wsrc + (size_t)((st) * 128 + i * 8) * DM, lds0 + K1_W + (st) * 16384 + (lw * 4 + i) * 1024); } \
    if (KV) glds16(kvs + (st) * 2048, lds0 + K1_KV + (st) * 4096 + lw * 1024); } while (0)
#define K1_PUB(n) LDS_VU32(lds + K1_FLG + lw * 4) = (n)
    if (kvwg) {
      K1_ISSUE(0, 1); RAWBAR(); K1_ISSUE(1, 1);
      WAIT_VM(9); K1_PUB(1); K1_ISSUE(2, 1); WAIT_VM(9); K1_PUB(2); K1_ISSUE(3, 1); WAIT_VM(9); K1_PUB(3);
    } else {
      K1_ISSUE(0, 0); RAWBAR(); K1_ISSUE(1, 0);
      WAIT_VM(8); K1_PUB(1); K1_ISSUE(2, 0); WAIT_VM(8); K1_PUB(2); K1_ISSUE(3, 0); WAIT_VM(8); K1_PUB(3);
    }
    WAIT_VM(0); K1_PUB(4);
    return;
  }
  if (tid < 8) LDS_VU32(lds + K1_FLG + tid * 4) = 0u;
  BAR();
  const int r32 = lane & 31, h = lane >> 5;
  const int blk = (lane >> 4) & 1, q = (lane & 15) >> 2, p = lane & 3;
  const int tt = w & 1, ft = w >> 1;
  const int tok = tt * 32 + r32;
  lds_ptr xb[8];
#pragma unroll
  for (int i = 0; i < 8; ++i) xb[i] = lds + K1_X + tok * 256 + (((2 * i + h) ^ (tok & 15)) * 16);
  const lds_ptr wb = lds + K1_W + (8 * h + q) * 128 + (((ft * 4 + blk * 2 + (p >> 1)) ^ ((q >> 1) << 2)) * 16) + 8 * (p & 1);
  const lds_ptr kb = lds + K1_KV + (8 * h + q) * 32 + 8 * p;
  const float piv = __uint_as_float((unsigned)x[(size_t)(tb * 64 + tok) * DM] << 16);
  f32x16 acc = {}, acc2 = {};
  float cs = 0.f, cs2 = 0.f, sx = 0.f, sxx = 0.f, sxb = 0.f, sxxb = 0.f;
  s16x4 a[2][4][2]; bf16x8 b[2][4];
#define K1_STEP(bu, MM, LD, s0, sc0) do { _Pragma("unroll") for (int i = 0; i < 4; ++i) { const int s = (s0) + i; \
      if (MM) { acc = MFMA32(cat8(a[bu][i][0], a[bu][i][1]), b[bu][i], acc); PINM(acc); } \
      if (LD) { a[1 - (bu)][i][0] = lds_tr(wb + s * 2048); a[1 - (bu)][i][1] = lds_tr(wb + s * 2048 + 512); b[1 - (bu)][i] = lds_b128(xb[s & 7] + (s >> 3) * 16384); } SB(); } \
    if (MM) { \
      if (ft == (bu)) { \
        if (kvwg) { bf16x8 ka[4]; \
          _Pragma("unroll") for (int i = 0; i < 4; ++i) ka[i] = cat8(lds_tr(kb + ((sc0) + i) * 512), lds_tr(kb + ((sc0) + i) * 512 + 128)); \
          if (fb == 0) { _Pragma("unroll") for (int i = 0; i < 4; ++i) acc2 = MFMA32(ka[i], b[bu][i], acc2); } \
          else { _Pragma("unroll") for (int i = 0; i < 4; ++i) acc2 = MFMA32(b[bu][i], ka[i], acc2); } \
          PINM(acc2); SB(); \
          _Pragma("unroll") for (int i = 0; i < 4; ++i) { const u32x4 u = __builtin_bit_cast(u32x4, ka[i]); \
            cs2 = dot2(u[0], ONES, cs2); cs2 = dot2(u[1], ONES, cs2); cs2 = dot2(u[2], ONES, cs2); cs2 = dot2(u[3], ONES, cs2); } PIN(cs2); } \
        _Pragma("unroll") for (int i = 0; i < 4; ++i) { const u32x4 u = __builtin_bit_cast(u32x4, b[bu][i]); \
          sx = dot2(u[0], ONES, sx); sxx = dot2(u[0], u[0], sxx); sxb = dot2(u[1], ONES, sxb); sxxb = dot2(u[1], u[1], sxxb); \
          sx = dot2(u[2], ONES, sx); sxx = dot2(u[2], u[2], sxx); sxb = dot2(u[3], ONES, sxb); sxxb = dot2(u[3], u[3], sxxb); } \
        PIN(sx); PIN(sxx); PIN(sxb); PIN(sxxb); } \
      if (tt == (bu)) { _Pragma("unroll") for (int i = 0; i < 4; ++i) { const u32x4 u = __builtin_bit_cast(u32x4, cat8(a[bu][i][0], a[bu][i][1])); \
          cs = dot2(u[0], ONES, cs); cs = dot2(u[1], ONES, cs); cs = dot2(u[2], ONES, cs); cs = dot2(u[3], ONES, cs); } PIN(cs); } \
      SB(); } } while (0)
  STAMP(1);
#pragma unroll
  for (int st = 0; st < 4; ++st) { WAIT_CNT(lds + K1_FLG, st + 1); K1_STEP(1, st > 0, true, st * 8, st * 8 - 4); K1_STEP(0, true, true, st * 8 + 4, st * 8); }
  K1_STEP(1, true, false, 0, 28);
  STAMP(2);
  const lds_ptr sc = lds + K1_SC;
  {
    sx += sxb; sxx += sxxb;
    f32x2 d; d[0] = swap32_sum(sx - 128.f * piv); d[1] = swap32_sum(__builtin_fmaf(-2.f * piv, sx, sxx) + 128.f * piv * piv);
    cs = swap32_sum(cs); cs2 = swap32_sum(cs2);
    if (h == 0) { LDS_F32(sc + (ft * 2 + tt) * 128 + r32 * 4) = cs; LDS_F32X2(sc + 512 + (tt * 2 + ft) * 256 + r32 * 8) = d; LDS_F32(sc + 1536 + w * 128 + r32 * 4) = cs2; }
    if (kvwg && ft == 1) {
#pragma unroll
      for (int g = 0; g < 4; ++g) { f32x4 t; t[0] = acc2[4 * g]; t[1] = acc2[4 * g + 1]; t[2] = acc2[4 * g + 2]; t[3] = acc2[4 * g + 3]; LDS_F32X4(sc + 2048 + tt * 4096 + g * 1024 + lane * 16) = t; }
    }
  }
  asm volatile("" ::: "memory");
  LDS_VU32(lds + K1_FLG + 16 + w * 4) = 1u;
  WAIT_CNT(lds + K1_FLG + 16, 1);
  float mean, rstd;
  {
    const f32x2 d = LDS_F32X2(sc + 512 + tt * 512 + r32 * 8) + LDS_F32X2(sc + 512 + tt * 512 + 256 + r32 * 8);
    const float dmn = d[0] * (1.f / DM);
    mean = rbf(piv + dmn); rstd = ln_rstd(fmaxf(d[1] * (1.f / DM) - dmn * dmn, 0.f));
  }
  {
    const float scl = rstd * QSCALE;
    float o[16];
#pragma unroll
    for (int g = 0; g < 4; ++g) {
      const f32x4 c4 = LDS_F32X4(sc + ft * 256 + (8 * g + 4 * h) * 4) + LDS_F32X4(sc + ft * 256 + 128 + (8 * g + 4 * h) * 4);
#pragma unroll
      for (int j = 0; j < 4; ++j) o[4 * g + j] = (acc[4 * g + j] - mean * c4[j]) * scl;
    }
    u32x4 p0, p1;
#pragma unroll
    for (int i = 0; i < 4; ++i) { p0[i] = pk2(o[2 * i], o[2 * i + 1]); p1[i] = pk2(o[8 + 2 * i], o[9 + 2 * i]); }
    char* dst = ws + WS_Q + (size_t)(tb * 64 + tok) * 1024 + (fb * 4 + ft * 2) * 32 + h * 16;
    *(u32x4*)dst = p0;
    *(u32x4*)(dst + 32) = p1;
  }
  if (kvwg && ft == 0) {
    const int gt = tb * 2 + tt;
#pragma unroll
    for (int g = 0; g < 4; ++g) { const f32x4 t = LDS_F32X4(sc + 2048 + tt * 4096 + g * 1024 + lane * 16);
#pragma unroll
      for (int j = 0; j < 4; ++j) acc2[4 * g + j] += t[j]; }
    if (fb == 0) {
      float o[8];
#pragma unroll
      for (int g = 0; g < 2; ++g) {
        const f32x4 c4 = LDS_F32X4(sc + 1536 + tt * 128 + (8 * g + 4 * h) * 4) + LDS_F32X4(sc + 1536 + (tt + 2) * 128 + (8 * g + 4 * h) * 4);
#pragma unroll
        for (int j = 0; j < 4; ++j) o[4 * g + j] = (acc2[4 * g + j] - mean * c4[j]) * rstd;
      }
      u32x4 p0;
#pragma unroll
      for (int i = 0; i < 4; ++i) p0[i] = pk2(o[2 * i], o[2 * i + 1]);
      *(u32x4*)(ws + WS_K + (size_t)gt * 1024 + h * 512 + r32 * 16) = p0;
      float n2 = 0.f;
#pragma unroll
      for (int i = 0; i < 4; ++i) n2 = dot2(p0[i], p0[i], n2);
      n2 = wave_max(swap32_sum(n2));
      if (lane == 0) *(float*)(ws + WS_KMAX + gt * 4) = n2;
    } else {
      const float cv = LDS_F32(sc + 1536 + tt * 128 + r32 * 4) + LDS_F32(sc + 1536 + (tt + 2) * 128 + r32 * 4);
      const lds_ptr sp = sc + 2048 + tt * 4096;
      LGKM0();
      if (h == 0) { f32x2 st; st[0] = mean; st[1] = rstd; LDS_F32X2(sp + r32 * 8) = st; }
      float o[16];
#pragma unroll
      for (int g = 0; g < 4; ++g) {
        const f32x4 s01 = LDS_F32X4(sp + (8 * g + 4 * h) * 8), s23 = LDS_F32X4(sp + (8 * g + 4 * h) * 8 + 16);
        o[4 * g + 0] = (acc2[4 * g + 0] - s01[0] * cv) * s01[1];
        o[4 * g + 1] = (acc2[4 * g + 1] - s01[2] * cv) * s01[3];
        o[4 * g + 2] = (acc2[4 * g + 2] - s23[0] * cv) * s23[1];
        o[4 * g + 3] = (acc2[4 * g + 3] - s23[2] * cv) * s23[3];
      }
      u32x4 p0, p1;
#pragma unroll
      for (int i = 0; i < 4; ++i) { p0[i] = pk2(o[2 * i], o[2 * i + 1]); p1[i] = pk2(o[8 + 2 * i], o[9 + 2 * i]); }
      if (r32 < 16) {
        char* dst = ws + WS_V + (size_t)gt * 1024 + h * 256 + r32 * 16;
        *(u32x4*)dst = p0;
        *(u32x4*)(dst + 512) = p1;
      }
    }
  }
  STAMP(3); STAMP_FLUSH(0);
}

constexpr int K2_PSZ = 2176;
constexpr int K2_RED = 0;
constexpr int K2_Q = 64 * K2_PSZ;
constexpr int K2_NM = K2_Q + 8192;
constexpr int K2_JUNK = K2_NM + 8192;
constexpr int K2_FLG = K2_JUNK + 256;
constexpr int K2_OB = K2_FLG + 512;
constexpr int K2_LDS = K2_OB + 2 * 1152;
constexpr int K2_MX = 0;

#define E_(r) "v_exp_f32 v" #r ", v" #r "\n\t"
#define C_(d, a, b) "v_cvt_pk_bf16_f32 v" #d ", v" #a ", v" #b "\n\t"
#define PVPREV_ "v_mfma_f32_32x32x16_bf16 v[200:215], %8, v[236:239], v[200:215]\n\t"
#define TILE_A1 E_(168) E_(169)
#define TILE_A2 E_(170) E_(171) E_(172) E_(173) "v_mfma_f32_32x32x16_bf16 v[184:199], %6, %7, v[216:231]\n\t" E_(174) E_(175) \
    C_(232, 168, 169) C_(233, 170, 171) C_(234, 172, 173) C_(235, 174, 175) E_(176) E_(177)
#define TILE_A3 E_(178) E_(179) E_(180) E_(181) E_(182) E_(183) C_(236, 176, 177) C_(237, 178, 179) C_(238, 180, 181) C_(239, 182, 183)
#define TILE_B1 E_(184) E_(185)
#define TILE_B2 E_(186) E_(187) E_(188) E_(189) "v_mfma_f32_32x32x16_bf16 v[168:183], %6, %7, v[216:231]\n\t" E_(190) E_(191) \
    C_(232, 184, 185) C_(233, 186, 187) C_(234, 188, 189) C_(235, 190, 191) E_(192) E_(193)
#define TILE_B3 E_(194) E_(195) E_(196) E_(197) E_(198) E_(199) C_(236, 192, 193) C_(237, 194, 195) C_(238, 196, 197) C_(239, 198, 199)
#define PV0_ "v_mfma_f32_32x32x16_bf16 v[200:215], %9, v[232:235], v[200:215]\n\t"
#define PV0Z_ "v_mfma_f32_32x32x16_bf16 v[200:215], %9, v[232:235], 0\n\t"
#define K2_ASM(TXT, KN, QN, VP, V0) asm volatile(TXT : "+{v[168:183]}"(sa), "+{v[184:199]}"(sb), "+{v[200:215]}"(o), "+{v[232:235]}"(p0), "+{v[236:239]}"(p1) \
    : "{v[216:231]}"(negm), "v"(KN), "v"(QN), "v"(VP), "v"(V0))
#define K2_ASM_NOC(TXT, V0) asm volatile(TXT : "+{v[168:183]}"(sa), "+{v[184:199]}"(sb), "+{v[200:215]}"(o), "+{v[232:235]}"(p0), "+{v[236:239]}"(p1) \
    : "v"(V0), "v"(V0), "v"(V0), "v"(V0), "v"(V0))

__global__ __launch_bounds__(512) void k2_attn(const u16* __restrict__ x, const u16* __restrict__ wd, char* __restrict__ ws) {
  extern __shared__ __attribute__((aligned(16))) char smem[];
  const lds_ptr lds = (lds_ptr)smem;
  const unsigned lds0 = (unsigned)(uintptr_t)smem;
  const int tid = threadIdx.x, lane = tid & 63;
  const int w = (int)rfl(tid >> 6);
  const int g8 = (blockIdx.x & 7) * 32 + (blockIdx.x >> 3);
  const int r32 = lane & 31, h = lane >> 5;
  STAMP_DECL STAMP(0);
  const u32x4 qown = *(const u32x4*)(ws + WS_Q + (size_t)(g8 * 8 + (r32 & 7)) * 1024 + (w * 4 + (r32 >> 3)) * 32 + h * 16);
  const float km2l = *(const float*)(ws + WS_KMAX + lane * 4);
  bf16x8 kf[8], vf[8][2];
  u32x4 vr[8];
  const char* ksrc = ws + WS_K + (size_t)w * 8192 + lane * 16;
  const char* vsrc = ws + WS_V + (size_t)w * 8192 + (r32 >> 4) * 512 + h * 256 + (lane & 15) * 16;
#define K2_LOADKV(j) do { kf[j] = *(const bf16x8*)(ksrc + (j) * 1024); vr[j] = *(const u32x4*)(vsrc + (j) * 1024); } while (0)
#define K2_SPLITV(j) do { u32x4 a_, b_; _Pragma("unroll") for (int i_ = 0; i_ < 4; ++i_) { \
    const auto r_ = __builtin_amdgcn_permlane16_swap(vr[j][i_], ONES, false, false); a_[i_] = r_[0]; b_[i_] = r_[1]; } \
    vf[j][0] = __builtin_bit_cast(bf16x8, a_); vf[j][1] = __builtin_bit_cast(bf16x8, b_); } while (0)
  K2_LOADKV(0); K2_LOADKV(1);
  if (w < 4) __builtin_amdgcn_s_setprio(2); else __builtin_amdgcn_s_setprio(1);
  if (tid >= 8 && tid < 128) LDS_VU32(lds + K2_FLG + tid * 4) = 0u;
  {
    float n2 = 0.f;
#pragma unroll
    for (int i = 0; i < 4; ++i) n2 = dot2(qown[i], qown[i], n2);
    const float mref = sqrtf(swap32_sum(n2) * wave_max(km2l));
    const f32x4 nm = {-mref, -mref, -mref, -mref};
    LDS_U32X4(lds + K2_Q + w * 1024 + lane * 16) = qown;
    LDS_F32X4(lds + K2_NM + w * 1024 + lane * 16) = nm;
    LDS_VU32(lds + K2_FLG + w * 4) = __any(!(mref <= 40.f)) ? 1u : 0u;
  }
  STAMP(1);
  BAR();
  STAMP(2);
  K2_LOADKV(2); K2_LOADKV(3); K2_LOADKV(4); K2_LOADKV(5); K2_LOADKV(6); K2_LOADKV(7);
  const lds_ptr qp = lds + K2_Q + lane * 16;
  if (__any(LDS_VU32(lds + K2_FLG + (lane & 7) * 4) != 0u)) {
    const f32x16 zero = {};
    for (int g = 0; g < 8; ++g) {
      const bf16x8 qf = lds_b128(qp + g * 1024);
      float mx = -3.0e38f;
#pragma unroll
      for (int j = 0; j < 8; ++j) {
        const f32x16 s = MFMA32(kf[j], qf, zero);
#pragma unroll
        for (int i = 0; i < 16; ++i) mx = fmaxf(mx, s[i]);
      }
      mx = swap32_max(mx);
      if (h == 0) LDS_F32(lds + K2_MX + (g * 8 + w) * 128 + r32 * 4) = mx;
    }
    BAR();
    float mx = LDS_F32(lds + K2_MX + w * 1024 + r32 * 4);
#pragma unroll
    for (int i = 1; i < 8; ++i) mx = fmaxf(mx, LDS_F32(lds + K2_MX + w * 1024 + i * 128 + r32 * 4));
    const f32x4 nm = {-mx, -mx, -mx, -mx};
    LDS_F32X4(lds + K2_NM + w * 1024 + lane * 16) = nm;
    BAR();
  }
  f32x16 negm;
#define K2_LOAD_NEGM(g) do { const lds_ptr np_ = lds + K2_NM + (g) * 1024 + lane * 16; \
    _Pragma("unroll") for (int i_ = 0; i_ < 4; ++i_) { const f32x4 t_ = *(volatile __attribute__((address_space(3))) f32x4*)(np_); \
      negm[4 * i_] = t_[0]; negm[4 * i_ + 1] = t_[1]; negm[4 * i_ + 2] = t_[2]; negm[4 * i_ + 3] = t_[3]; } } while (0)
  K2_LOAD_NEGM(0);
  f32x16 o = {}, sa, sb = {};
  u32x4 p0 = {}, p1 = {};
  bf16x8 qf = lds_b128(qp);
  sa = MFMA32(kf[0], qf, negm);
  const lds_ptr redw = lds + K2_RED + w * K2_PSZ + lane * 16;
#define K2_WRITE_PARTIAL(g) do { const lds_ptr rp_ = redw + (g) * (8 * K2_PSZ); f32x4 t0_, t1_; \
    t0_[0] = o[0]; t0_[1] = o[1]; t0_[2] = o[2]; t0_[3] = o[3]; t1_[0] = o[4]; t1_[1] = o[5]; t1_[2] = o[6]; t1_[3] = o[7]; \
    LDS_F32X4(rp_) = t0_; LDS_F32X4(rp_ + 1024) = t1_; LDS_F32(rp_ + 2048 - lane * 16 + r32 * 4) = o[8]; \
    asm volatile("" ::: "memory"); LDS_VU32(lds + K2_FLG + 32 + ((g) * 8 + w) * 4) = 1u; } while (0)
#define VMWAIT0() __builtin_amdgcn_s_waitcnt(0x0F70)
#define SPL(j) do { if (FIRST_) { K2_SPLITV(j); asm volatile("s_nop 1" : "+v"(vf[j][0]), "+v"(vf[j][1])); } } while (0)
#define K2_ITER(g, FIRST) do { constexpr bool FIRST_ = FIRST; \
    SPL(0); \
    if (FIRST) K2_ASM(TILE_A1 TILE_A2 PV0Z_ TILE_A3, kf[1], qf, vf[0][0], vf[0][0]); \
    else { K2_ASM(TILE_A1 PVPREV_ TILE_A2, kf[1], qf, vf[7][1], vf[0][0]); K2_WRITE_PARTIAL((g) - 1); K2_ASM(PV0Z_ TILE_A3, kf[1], qf, vf[7][1], vf[0][0]); } \
    const bf16x8 qn = lds_b128(qp + (((g) + 1) & 7) * 1024); \
    SPL(1); K2_ASM(TILE_B1 PVPREV_ TILE_B2 PV0_ TILE_B3, kf[2], qf, vf[0][1], vf[1][0]); \
    SPL(2); K2_ASM(TILE_A1 PVPREV_ TILE_A2 PV0_ TILE_A3, kf[3], qf, vf[1][1], vf[2][0]); \
    SPL(3); K2_ASM(TILE_B1 PVPREV_ TILE_B2 PV0_ TILE_B3, kf[4], qf, vf[2][1], vf[3][0]); \
    SPL(4); K2_ASM(TILE_A1 PVPREV_ TILE_A2 PV0_ TILE_A3, kf[5], qf, vf[3][1], vf[4][0]); \
    SPL(5); K2_ASM(TILE_B1 PVPREV_ TILE_B2 PV0_ TILE_B3, kf[6], qf, vf[4][1], vf[5][0]); \
    SPL(6); K2_ASM(TILE_A1 PVPREV_ TILE_A2, kf[7], qf, vf[5][1], vf[6][0]); \
    K2_LOAD_NEGM(((g) + 1) & 7); \
    K2_ASM_NOC(PV0_ TILE_A3, vf[6][0]); \
    SPL(7); K2_ASM(TILE_B1 PVPREV_ TILE_B2 PV0_ TILE_B3, kf[0], qn, vf[6][1], vf[7][0]); \
    qf = qn; } while (0)

  STAMP(3);
  asm volatile("s_nop 15" : "+{v[168:183]}"(sa));
  K2_ITER(0, true);
  VMWAIT0();
  for (int g = 1; g < 8; ++g) {
    if (g <= 4) {
      const int wi = (blockIdx.x >> 3) * 8 + w;
      const char* src = g == 2 ? (const char*)ws + WS_WO + (wi * 16 + (lane & 15)) * 128 : (g == 1 ? (const char*)wd : ws + WS_WGU + (g - 3) * (NAT * 16384)) + (wi * 43 + (lane < 43 ? lane : 42)) * 128;
      glds4(src, lds0 + K2_JUNK);
    }
    K2_ITER(g, false);
  }
  STAMP(4);
  K2_ASM("s_nop 1\n\t" PVPREV_ "s_nop 15", kf[0], qf, vf[7][1], vf[7][0]);
  K2_WRITE_PARTIAL(7);
  if (w >= 4) { STAMP(5); STAMP_FLUSH(1); return; }
  asm volatile("s_waitcnt vmcnt(0)" ::: "memory");
  VMWAIT0();
  const int g4 = lane >> 4, tok8 = lane & 7;
  const char* wsrc = ws + WS_WO + (size_t)w * 16384 + lane * 16;
  bf16x8 wr[5][8];
#define K2_WLOAD(hh) do { if ((hh) < 16) { _Pragma("unroll") for (int i_ = 0; i_ < 8; ++i_) wr[(hh) % 5][i_] = *(const bf16x8*)(wsrc + (size_t)((hh) >> 1) * 65536 + ((hh) & 1) * 8192 + i_ * 1024); } } while (0)
  f32x4 oacc[8];
  const float piv = __uint_as_float((unsigned)x[(size_t)(g8 * 8 + tok8) * DM + w * 128] << 16);
  {
    u32x4 xr[4];
#pragma unroll
    for (int r = 0; r < 4; ++r) xr[r] = *(const u32x4*)(x + (size_t)(g8 * 8 + tok8) * DM + w * 128 + g4 * 32 + r * 8);
    K2_WLOAD(0); K2_WLOAD(1); K2_WLOAD(2);
#pragma unroll
    for (int r = 0; r < 4; ++r)
#pragma unroll
      for (int i = 0; i < 4; ++i) { oacc[2 * i][r] = bflo(xr[r][i]); oacc[2 * i + 1][r] = bfhi(xr[r][i]); }
  }
#define K2_PROJ(g) do { \
    while (!__all(LDS_VU32(lds + K2_FLG + 32 + ((g) * 8 + tok8) * 4) != 0u)) __builtin_amdgcn_s_sleep(2); \
    asm volatile("" ::: "memory"); \
    { const lds_ptr rp_ = lds + K2_RED + (g) * (8 * K2_PSZ) + (w >> 1) * 1024 + lane * 16 + (w & 1) * 8, lp_ = lds + K2_RED + (g) * (8 * K2_PSZ) + 2048 + r32 * 4; \
      f32x2 t_[8]; float l_[8]; \
      _Pragma("unroll") for (int i_ = 0; i_ < 8; ++i_) { t_[i_] = LDS_F32X2(rp_ + i_ * K2_PSZ); l_[i_] = LDS_F32(lp_ + i_ * K2_PSZ); } \
      SB(); K2_WLOAD(2 * (g) + 3); SB(); \
      const f32x2 ts_ = ((t_[0] + t_[1]) + (t_[2] + t_[3])) + ((t_[4] + t_[5]) + (t_[6] + t_[7])); \
      const float inv_ = __builtin_amdgcn_rcpf(((l_[0] + l_[1]) + (l_[2] + l_[3])) + ((l_[4] + l_[5]) + (l_[6] + l_[7]))); \
      LDS_VU32(lds + K2_OB + ((g) & 1) * 1152 + (r32 & 7) * 144 + (r32 >> 3) * 32 + h * 8 + (w >> 1) * 16 + (w & 1) * 4) = pk2(ts_[0] * inv_, ts_[1] * inv_); \
      LDS_VU32(lds + K2_FLG + 288 + ((g) * 4 + w) * 4) = 1u; } \
    SB(); K2_WLOAD(2 * (g) + 4); SB(); \
    while (!__all(LDS_VU32(lds + K2_FLG + 288 + ((g) * 4 + (lane & 3)) * 4) != 0u)) __builtin_amdgcn_s_sleep(1); \
    asm volatile("" ::: "memory"); \
    const bf16x8 b0_ = lds_b128(lds + K2_OB + ((g) & 1) * 1152 + tok8 * 144 + g4 * 16), b1_ = lds_b128(lds + K2_OB + ((g) & 1) * 1152 + tok8 * 144 + 64 + g4 * 16); \
    _Pragma("unroll") for (int f_ = 0; f_ < 8; ++f_) oacc[f_] = MFMA16(wr[(2 * (g)) % 5][f_], b0_, oacc[f_]); \
    _Pragma("unroll") for (int f_ = 0; f_ < 8; ++f_) { oacc[f_] = MFMA16(wr[(2 * (g) + 1) % 5][f_], b1_, oacc[f_]); PINM(oacc[f_]); } } while (0)
  STAMP(5);
  K2_PROJ(0); K2_PROJ(1); K2_PROJ(2); K2_PROJ(3); K2_PROJ(4); K2_PROJ(5); K2_PROJ(6); K2_PROJ(7);
  STAMP(6);
  {
    const int tok = g8 * 8 + tok8;
    u32x4 hb[4];
#pragma unroll
    for (int r = 0; r < 4; ++r)
#pragma unroll
      for (int i = 0; i < 4; ++i) hb[r][i] = pk2(oacc[2 * i][r], oacc[2 * i + 1][r]);
    if ((lane & 8) == 0) {
#pragma unroll
      for (int r = 0; r < 4; ++r) *(u32x4*)(ws + WS_H + (size_t)(tok >> 5) * 32768 + (w * 16 + g4 * 4 + r) * 512 + (tok & 31) * 16) = hb[r];
    }
    float sx = 0.f, sxx = 0.f;
#pragma unroll
    for (int r = 0; r < 4; ++r)
#pragma unroll
      for (int i = 0; i < 4; ++i) { sx = dot2(hb[r][i], ONES, sx); sxx = dot2(hb[r][i], hb[r][i], sxx); }
    const float d1 = swap32_sum(swap16_sum(sx - 32.f * piv)), d2 = swap32_sum(swap16_sum(__builtin_fmaf(-2.f * piv, sx, sxx) + 32.f * piv * piv));
    f32x2 st; st[0] = piv + d1 * (1.f / 128); st[1] = fmaxf(d2 - d1 * d1 * (1.f / 128), 0.f);
    if (lane < 8) *(f32x2*)(ws + WS_ST + ((size_t)((tok >> 5) * 4 + w) * 32 + (tok & 31)) * 8) = st;
  }
  STAMP(7); STAMP_FLUSH(1);
}

constexpr int K4_SLOT = 32768, K4_LDS = 5 * K4_SLOT;

template <int MODE>
__global__ __launch_bounds__(512) void k64(const u16* __restrict__ W, const u16* __restrict__ x, char* __restrict__ ws, u16* __restrict__ out) {
  constexpr int NKS = MODE ? NAT : 32;
  extern __shared__ __attribute__((aligned(16))) char smem[];
  const lds_ptr lds = (lds_ptr)smem;
  const unsigned lds0 = (unsigned)(uintptr_t)smem;
  const int tid = threadIdx.x, lane = tid & 63;
  const int w = (int)rfl(tid >> 6);
  const int bid = blockIdx.x;
  const int tb = (bid & 7) * 4 + ((bid >> 3) & 3), nb = bid >> 5;
  const int r32 = lane & 31, h = lane >> 5;
  STAMP_DECL STAMP(0);
  if (w >= 4) {
    const int lw = w - 4;
    const int rr = lane >> 3, pos = lane & 7;
    const char* wcol = (const char*)W + nb * 128 + (pos ^ (((rr >> 1) & 1) << 2)) * 16;
    const char* wsrc = wcol + (size_t)(64 * (lw >> 1) + 8 * rr + 4 * (lw & 1)) * 1024;
    const char* wlast = wcol + (size_t)(1344 + 8 * (rr & 3) + (rr >> 2)) * 1024;
    const char* asrc = ws + WS_ACT + ((size_t)(tb * 2 + (lw >> 1)) * NKS + (lw & 1) * 4) * 1024 + lane * 16;
#define K4_ISSUE(st) do { const unsigned sl_ = lds0 + ((st) % 5) * K4_SLOT; _Pragma("unroll") for (int i = 0; i < 4; ++i) { \
    const bool oka_ = (st) < 10 || !((lw & 1) && i >= 2); \
    glds16(asrc + (oka_ ? (st) * 8192 + i * 1024 : 0), sl_ + 16384 + (lw * 4 + i) * 1024); \
    glds16(((st) < 10 || lw < 2) ? wsrc + (size_t)(st) * 131072 + i * 1024 : wlast + i * 2048, sl_ + (lw * 4 + i) * 1024); } } while (0)
    if (MODE == 0) {
      K4_ISSUE(0); K4_ISSUE(1);
      WAIT_VM(8); RAWBAR(); K4_ISSUE(2);
      WAIT_VM(8); RAWBAR(); K4_ISSUE(3);
      WAIT_VM(8); RAWBAR();
      WAIT_VM(0); RAWBAR();
    } else {
      K4_ISSUE(0); K4_ISSUE(1); K4_ISSUE(2);
      WAIT_VM(16); RAWBAR(); K4_ISSUE(3); K4_ISSUE(4);
      WAIT_VM(24); RAWBAR(); K4_ISSUE(5);
      WAIT_VM(24); RAWBAR(); K4_ISSUE(6);
      WAIT_VM(24); RAWBAR(); K4_ISSUE(7);
      WAIT_VM(24); RAWBAR(); K4_ISSUE(8);
      WAIT_VM(24); RAWBAR(); K4_ISSUE(9);
      WAIT_VM(24); RAWBAR(); K4_ISSUE(10);
      WAIT_VM(24); RAWBAR();
      WAIT_VM(16); RAWBAR();
      WAIT_VM(8); RAWBAR();
      WAIT_VM(0); RAWBAR();
    }
    STAMP(7); STAMP_FLUSH(MODE ? 4 : 2);
    return;
  }
  const int nt = w & 1, tt = w >> 1;
  const int tok = tb * 64 + tt * 32 + r32;
  const int ncol = nb * 64 + nt * 32 + h * 16;
  const int blk = (lane >> 4) & 1, q = (lane & 15) >> 2, p = lane & 3;
  char* hfrag = ws + WS_H + (size_t)(tok >> 5) * 32768 + (ncol >> 3) * 512 + r32 * 16;
  u32x4 h0, h1;
  if (MODE) { h0 = *(const u32x4*)(hfrag); h1 = *(const u32x4*)(hfrag + 512); }
  else { h0 = *(const u32x4*)(x + (size_t)tok * DM + ncol); h1 = *(const u32x4*)(x + (size_t)tok * DM + ncol + 8); }
  f32x16 acc = {};
  const int cbyte = nt * 64 + (16 * (p & 1) + 8 * blk + 4 * (p >> 1)) * 2;
  lds_ptr ap[3], bp[3];
#pragma unroll
  for (int u = 0; u < 3; ++u) { ap[u] = lds + u * 2 * K4_SLOT + (4 * h + q) * 128 + (cbyte ^ ((q >> 1) << 6)); bp[u] = lds + u * 2 * K4_SLOT + 16384 + tt * 8192 + lane * 16; }
  s16x4 fa[2][4][2]; bf16x8 fb_[2][4];
#define K4_U(st) (((st) % 5) >> 1)
#define K4_O(st) ((((st) % 5) & 1) * K4_SLOT)
#define K4_LD1(bu, st, k0, k) do { fa[bu][k][0] = lds_tr(ap[K4_U(st)] + K4_O(st) + ((k0) + (k)) * 2048); fa[bu][k][1] = lds_tr(ap[K4_U(st)] + K4_O(st) + ((k0) + (k)) * 2048 + 1024); \
    fb_[bu][k] = lds_b128(bp[K4_U(st)] + K4_O(st) + ((k0) + (k)) * 1024); } while (0)
#define K4_STEP(bu, NM, NL, st, k0) do { _Pragma("unroll") for (int k = 0; k < 4; ++k) { \
    if (k < (NM)) { acc = MFMA32(cat8(fa[bu][k][0], fa[bu][k][1]), fb_[bu][k], acc); PINM(acc); } \
    if (k < (NL)) K4_LD1(1 - (bu), st, k0, k); SB(); } } while (0)
#define K4_STAGE(st, NK2) do { BAR(); K4_STEP(1, (st) > 0 ? 4 : 0, 4, st, 0); K4_STEP(0, 4, NK2, st, 4); } while (0)
  STAMP(1);
  K4_STAGE(0, 4); STAMP(2); K4_STAGE(1, 4); STAMP(3); K4_STAGE(2, 4); K4_STAGE(3, 4);
  if (MODE) {
    K4_STAGE(4, 4); STAMP(4); K4_STAGE(5, 4); K4_STAGE(6, 4); K4_STAGE(7, 4); K4_STAGE(8, 4); K4_STAGE(9, 4); STAMP(5); K4_STAGE(10, 2);
    K4_STEP(1, 2, 0, 0, 0);
  } else {
    K4_STEP(1, 4, 0, 0, 0);
  }
  STAMP(6);
  u32x4 o0, o1;
#pragma unroll
  for (int i = 0; i < 4; ++i) {
    o0[i] = pk2(acc[2 * i] + bflo(h0[i]), acc[2 * i + 1] + bfhi(h0[i]));
    o1[i] = pk2(acc[8 + 2 * i] + bflo(h1[i]), acc[9 + 2 * i] + bfhi(h1[i]));
  }
  if (MODE) {
    u16* dst = out + (size_t)tok * DM + ncol;
    *(u32x4*)dst = o0;
    *(u32x4*)(dst + 8) = o1;
  } else {
    *(u32x4*)hfrag = o0;
    *(u32x4*)(hfrag + 512) = o1;
    float v[16], s = 0.f;
#pragma unroll
    for (int i = 0; i < 4; ++i) { v[2 * i] = bflo(o0[i]); v[2 * i + 1] = bfhi(o0[i]); v[8 + 2 * i] = bflo(o1[i]); v[9 + 2 * i] = bfhi(o1[i]); }
#pragma unroll
    for (int i = 0; i < 16; ++i) s += v[i];
    const float m32 = swap32_sum(s) * (1.f / 32);
    float ss = 0.f;
#pragma unroll
    for (int i = 0; i < 16; ++i) ss += (v[i] - m32) * (v[i] - m32);
    f32x2 st; st[0] = m32; st[1] = swap32_sum(ss);
    if (h == 0) *(f32x2*)(ws + WS_ST + ((size_t)((tok >> 5) * 16 + nb * 2 + nt) * 32 + r32) * 8) = st;
  }
  STAMP(7); STAMP_FLUSH(MODE ? 4 : 2);
}

constexpr int K3_SLOT = 12288, K3_FLG = 13 * K3_SLOT, K3_SC = K3_FLG + 64, K3_LDS = K3_SC + 768;

__global__ __launch_bounds__(512) void k3_gateup(const u16* __restrict__ wd, char* __restrict__ ws) {
  extern __shared__ __attribute__((aligned(16))) char smem[];
  const lds_ptr lds = (lds_ptr)smem;
  const unsigned lds0 = (unsigned)(uintptr_t)smem;
  const int tid = threadIdx.x, lane = tid & 63;
  const int w = (int)rfl(tid >> 6);
  const int bid = blockIdx.x, jx = bid >> 3;
  if (jx >= 30) {
#define K3_NW 6
    const char* src = (const char*)wd + (size_t)(((jx & 1) * 8 + w) * 64 + lane) * 128;
#pragma unroll
    for (int i = 0; i < K3_NW; ++i) glds4(src + i * 131072, lds0 + w * 256);
    return;
  }
  const int rb = (bid & 7) * 2 + (jx & 1), cb = jx >> 1;
  const int at0 = cb * 6 > 80 ? 80 : cb * 6;
  STAMP_DECL STAMP(0);
  if (w >= 4) {
    const int lw = w - 4;
    const char* wsrc[3];
#pragma unroll
    for (int i = 0; i < 3; ++i) wsrc[i] = ws + WS_WGU + (size_t)(at0 + ((lw * 3 + i) >> 1)) * 32768 + ((lw * 3 + i) & 1) * 1024 + lane * 16;
#define K3_ISSUE(st) do { if ((st) >= 13) WAIT_CNT(lds + K3_FLG + 16, (st) - 12); \
    _Pragma("unroll") for (int i = 0; i < 3; ++i) glds16(wsrc[i] + (st) * 2048, lds0 + ((st) % 13) * K3_SLOT + (lw * 3 + i) * 1024); } while (0)
#define K3_PUB(n) LDS_VU32(lds + K3_FLG + lw * 4) = (n)
    K3_ISSUE(0); K3_ISSUE(1);
    RAWBAR();

    K3_ISSUE(2); WAIT_VM(6); K3_PUB(1);
    K3_ISSUE(3); WAIT_VM(6); K3_PUB(2);
    K3_ISSUE(4); WAIT_VM(6); K3_PUB(3);
    K3_ISSUE(5); WAIT_VM(6); K3_PUB(4);
    K3_ISSUE(6); WAIT_VM(6); K3_PUB(5);
    K3_ISSUE(7); WAIT_VM(6); K3_PUB(6);
    K3_ISSUE(8); WAIT_VM(6); K3_PUB(7);
    K3_ISSUE(9); WAIT_VM(6); K3_PUB(8);
    K3_ISSUE(10); WAIT_VM(6); K3_PUB(9);
    K3_ISSUE(11); WAIT_VM(6); K3_PUB(10);
    K3_ISSUE(12); WAIT_VM(6); K3_PUB(11);
    K3_ISSUE(13); WAIT_VM(6); K3_PUB(12);
    K3_ISSUE(14); WAIT_VM(6); K3_PUB(13);
    K3_ISSUE(15); WAIT_VM(6); K3_PUB(14);
    WAIT_VM(3); K3_PUB(15);
    WAIT_VM(0); K3_PUB(16);
    if (lw < 3) {
      const int i = lw * 64 + lane;
      const float* cp = (const float*)(ws + WS_CS) + ((i >> 4) & 1) * (8 * 1408) + ffeat(at0 + (i >> 5), i & 15);
      float c[8];
#pragma unroll
      for (int j = 0; j < 8; ++j) c[j] = cp[j * 1408];
      LDS_F32(lds + K3_SC + i * 4) = ((c[0] + c[1]) + (c[2] + c[3])) + ((c[4] + c[5]) + (c[6] + c[7]));
    }
    BAR();
    return;
  }
  const int r32 = lane & 31, h = lane >> 5;
  const char* bsrc = ws + WS_H + (size_t)(rb * 4 + w) * 32768 + lane * 16;
#define K3_BD 8
  bf16x8 fb_[K3_BD];
#define K3_LDB(kk) fb_[(kk) % K3_BD] = *(const bf16x8*)(bsrc + (kk) * 1024)
#pragma unroll
  for (int i = 0; i < 4; ++i) K3_LDB(i);
  if (tid < 8) LDS_VU32(lds + K3_FLG + tid * 4) = 0u;
  BAR();
#pragma unroll
  for (int i = 4; i < K3_BD; ++i) K3_LDB(i);
  f32x16 acc[6] = {};
  lds_ptr ap[3];
#pragma unroll
  for (int u = 0; u < 3; ++u) { ap[u] = lds + u * 5 * K3_SLOT + lane * 16; PIN(ap[u]); }
  bf16x8 fa[2][6];
  u32x4 rdy;
#define K3_A(kk, i) lds_b128(ap[(((kk) >> 1) % 13) / 5] + ((((kk) >> 1) % 13) % 5) * K3_SLOT + (i) * 2048 + ((kk) & 1) * 1024)
#define K3_STEP(kk) do { constexpr int n_ = (kk) + 2; \
    if (n_ < 32 && (n_ & 1) == 0 && __builtin_expect(rfl(min4(rdy)) < n_ / 2u + 1u, 0)) WAIT_CNT(lds + K3_FLG, n_ / 2 + 1); \
    _Pragma("unroll") for (int i = 0; i < 6; ++i) { \
      acc[i] = MFMA32(fa[(kk) & 1][i], fb_[(kk) % K3_BD], acc[i]); PINM(acc[i]); \
      if (i == 0 && (kk) > 0 && (kk) - 1 + K3_BD < 32) K3_LDB((kk) - 1 + K3_BD); \
      if (n_ < 32) fa[(kk) & 1][i] = K3_A(n_, i); \
      if ((n_ & 1) == 1 && i == 2) rdy = LDS_VU32X4(lds + K3_FLG); \
      SB(); } \
    if (((kk) & 1) == 1 && (kk) < 6) LDS_VU32(lds + K3_FLG + 16 + w * 4) = ((kk) + 1) / 2;   } while (0)
#define K3_STEP4(kk) do { K3_STEP(kk); K3_STEP((kk) + 1); K3_STEP((kk) + 2); K3_STEP((kk) + 3); } while (0)
  STAMP(1);
  WAIT_CNT(lds + K3_FLG, 1);
#pragma unroll
  for (int i = 0; i < 6; ++i) { fa[0][i] = K3_A(0, i); fa[1][i] = K3_A(1, i); }
  rdy = LDS_VU32X4(lds + K3_FLG);
  K3_STEP4(0); STAMP(2); K3_STEP4(4); STAMP(3);
  const f32x2 lp0 = *(const f32x2*)(ws + WS_ST + ((size_t)((rb * 4 + w) * 4 + h * 2) * 32 + r32) * 8), lp1 = *(const f32x2*)(ws + WS_ST + ((size_t)((rb * 4 + w) * 4 + h * 2 + 1) * 32 + r32) * 8);
  K3_STEP4(8); K3_STEP4(12); STAMP(4); K3_STEP4(16); K3_STEP4(20); K3_STEP4(24); STAMP(5);
  float mean = swap32_sum(lp0[0] + lp1[0]) * 0.25f;
  float rstd = ln_rstd(swap32_sum(lp0[1] + lp1[1] + 128.f * ((lp0[0] - mean) * (lp0[0] - mean) + (lp1[0] - mean) * (lp1[0] - mean))) * (1.f / DM));
  mean = rbf(mean);
  PIN(mean); PIN(rstd);
  BAR();
  K3_STEP4(28);
  STAMP(6);
  const float ke = -1.4426950408889634f * rstd, ir2 = __builtin_amdgcn_rcpf(rstd * rstd);
  const f32x2 nm2 = {-mean, -mean}, ke2 = {ke, ke}, ir22 = {ir2, ir2};
  f32x4 call[6][4];
#pragma unroll
  for (int i = 0; i < 6; ++i)
#pragma unroll
    for (int g = 0; g < 4; ++g) call[i][g] = LDS_F32X4(lds + K3_SC + i * 128 + (8 * g + 4 * h) * 4);
  SB();
#pragma unroll
  for (int i = 0; i < 6; ++i) {
    const f32x4 (&c4)[4] = call[i];
    f32x2 tg[4], tu[4], e[4];
#pragma unroll
    for (int j = 0; j < 4; ++j) {
      const f32x2 ag2 = {acc[i][2 * j], acc[i][2 * j + 1]}, au2 = {acc[i][8 + 2 * j], acc[i][9 + 2 * j]};
      const f32x2 cg2 = {c4[j >> 1][2 * (j & 1)], c4[j >> 1][2 * (j & 1) + 1]}, cu2 = {c4[2 + (j >> 1)][2 * (j & 1)], c4[2 + (j >> 1)][2 * (j & 1) + 1]};
      tg[j] = __builtin_elementwise_fma(nm2, cg2, ag2); tu[j] = __builtin_elementwise_fma(nm2, cu2, au2);
      e[j] = tg[j] * ke2;
    }
#pragma unroll
    for (int j = 0; j < 4; ++j) { e[j][0] = __builtin_amdgcn_exp2f(e[j][0]); e[j][1] = __builtin_amdgcn_exp2f(e[j][1]); tg[j] = tg[j] * tu[j]; }
#pragma unroll
    for (int j = 0; j < 4; ++j) e[j] = __builtin_elementwise_fma(e[j], ir22, ir22);
#pragma unroll
    for (int j = 0; j < 4; ++j) { e[j][0] = __builtin_amdgcn_rcpf(e[j][0]); e[j][1] = __builtin_amdgcn_rcpf(e[j][1]); }
    u32x4 pk;
#pragma unroll
    for (int j = 0; j < 4; ++j) { const f32x2 a2 = tg[j] * e[j]; pk[j] = pk2(a2[0], a2[1]); }
    *(u32x4*)(ws + WS_ACT + ((size_t)(rb * 4 + w) * NAT + at0 + i) * 1024 + lane * 16) = pk;
  }
  STAMP(7); STAMP_FLUSH(3);
}

extern "C" void kernel_launch(void* const* d_in, const int* in_sizes, int n_in, void* d_out, int out_size, void* d_ws, size_t ws_size,
                              hipStream_t stream) {
  const u16* x = (const u16*)d_in[0];
  const u16* wq = (const u16*)d_in[1];
  const u16* wk = (const u16*)d_in[2];
  const u16* wv = (const u16*)d_in[3];
  const u16* wo = (const u16*)d_in[4];
  const u16* wg = (const u16*)d_in[6];
  const u16* wu = (const u16*)d_in[7];
  const u16* wd = (const u16*)d_in[8];
  char* ws = (char*)d_ws;
  (void)hipFuncSetAttribute((const void*)k1_qkv, hipFuncAttributeMaxDynamicSharedMemorySize, K1_LDS);
  (void)hipFuncSetAttribute((const void*)k2_attn, hipFuncAttributeMaxDynamicSharedMemorySize, K2_LDS);
  (void)hipFuncSetAttribute((const void*)k3_gateup, hipFuncAttributeMaxDynamicSharedMemorySize, K3_LDS);
  (void)hipFuncSetAttribute((const void*)k64<1>, hipFuncAttributeMaxDynamicSharedMemorySize, K4_LDS);
  k1_qkv<<<256, 640, K1_LDS, stream>>>(x, wq, wk, wv, wo, wg, wu, ws);
  k2_attn<<<256, 512, K2_LDS, stream>>>(x, wd, ws);
  k3_gateup<<<256, 512, K3_LDS, stream>>>(wd, ws);
  k64<1><<<256, 512, K4_LDS, stream>>>(wd, x, ws, (u16*)d_out);
}
```

```cpp
#include <hip/hip_runtime.h>
#include <hip/hip_bf16.h>
#include <stdint.h>

typedef __attribute__((ext_vector_type(8))) short bf16x8;
typedef __attribute__((ext_vector_type(4))) short s16x4;
typedef __attribute__((ext_vector_type(2))) __bf16 bf16x2;
typedef __attribute__((ext_vector_type(4))) unsigned u32x4;
typedef __attribute__((ext_vector_type(2))) unsigned u32x2;
typedef __attribute__((ext_vector_type(16))) float f32x16;
typedef __attribute__((ext_vector_type(4))) float f32x4;
typedef __attribute__((ext_vector_type(2))) float f32x2;
typedef __attribute__((address_space(3))) char* lds_ptr;
typedef unsigned short u16;

#define DI __device__ __forceinline__
#define MFMA32(a, b, c) __builtin_amdgcn_mfma_f32_32x32x16_bf16((a), (b), (c), 0, 0, 0)
#define MFMA16(a, b, c) __builtin_amdgcn_mfma_f32_16x16x32_bf16((a), (b), (c), 0, 0, 0)
#define WAIT_VM_BAR(N) asm volatile("s_waitcnt vmcnt(" #N ") lgkmcnt(0)\n\ts_barrier" ::: "memory")
#define BAR() asm volatile("s_waitcnt lgkmcnt(0)\n\ts_barrier" ::: "memory")

constexpr int SEQ = 2048, DM = 512, FF = 1376, NAT = 86;
constexpr float QSCALE = 0.25f * 1.4426950408889634f;
constexpr float LN_EPS = 1e-5f;

constexpr size_t WS_Q = 0;
constexpr size_t WS_K = WS_Q + (size_t)SEQ * DM * 2;
constexpr size_t WS_V = WS_K + 65536;
constexpr size_t WS_KMAX = WS_V + 65536;
constexpr size_t WS_WO = WS_KMAX + 4096;
constexpr size_t WS_H = WS_WO + (size_t)DM * DM * 2;
constexpr size_t WS_ST = WS_H + (size_t)SEQ * DM * 2;
constexpr size_t WS_CS = WS_ST + (size_t)SEQ * 16 * 8;
constexpr size_t WS_WGU = WS_CS + 2 * 8 * 1408 * 4;
constexpr size_t WS_ACT = WS_WGU + (size_t)NAT * 32768;

#define STAMP_DECL
#define STAMP(i) do { } while (0)
#define STAMP_FLUSH(kid) do { } while (0)
#define ACC_BEGIN() do { } while (0)
#define ACC_END(i) do { } while (0)
DI void glds16(const void* g, unsigned lds_base) {
  unsigned sv;
  asm volatile("s_mov_b32 %0, m0\n\ts_mov_b32 m0, %2\n\ts_nop 0\n\tglobal_load_lds_dwordx4 %1, off\n\ts_mov_b32 m0, %0"
               : "=&s"(sv) : "v"(g), "s"(lds_base) : "memory");
}
DI void glds4(const void* g, unsigned lds_base) {
  unsigned sv;
  asm volatile("s_mov_b32 %0, m0\n\ts_mov_b32 m0, %2\n\ts_nop 0\n\tglobal_load_lds_dword %1, off\n\ts_mov_b32 m0, %0"
               : "=&s"(sv) : "v"(g), "s"(lds_base) : "memory");
}
#define CODE_ADDR(sym) ({ unsigned lo_, hi_; asm volatile("s_getpc_b64 s[40:41]\n\ts_add_u32 %0, s40, " #sym "@rel32@lo+4\n\ts_addc_u32 %1, s41, " #sym "@rel32@hi+12" \
    : "=s"(lo_), "=s"(hi_) :: "s40", "s41", "scc"); (const char*)(((unsigned long long)hi_ << 32) | lo_); })
DI unsigned rfl(unsigned v) { return (unsigned)__builtin_amdgcn_readfirstlane((int)v); }
DI unsigned pk2(float lo, float hi) {
  bf16x2 v; v[0] = (__bf16)lo; v[1] = (__bf16)hi;
  return __builtin_bit_cast(unsigned, v);
}
DI int ffeat(int at, int rho) { return at < 84 ? 64 * (at >> 2) + 8 * (rho & 7) + 2 * (at & 3) + (rho >> 3) : 1344 + 8 * (rho & 3) + 4 * (at - 84) + (rho >> 2); }
DI void st16_wt(void* p, u32x4 v) { asm volatile("global_store_dwordx4 %0, %1, off sc1\n\ts_nop 1" :: "v"(p), "v"(v) : "memory"); }
DI float rbf(float v) { return (float)(__bf16)v; }
DI float ln_rstd(float var) { return __builtin_amdgcn_rcpf(rbf(__builtin_amdgcn_sqrtf(rbf(rbf(var) + LN_EPS)))); }
DI float bflo(unsigned u) { return __uint_as_float(u << 16); }
DI float bfhi(unsigned u) { return __uint_as_float(u & 0xffff0000u); }
DI float dot2(unsigned a, unsigned b, float c) {
  return __builtin_amdgcn_fdot2_f32_bf16(__builtin_bit_cast(bf16x2, a), __builtin_bit_cast(bf16x2, b), c, false);
}
DI bf16x8 lds_b128(lds_ptr p) { return *(const __attribute__((address_space(3))) bf16x8*)p; }
DI s16x4 lds_tr(lds_ptr p) {
  typedef short v4i16_t __attribute__((ext_vector_type(4)));
  return __builtin_bit_cast(s16x4, __builtin_amdgcn_ds_read_tr16_b64_v4i16((__attribute__((address_space(3))) v4i16_t*)p));
}
DI float swap32_sum(float v) {
  auto r = __builtin_amdgcn_permlane32_swap(__float_as_uint(v), __float_as_uint(v), false, false);
  return __uint_as_float(r[0]) + __uint_as_float(r[1]);
}
DI float swap16_sum(float v) {
  auto r = __builtin_amdgcn_permlane16_swap(__float_as_uint(v), __float_as_uint(v), false, false);
  return __uint_as_float(r[0]) + __uint_as_float(r[1]);
}
DI float swap32_max(float v) {
  auto r = __builtin_amdgcn_permlane32_swap(__float_as_uint(v), __float_as_uint(v), false, false);
  return fmaxf(__uint_as_float(r[0]), __uint_as_float(r[1]));
}
template <int CTRL, int RM>
DI float dpp(float old, float v) { return __uint_as_float((unsigned)__builtin_amdgcn_update_dpp((int)__float_as_uint(old), (int)__float_as_uint(v), CTRL, RM, 0xf, false)); }
DI float wave_sum(float v) {
  v += dpp<0x111, 0xf>(0.f, v); v += dpp<0x112, 0xf>(0.f, v); v += dpp<0x114, 0xf>(0.f, v); v += dpp<0x118, 0xf>(0.f, v);
  v += dpp<0x142, 0xa>(0.f, v); v += dpp<0x143, 0xc>(0.f, v);
  return __uint_as_float((unsigned)__builtin_amdgcn_readlane((int)__float_as_uint(v), 63));
}
DI float wave_max(float v) {
  v = fmaxf(v, dpp<0x111, 0xf>(0.f, v)); v = fmaxf(v, dpp<0x112, 0xf>(0.f, v)); v = fmaxf(v, dpp<0x114, 0xf>(0.f, v)); v = fmaxf(v, dpp<0x118, 0xf>(0.f, v));
  v = fmaxf(v, dpp<0x142, 0xa>(0.f, v)); v = fmaxf(v, dpp<0x143, 0xc>(0.f, v));
  return __uint_as_float((unsigned)__builtin_amdgcn_readlane((int)__float_as_uint(v), 63));
}
DI float quad_sum(float v) { v += dpp<0xb1, 0xf>(0.f, v); v += dpp<0x4e, 0xf>(0.f, v); return v; }

#define SB() __builtin_amdgcn_sched_barrier(0)
#define PIN(x) asm volatile("" : "+v"(x))
#define PINM(x) asm volatile("" : "+v"(x) :: "memory")
#define LDS_F32X4(p) (*(__attribute__((address_space(3))) f32x4*)(p))
#define LDS_F32X2(p) (*(__attribute__((address_space(3))) f32x2*)(p))
#define LDS_F32(p) (*(__attribute__((address_space(3))) float*)(p))
#define LDS_U32X2(p) (*(__attribute__((address_space(3))) u32x2*)(p))
#define LDS_U32X4(p) (*(__attribute__((address_space(3))) u32x4*)(p))
#define LDS_VU32(p) (*(volatile __attribute__((address_space(3))) unsigned*)(p))
#define WAIT_VM(N) asm volatile("s_waitcnt vmcnt(" #N ")" ::: "memory")
#define RAWBAR() asm volatile("s_barrier" ::: "memory")
#define LGKM0() asm volatile("s_waitcnt lgkmcnt(0)" ::: "memory")
DI bf16x8 cat8(s16x4 a, s16x4 b) { return __builtin_shufflevector(a, b, 0, 1, 2, 3, 4, 5, 6, 7); }
constexpr unsigned ONES = 0x3f803f80u;
#define LDS_VU32X4(p) (*(volatile __attribute__((address_space(3))) u32x4*)(p))
DI unsigned min4(u32x4 v) { return min(min(v[0], v[1]), min(v[2], v[3])); }
#define WAIT_CNT(p, n) do { while (rfl(min4(LDS_VU32X4(p))) < (unsigned)(n)) __builtin_amdgcn_s_sleep(1); asm volatile("" ::: "memory"); } while (0)

constexpr int K1_X = 0, K1_W = 65536, K1_KV = 131072, K1_SC = 147456, K1_FLG = K1_SC + 2048 + 8192, K1_JUNK = K1_FLG + 32, K1_LDS = K1_JUNK + 256;

extern "C" __global__ __launch_bounds__(640) void k1_qkv(const u16* __restrict__ x, const u16* __restrict__ wq, const u16* __restrict__ wk,
                                              const u16* __restrict__ wv, const u16* __restrict__ wo, const u16* __restrict__ wg,
                                              const u16* __restrict__ wu, char* __restrict__ ws) {
  extern __shared__ __attribute__((aligned(16))) char smem[];
  const lds_ptr lds = (lds_ptr)smem;
  const unsigned lds0 = (unsigned)(uintptr_t)smem;
  const int tid = threadIdx.x, lane = tid & 63;
  const int w = (int)rfl(tid >> 6);
  if (w < 3) {
    const char *c0 = CODE_ADDR(k1_qkv), *c1 = CODE_ADDR(k2_attn);
    const char* src = c0 + tid * 128;
    glds4(src < c1 ? src : c0, lds0 + K1_JUNK);
  }
  const int bid = blockIdx.x;
  const int tb = (bid & 7) * 4 + ((bid >> 3) & 3), fb = bid >> 5;
  const bool kvwg = fb < 2;
  STAMP_DECL STAMP(0);
  if (w >= 8) {
    RAWBAR();
    const int job = (w - 8) * 256 + (255 - bid);
    if (w == 9 && bid < 64) {
      const char *c0 = CODE_ADDR(k2_attn), *c1 = CODE_ADDR(k_end_marker);
      const char* src = c0 + ((bid >> 3) * 64 + lane) * 128;
      glds4(src < c1 ? src : c0, lds0 + K1_JUNK);
    }
    if (job < 416) {
        const bool isw = job < 64;
        const int jg = job - 64, mat = jg >= 176, jj = jg - mat * 176, ksl = jj & 7, cg = jj >> 3;
        const int col = cg * 64 + (lane & 7) * 8, kc = ksl * 8 + (lane >> 3);
        const u16* src = isw ? wo + (size_t)((job >> 3) * 64 + (job & 1) * 32 + 8 * (lane >> 4)) * DM + ((job >> 1) & 3) * 128 + (lane & 15) * 8
                             : (mat ? wu : wg) + (size_t)kc * 8 * FF + (col < FF ? col : 0);
        const int ld = isw ? DM : FF;
#define K1_SJ 2
        WAIT_CNT(lds + K1_FLG, K1_SJ);
        u32x4 R[8];
#pragma unroll
        for (int j = 0; j < 8; ++j) R[j] = *(const u32x4*)(src + (size_t)j * ld);
        char* dst = isw ? ws + WS_WO + (size_t)job * 8192 + lane * 16
                        : ws + WS_WGU + ((size_t)(cg * 128 + (kc >> 1)) * 64 + (kc & 1) * 32 + mat * 16 + (lane & 7)) * 16;
        float c[8];
#pragma unroll
        for (int m = 0; m < 4; ++m) {
          u32x4 lo, hi;
#pragma unroll
          for (int i = 0; i < 4; ++i) { lo[i] = __builtin_amdgcn_perm(R[2 * i + 1][m], R[2 * i][m], 0x05040100u); hi[i] = __builtin_amdgcn_perm(R[2 * i + 1][m], R[2 * i][m], 0x07060302u); }
          const int o0 = isw ? 2 * m * 1024 : cg < 21 ? m * 32768 : (m >> 1) * 32768 + (m & 1) * 128, o1 = o0 + (isw ? 1024 : cg < 21 ? 128 : 64);
          if (isw || col < FF) { st16_wt(dst + o0, lo); st16_wt(dst + o1, hi); }
          c[2 * m] = dot2(lo[3], ONES, dot2(lo[2], ONES, dot2(lo[1], ONES, dot2(lo[0], ONES, 0.f))));
          c[2 * m + 1] = dot2(hi[3], ONES, dot2(hi[2], ONES, dot2(hi[1], ONES, dot2(hi[0], ONES, 0.f))));
        }
        if (!isw) {
#pragma unroll
          for (int i = 0; i < 8; ++i) { c[i] += dpp<0x128, 0xf>(0.f, c[i]); c[i] = swap32_sum(swap16_sum(c[i])); }
          if (lane < 8) {
            float* cd = (float*)(ws + WS_CS) + (mat * 8 + ksl) * 1408 + cg * 64 + lane * 8;
            *(f32x4*)cd = f32x4{c[0], c[1], c[2], c[3]};
            *(f32x4*)(cd + 4) = f32x4{c[4], c[5], c[6], c[7]};
          }
        }
    }
    return;
  }
  if (w >= 4) {
    const int lw = w - 4;
    const u16* xs = x + (size_t)(tb * 64 + lw * 16 + (lane >> 4)) * DM;
    const int xc = (lane & 15) ^ (lane >> 4);
    const int rr = lane >> 3, pos = lane & 7;
    const u16* wsrc = wq + (size_t)(lw * 32 + rr) * DM + fb * 64 + ((pos ^ (((rr >> 1) & 1) << 2)) * 8);
    const u16* kvs = (fb ? wv : wk) + lw * 512 + lane * 8;
#define K1_ISSUE(st, KV) do { _Pragma("unroll") for (int i = 0; i < 4; ++i) { \
      glds16(xs + (size_t)i * 4 * DM + (st) * 128 + ((xc ^ (4 * i)) * 8), lds0 + K1_X + (st) * 16384 + (lw * 4 + i) * 1024); \
      glds16(wsrc + (size_t)((st) * 128 + i * 8) * DM, lds0 + K1_W + (st) * 16384 + (lw * 4 + i) * 1024); } \
    if (KV) glds16(kvs + (st) * 2048, lds0 + K1_KV + (st) * 4096 + lw * 1024); } while (0)
#define K1_PUB(n) LDS_VU32(lds + K1_FLG + lw * 4) = (n)
    if (kvwg) {
      K1_ISSUE(0, 1); RAWBAR(); K1_ISSUE(1, 1);
      WAIT_VM(9); K1_PUB(1); K1_ISSUE(2, 1); WAIT_VM(9); K1_PUB(2); K1_ISSUE(3, 1); WAIT_VM(9); K1_PUB(3);
    } else {
      K1_ISSUE(0, 0); RAWBAR(); K1_ISSUE(1, 0);
      WAIT_VM(8); K1_PUB(1); K1_ISSUE(2, 0); WAIT_VM(8); K1_PUB(2); K1_ISSUE(3, 0); WAIT_VM(8); K1_PUB(3);
    }
    WAIT_VM(0); K1_PUB(4);
    return;
  }
  if (tid < 8) LDS_VU32(lds + K1_FLG + tid * 4) = 0u;
  BAR();
  const int r32 = lane & 31, h = lane >> 5;
  const int blk = (lane >> 4) & 1, q = (lane & 15) >> 2, p = lane & 3;
  const int tt = w & 1, ft = w >> 1;
  const int tok = tt * 32 + r32;
  lds_ptr xb[8];
#pragma unroll
  for (int i = 0; i < 8; ++i) xb[i] = lds + K1_X + tok * 256 + (((2 * i + h) ^ (tok & 15)) * 16);
  const lds_ptr wb = lds + K1_W + (8 * h + q) * 128 + (((ft * 4 + blk * 2 + (p >> 1)) ^ ((q >> 1) << 2)) * 16) + 8 * (p & 1);
  const lds_ptr kb = lds + K1_KV + (8 * h + q) * 32 + 8 * p;
  const float piv = __uint_as_float((unsigned)x[(size_t)(tb * 64 + tok) * DM] << 16);
  f32x16 acc = {}, acc2 = {};
  float cs = 0.f, cs2 = 0.f, sx = 0.f, sxx = 0.f, sxb = 0.f, sxxb = 0.f;
  s16x4 a[2][4][2]; bf16x8 b[2][4];
#define K1_STEP(bu, MM, LD, s0, sc0) do { _Pragma("unroll") for (int i = 0; i < 4; ++i) { const int s = (s0) + i; \
      if (MM) { acc = MFMA32(cat8(a[bu][i][0], a[bu][i][1]), b[bu][i], acc); PINM(acc); } \
      if (LD) { a[1 - (bu)][i][0] = lds_tr(wb + s * 2048); a[1 - (bu)][i][1] = lds_tr(wb + s * 2048 + 512); b[1 - (bu)][i] = lds_b128(xb[s & 7] + (s >> 3) * 16384); } SB(); } \
    if (MM) { \
      if (ft == (bu)) { \
        if (kvwg) { bf16x8 ka[4]; \
          _Pragma("unroll") for (int i = 0; i < 4; ++i) ka[i] = cat8(lds_tr(kb + ((sc0) + i) * 512), lds_tr(kb + ((sc0) + i) * 512 + 128)); \
          if (fb == 0) { _Pragma("unroll") for (int i = 0; i < 4; ++i) acc2 = MFMA32(ka[i], b[bu][i], acc2); } \
          else { _Pragma("unroll") for (int i = 0; i < 4; ++i) acc2 = MFMA32(b[bu][i], ka[i], acc2); } \
          PINM(acc2); SB(); \
          _Pragma("unroll") for (int i = 0; i < 4; ++i) { const u32x4 u = __builtin_bit_cast(u32x4, ka[i]); \
            cs2 = dot2(u[0], ONES, cs2); cs2 = dot2(u[1], ONES, cs2); cs2 = dot2(u[2], ONES, cs2); cs2 = dot2(u[3], ONES, cs2); } PIN(cs2); } \
        _Pragma("unroll") for (int i = 0; i < 4; ++i) { const u32x4 u = __builtin_bit_cast(u32x4, b[bu][i]); \
          sx = dot2(u[0], ONES, sx); sxx = dot2(u[0], u[0], sxx); sxb = dot2(u[1], ONES, sxb); sxxb = dot2(u[1], u[1], sxxb); \
          sx = dot2(u[2], ONES, sx); sxx = dot2(u[2], u[2], sxx); sxb = dot2(u[3], ONES, sxb); sxxb = dot2(u[3], u[3], sxxb); } \
        PIN(sx); PIN(sxx); PIN(sxb); PIN(sxxb); } \
      if (tt == (bu)) { _Pragma("unroll") for (int i = 0; i < 4; ++i) { const u32x4 u = __builtin_bit_cast(u32x4, cat8(a[bu][i][0], a[bu][i][1])); \
          cs = dot2(u[0], ONES, cs); cs = dot2(u[1], ONES, cs); cs = dot2(u[2], ONES, cs); cs = dot2(u[3], ONES, cs); } PIN(cs); } \
      SB(); } } while (0)
  STAMP(1);
#pragma unroll
  for (int st = 0; st < 4; ++st) { WAIT_CNT(lds + K1_FLG, st + 1); K1_STEP(1, st > 0, true, st * 8, st * 8 - 4); K1_STEP(0, true, true, st * 8 + 4, st * 8); }
  K1_STEP(1, true, false, 0, 28);
  STAMP(2);
  const lds_ptr sc = lds + K1_SC;
  {
    sx += sxb; sxx += sxxb;
    f32x2 d; d[0] = swap32_sum(sx - 128.f * piv); d[1] = swap32_sum(__builtin_fmaf(-2.f * piv, sx, sxx) + 128.f * piv * piv);
    cs = swap32_sum(cs); cs2 = swap32_sum(cs2);
    if (h == 0) { LDS_F32(sc + (ft * 2 + tt) * 128 + r32 * 4) = cs; LDS_F32X2(sc + 512 + (tt * 2 + ft) * 256 + r32 * 8) = d; LDS_F32(sc + 1536 + w * 128 + r32 * 4) = cs2; }
    if (kvwg && ft == 1) {
#pragma unroll
      for (int g = 0; g < 4; ++g) { f32x4 t; t[0] = acc2[4 * g]; t[1] = acc2[4 * g + 1]; t[2] = acc2[4 * g + 2]; t[3] = acc2[4 * g + 3]; LDS_F32X4(sc + 2048 + tt * 4096 + g * 1024 + lane * 16) = t; }
    }
  }
  asm volatile("" ::: "memory");
  LDS_VU32(lds + K1_FLG + 16 + w * 4) = 1u;
  WAIT_CNT(lds + K1_FLG + 16, 1);
  float mean, rstd;
  {
    const f32x2 d = LDS_F32X2(sc + 512 + tt * 512 + r32 * 8) + LDS_F32X2(sc + 512 + tt * 512 + 256 + r32 * 8);
    const float dmn = d[0] * (1.f / DM);
    mean = rbf(piv + dmn); rstd = ln_rstd(fmaxf(d[1] * (1.f / DM) - dmn * dmn, 0.f));
  }
  {
    const float scl = rstd * QSCALE;
    float o[16];
#pragma unroll
    for (int g = 0; g < 4; ++g) {
      const f32x4 c4 = LDS_F32X4(sc + ft * 256 + (8 * g + 4 * h) * 4) + LDS_F32X4(sc + ft * 256 + 128 + (8 * g + 4 * h) * 4);
#pragma unroll
      for (int j = 0; j < 4; ++j) o[4 * g + j] = (acc[4 * g + j] - mean * c4[j]) * scl;
    }
    u32x4 p0, p1;
#pragma unroll
    for (int i = 0; i < 4; ++i) { p0[i] = pk2(o[2 * i], o[2 * i + 1]); p1[i] = pk2(o[8 + 2 * i], o[9 + 2 * i]); }
    char* dst = ws + WS_Q + (size_t)(tb * 64 + tok) * 1024 + (fb * 4 + ft * 2) * 32 + h * 16;
    *(u32x4*)dst = p0;
    *(u32x4*)(dst + 32) = p1;
  }
  if (kvwg && ft == 0) {
    const int gt = tb * 2 + tt;
#pragma unroll
    for (int g = 0; g < 4; ++g) { const f32x4 t = LDS_F32X4(sc + 2048 + tt * 4096 + g * 1024 + lane * 16);
#pragma unroll
      for (int j = 0; j < 4; ++j) acc2[4 * g + j] += t[j]; }
    if (fb == 0) {
      float o[8];
#pragma unroll
      for (int g = 0; g < 2; ++g) {
        const f32x4 c4 = LDS_F32X4(sc + 1536 + tt * 128 + (8 * g + 4 * h) * 4) + LDS_F32X4(sc + 1536 + (tt + 2) * 128 + (8 * g + 4 * h) * 4);
#pragma unroll
        for (int j = 0; j < 4; ++j) o[4 * g + j] = (acc2[4 * g + j] - mean * c4[j]) * rstd;
      }
      u32x4 p0;
#pragma unroll
      for (int i = 0; i < 4; ++i) p0[i] = pk2(o[2 * i], o[2 * i + 1]);
      *(u32x4*)(ws + WS_K + (size_t)gt * 1024 + h * 512 + r32 * 16) = p0;
      float n2 = 0.f;
#pragma unroll
      for (int i = 0; i < 4; ++i) n2 = dot2(p0[i], p0[i], n2);
      n2 = wave_max(swap32_sum(n2));
      if (lane == 0) *(float*)(ws + WS_KMAX + gt * 4) = n2;
    } else {
      const float cv = LDS_F32(sc + 1536 + tt * 128 + r32 * 4) + LDS_F32(sc + 1536 + (tt + 2) * 128 + r32 * 4);
      const lds_ptr sp = sc + 2048 + tt * 4096;
      LGKM0();
      if (h == 0) { f32x2 st; st[0] = mean; st[1] = rstd; LDS_F32X2(sp + r32 * 8) = st; }
      float o[16];
#pragma unroll
      for (int g = 0; g < 4; ++g) {
        const f32x4 s01 = LDS_F32X4(sp + (8 * g + 4 * h) * 8), s23 = LDS_F32X4(sp + (8 * g + 4 * h) * 8 + 16);
        o[4 * g + 0] = (acc2[4 * g + 0] - s01[0] * cv) * s01[1];
        o[4 * g + 1] = (acc2[4 * g + 1] - s01[2] * cv) * s01[3];
        o[4 * g + 2] = (acc2[4 * g + 2] - s23[0] * cv) * s23[1];
        o[4 * g + 3] = (acc2[4 * g + 3] - s23[2] * cv) * s23[3];
      }
      u32x4 p0, p1;
#pragma unroll
      for (int i = 0; i < 4; ++i) { p0[i] = pk2(o[2 * i], o[2 * i + 1]); p1[i] = pk2(o[8 + 2 * i], o[9 + 2 * i]); }
      if (r32 < 16) {
        char* dst = ws + WS_V + (size_t)gt * 1024 + h * 256 + r32 * 16;
        *(u32x4*)dst = p0;
        *(u32x4*)(dst + 512) = p1;
      }
    }
  }
  STAMP(3); STAMP_FLUSH(0);
}

constexpr int K2_PSZ = 2176;
constexpr int K2_RED = 0;
constexpr int K2_Q = 64 * K2_PSZ;
constexpr int K2_NM = K2_Q + 8192;
constexpr int K2_JUNK = K2_NM + 8192;
constexpr int K2_FLG = K2_JUNK + 256;
constexpr int K2_OB = K2_FLG + 512;
constexpr int K2_LDS = K2_OB + 2 * 1152;
constexpr int K2_MX = 0;

#define E_(r) "v_exp_f32 v" #r ", v" #r "\n\t"
#define C_(d, a, b) "v_cvt_pk_bf16_f32 v" #d ", v" #a ", v" #b "\n\t"
#define PVPREV_ "v_mfma_f32_32x32x16_bf16 v[200:215], %8, v[236:239], v[200:215]\n\t"
#define TILE_A1 E_(168) E_(169)
#define TILE_A2 E_(170) E_(171) E_(172) E_(173) "v_mfma_f32_32x32x16_bf16 v[184:199], %6, %7, v[216:231]\n\t" E_(174) E_(175) \
    C_(232, 168, 169) C_(233, 170, 171) C_(234, 172, 173) C_(235, 174, 175) E_(176) E_(177)
#define TILE_A3 E_(178) E_(179) E_(180) E_(181) E_(182) E_(183) C_(236, 176, 177) C_(237, 178, 179) C_(238, 180, 181) C_(239, 182, 183)
#define TILE_B1 E_(184) E_(185)
#define TILE_B2 E_(186) E_(187) E_(188) E_(189) "v_mfma_f32_32x32x16_bf16 v[168:183], %6, %7, v[216:231]\n\t" E_(190) E_(191) \
    C_(232, 184, 185) C_(233, 186, 187) C_(234, 188, 189) C_(235, 190, 191) E_(192) E_(193)
#define TILE_B3 E_(194) E_(195) E_(196) E_(197) E_(198) E_(199) C_(236, 192, 193) C_(237, 194, 195) C_(238, 196, 197) C_(239, 198, 199)
#define PV0_ "v_mfma_f32_32x32x16_bf16 v[200:215], %9, v[232:235], v[200:215]\n\t"
#define PV0Z_ "v_mfma_f32_32x32x16_bf16 v[200:215], %9, v[232:235], 0\n\t"
#define K2_ASM(TXT, KN, QN, VP, V0) asm volatile(TXT : "+{v[168:183]}"(sa), "+{v[184:199]}"(sb), "+{v[200:215]}"(o), "+{v[232:235]}"(p0), "+{v[236:239]}"(p1) \
    : "{v[216:231]}"(negm), "v"(KN), "v"(QN), "v"(VP), "v"(V0))
#define K2_ASM_NOC(TXT, V0) asm volatile(TXT : "+{v[168:183]}"(sa), "+{v[184:199]}"(sb), "+{v[200:215]}"(o), "+{v[232:235]}"(p0), "+{v[236:239]}"(p1) \
    : "v"(V0), "v"(V0), "v"(V0), "v"(V0), "v"(V0))

extern "C" __global__ __launch_bounds__(512) void k2_attn(const u16* __restrict__ x, const u16* __restrict__ wd, char* __restrict__ ws) {
  extern __shared__ __attribute__((aligned(16))) char smem[];
  const lds_ptr lds = (lds_ptr)smem;
  const unsigned lds0 = (unsigned)(uintptr_t)smem;
  const int tid = threadIdx.x, lane = tid & 63;
  const int w = (int)rfl(tid >> 6);
  const int g8 = (blockIdx.x & 7) * 32 + (blockIdx.x >> 3);
  const int r32 = lane & 31, h = lane >> 5;
  STAMP_DECL STAMP(0);
  const u32x4 qown = *(const u32x4*)(ws + WS_Q + (size_t)(g8 * 8 + (r32 & 7)) * 1024 + (w * 4 + (r32 >> 3)) * 32 + h * 16);
  const float km2l = *(const float*)(ws + WS_KMAX + lane * 4);
  bf16x8 kf[8], vf[8][2];
  u32x4 vr[8];
  const char* ksrc = ws + WS_K + (size_t)w * 8192 + lane * 16;
  const char* vsrc = ws + WS_V + (size_t)w * 8192 + (r32 >> 4) * 512 + h * 256 + (lane & 15) * 16;
#define K2_LOADKV(j) do { kf[j] = *(const bf16x8*)(ksrc + (j) * 1024); vr[j] = *(const u32x4*)(vsrc + (j) * 1024); } while (0)
#define K2_SPLITV(j) do { u32x4 a_, b_; _Pragma("unroll") for (int i_ = 0; i_ < 4; ++i_) { \
    const auto r_ = __builtin_amdgcn_permlane16_swap(vr[j][i_], ONES, false, false); a_[i_] = r_[0]; b_[i_] = r_[1]; } \
    vf[j][0] = __builtin_bit_cast(bf16x8, a_); vf[j][1] = __builtin_bit_cast(bf16x8, b_); } while (0)
  K2_LOADKV(0); K2_LOADKV(1);
  if (w < 4) __builtin_amdgcn_s_setprio(2); else __builtin_amdgcn_s_setprio(1);
  if (tid >= 8 && tid < 128) LDS_VU32(lds + K2_FLG + tid * 4) = 0u;
  {
    float n2 = 0.f;
#pragma unroll
    for (int i = 0; i < 4; ++i) n2 = dot2(qown[i], qown[i], n2);
    const float mref = sqrtf(swap32_sum(n2) * wave_max(km2l));
    const f32x4 nm = {-mref, -mref, -mref, -mref};
    LDS_U32X4(lds + K2_Q + w * 1024 + lane * 16) = qown;
    LDS_F32X4(lds + K2_NM + w * 1024 + lane * 16) = nm;
    LDS_VU32(lds + K2_FLG + w * 4) = __any(!(mref <= 40.f)) ? 1u : 0u;
  }
  STAMP(1);
  BAR();
  STAMP(2);
  K2_LOADKV(2); K2_LOADKV(3); K2_LOADKV(4); K2_LOADKV(5); K2_LOADKV(6); K2_LOADKV(7);
  const lds_ptr qp = lds + K2_Q + lane * 16;
  if (__any(LDS_VU32(lds + K2_FLG + (lane & 7) * 4) != 0u)) {
    const f32x16 zero = {};
    for (int g = 0; g < 8; ++g) {
      const bf16x8 qf = lds_b128(qp + g * 1024);
      float mx = -3.0e38f;
#pragma unroll
      for (int j = 0; j < 8; ++j) {
        const f32x16 s = MFMA32(kf[j], qf, zero);
#pragma unroll
        for (int i = 0; i < 16; ++i) mx = fmaxf(mx, s[i]);
      }
      mx = swap32_max(mx);
      if (h == 0) LDS_F32(lds + K2_MX + (g * 8 + w) * 128 + r32 * 4) = mx;
    }
    BAR();
    float mx = LDS_F32(lds + K2_MX + w * 1024 + r32 * 4);
#pragma unroll
    for (int i = 1; i < 8; ++i) mx = fmaxf(mx, LDS_F32(lds + K2_MX + w * 1024 + i * 128 + r32 * 4));
    const f32x4 nm = {-mx, -mx, -mx, -mx};
    LDS_F32X4(lds + K2_NM + w * 1024 + lane * 16) = nm;
    BAR();
  }
  f32x16 negm;
#define K2_LOAD_NEGM(g) do { const lds_ptr np_ = lds + K2_NM + (g) * 1024 + lane * 16; \
    _Pragma("unroll") for (int i_ = 0; i_ < 4; ++i_) { const f32x4 t_ = *(volatile __attribute__((address_space(3))) f32x4*)(np_); \
      negm[4 * i_] = t_[0]; negm[4 * i_ + 1] = t_[1]; negm[4 * i_ + 2] = t_[2]; negm[4 * i_ + 3] = t_[3]; } } while (0)
  K2_LOAD_NEGM(0);
  f32x16 o = {}, sa, sb = {};
  u32x4 p0 = {}, p1 = {};
  bf16x8 qf = lds_b128(qp);
  sa = MFMA32(kf[0], qf, negm);
  const lds_ptr redw = lds + K2_RED + w * K2_PSZ + lane * 16;
#define K2_WRITE_PARTIAL(g) do { const lds_ptr rp_ = redw + (g) * (8 * K2_PSZ); f32x4 t0_, t1_; \
    t0_[0] = o[0]; t0_[1] = o[1]; t0_[2] = o[2]; t0_[3] = o[3]; t1_[0] = o[4]; t1_[1] = o[5]; t1_[2] = o[6]; t1_[3] = o[7]; \
    LDS_F32X4(rp_) = t0_; LDS_F32X4(rp_ + 1024) = t1_; LDS_F32(rp_ + 2048 - lane * 16 + r32 * 4) = o[8]; \
    asm volatile("" ::: "memory"); LDS_VU32(lds + K2_FLG + 32 + ((g) * 8 + w) * 4) = 1u; } while (0)
#define VMWAIT0() __builtin_amdgcn_s_waitcnt(0x0F70)
#define SPL(j) do { if (FIRST_) { K2_SPLITV(j); asm volatile("s_nop 1" : "+v"(vf[j][0]), "+v"(vf[j][1])); } } while (0)
#define K2_ITER(g, FIRST) do { constexpr bool FIRST_ = FIRST; \
    SPL(0); \
    if (FIRST) K2_ASM(TILE_A1 TILE_A2 PV0Z_ TILE_A3, kf[1], qf, vf[0][0], vf[0][0]); \
    else { K2_ASM(TILE_A1 PVPREV_ TILE_A2, kf[1], qf, vf[7][1], vf[0][0]); K2_WRITE_PARTIAL((g) - 1); K2_ASM(PV0Z_ TILE_A3, kf[1], qf, vf[7][1], vf[0][0]); } \
    const bf16x8 qn = lds_b128(qp + (((g) + 1) & 7) * 1024); \
    SPL(1); K2_ASM(TILE_B1 PVPREV_ TILE_B2 PV0_ TILE_B3, kf[2], qf, vf[0][1], vf[1][0]); \
    SPL(2); K2_ASM(TILE_A1 PVPREV_ TILE_A2 PV0_ TILE_A3, kf[3], qf, vf[1][1], vf[2][0]); \
    SPL(3); K2_ASM(TILE_B1 PVPREV_ TILE_B2 PV0_ TILE_B3, kf[4], qf, vf[2][1], vf[3][0]); \
    SPL(4); K2_ASM(TILE_A1 PVPREV_ TILE_A2 PV0_ TILE_A3, kf[5], qf, vf[3][1], vf[4][0]); \
    SPL(5); K2_ASM(TILE_B1 PVPREV_ TILE_B2 PV0_ TILE_B3, kf[6], qf, vf[4][1], vf[5][0]); \
    SPL(6); K2_ASM(TILE_A1 PVPREV_ TILE_A2, kf[7], qf, vf[5][1], vf[6][0]); \
    K2_LOAD_NEGM(((g) + 1) & 7); \
    K2_ASM_NOC(PV0_ TILE_A3, vf[6][0]); \
    SPL(7); K2_ASM(TILE_B1 PVPREV_ TILE_B2 PV0_ TILE_B3, kf[0], qn, vf[6][1], vf[7][0]); \
    qf = qn; } while (0)

  STAMP(3);
  asm volatile("s_nop 15" : "+{v[168:183]}"(sa));
  K2_ITER(0, true);
  VMWAIT0();
  for (int g = 1; g < 8; ++g) {
    if (g <= 4) {
      const int wi = (blockIdx.x >> 3) * 8 + w;
      const char* src = g == 2 ? (const char*)ws + WS_WO + (wi * 16 + (lane & 15)) * 128 : (g == 1 ? (const char*)wd : ws + WS_WGU + (g - 3) * (NAT * 16384)) + (wi * 43 + (lane < 43 ? lane : 42)) * 128;
      glds4(src, lds0 + K2_JUNK);
    }
    if (g == 6 && w == 7 && blockIdx.x < 32) {
      const char *c0 = CODE_ADDR(k4_down), *c1 = CODE_ADDR(k_end_marker);
      const char* src = c0 + ((blockIdx.x >> 3) * 64 + lane) * 128;
      glds4(src < c1 ? src : c0, lds0 + K2_JUNK);
    }
    K2_ITER(g, false);
  }
  STAMP(4);
  K2_ASM("s_nop 1\n\t" PVPREV_ "s_nop 15", kf[0], qf, vf[7][1], vf[7][0]);
  K2_WRITE_PARTIAL(7);
  if (w >= 4) { STAMP(5); STAMP_FLUSH(1); return; }
  asm volatile("s_waitcnt vmcnt(0)" ::: "memory");
  VMWAIT0();
  const int g4 = lane >> 4, tok8 = lane & 7;
  const char* wsrc = ws + WS_WO + (size_t)w * 16384 + lane * 16;
  bf16x8 wr[5][8];
#define K2_WLOAD(hh) do { if ((hh) < 16) { _Pragma("unroll") for (int i_ = 0; i_ < 8; ++i_) wr[(hh) % 5][i_] = *(const bf16x8*)(wsrc + (size_t)((hh) >> 1) * 65536 + ((hh) & 1) * 8192 + i_ * 1024); } } while (0)
  f32x4 oacc[8];
  const float piv = __uint_as_float((unsigned)x[(size_t)(g8 * 8 + tok8) * DM + w * 128] << 16);
  {
    u32x4 xr[4];
#pragma unroll
    for (int r = 0; r < 4; ++r) xr[r] = *(const u32x4*)(x + (size_t)(g8 * 8 + tok8) * DM + w * 128 + g4 * 32 + r * 8);
    K2_WLOAD(0); K2_WLOAD(1); K2_WLOAD(2);
#pragma unroll
    for (int r = 0; r < 4; ++r)
#pragma unroll
      for (int i = 0; i < 4; ++i) { oacc[2 * i][r] = bflo(xr[r][i]); oacc[2 * i + 1][r] = bfhi(xr[r][i]); }
  }
#define K2_PROJ(g) do { \
    while (!__all(LDS_VU32(lds + K2_FLG + 32 + ((g) * 8 + tok8) * 4) != 0u)) __builtin_amdgcn_s_sleep(2); \
    asm volatile("" ::: "memory"); \
    { const lds_ptr rp_ = lds + K2_RED + (g) * (8 * K2_PSZ) + (w >> 1) * 1024 + lane * 16 + (w & 1) * 8, lp_ = lds + K2_RED + (g) * (8 * K2_PSZ) + 2048 + r32 * 4; \
      f32x2 t_[8]; float l_[8]; \
      _Pragma("unroll") for (int i_ = 0; i_ < 8; ++i_) { t_[i_] = LDS_F32X2(rp_ + i_ * K2_PSZ); l_[i_] = LDS_F32(lp_ + i_ * K2_PSZ); } \
      SB(); K2_WLOAD(2 * (g) + 3); SB(); \
      const f32x2 ts_ = ((t_[0] + t_[1]) + (t_[2] + t_[3])) + ((t_[4] + t_[5]) + (t_[6] + t_[7])); \
      const float inv_ = __builtin_amdgcn_rcpf(((l_[0] + l_[1]) + (l_[2] + l_[3])) + ((l_[4] + l_[5]) + (l_[6] + l_[7]))); \
      LDS_VU32(lds + K2_OB + ((g) & 1) * 1152 + (r32 & 7) * 144 + (r32 >> 3) * 32 + h * 8 + (w >> 1) * 16 + (w & 1) * 4) = pk2(ts_[0] * inv_, ts_[1] * inv_); \
      LDS_VU32(lds + K2_FLG + 288 + ((g) * 4 + w) * 4) = 1u; } \
    SB(); K2_WLOAD(2 * (g) + 4); SB(); \
    while (!__all(LDS_VU32(lds + K2_FLG + 288 + ((g) * 4 + (lane & 3)) * 4) != 0u)) __builtin_amdgcn_s_sleep(1); \
    asm volatile("" ::: "memory"); \
    const bf16x8 b0_ = lds_b128(lds + K2_OB + ((g) & 1) * 1152 + tok8 * 144 + g4 * 16), b1_ = lds_b128(lds + K2_OB + ((g) & 1) * 1152 + tok8 * 144 + 64 + g4 * 16); \
    _Pragma("unroll") for (int f_ = 0; f_ < 8; ++f_) oacc[f_] = MFMA16(wr[(2 * (g)) % 5][f_], b0_, oacc[f_]); \
    _Pragma("unroll") for (int f_ = 0; f_ < 8; ++f_) { oacc[f_] = MFMA16(wr[(2 * (g) + 1) % 5][f_], b1_, oacc[f_]); PINM(oacc[f_]); } } while (0)
  STAMP(5);
  K2_PROJ(0); K2_PROJ(1); K2_PROJ(2); K2_PROJ(3); K2_PROJ(4); K2_PROJ(5); K2_PROJ(6); K2_PROJ(7);
  STAMP(6);
  {
    const int tok = g8 * 8 + tok8;
    u32x4 hb[4];
#pragma unroll
    for (int r = 0; r < 4; ++r)
#pragma unroll
      for (int i = 0; i < 4; ++i) hb[r][i] = pk2(oacc[2 * i][r], oacc[2 * i + 1][r]);
    if ((lane & 8) == 0) {
#pragma unroll
      for (int r = 0; r < 4; ++r) *(u32x4*)(ws + WS_H + (size_t)(tok >> 5) * 32768 + (w * 16 + g4 * 4 + r) * 512 + (tok & 31) * 16) = hb[r];
    }
    float sx = 0.f, sxx = 0.f;
#pragma unroll
    for (int r = 0; r < 4; ++r)
#pragma unroll
      for (int i = 0; i < 4; ++i) { sx = dot2(hb[r][i], ONES, sx); sxx = dot2(hb[r][i], hb[r][i], sxx); }
    const float d1 = swap32_sum(swap16_sum(sx - 32.f * piv)), d2 = swap32_sum(swap16_sum(__builtin_fmaf(-2.f * piv, sx, sxx) + 32.f * piv * piv));
    f32x2 st; st[0] = piv + d1 * (1.f / 128); st[1] = fmaxf(d2 - d1 * d1 * (1.f / 128), 0.f);
    if (lane < 8) *(f32x2*)(ws + WS_ST + ((size_t)((tok >> 5) * 4 + w) * 32 + (tok & 31)) * 8) = st;
  }
  STAMP(7); STAMP_FLUSH(1);
}

constexpr int K4_SLOT = 32768, K4_LDS = 5 * K4_SLOT;

extern "C" __global__ __launch_bounds__(512) void k4_down(const u16* __restrict__ W, const u16* __restrict__ x, char* __restrict__ ws, u16* __restrict__ out) {
  constexpr int MODE = 1, NKS = NAT;
  extern __shared__ __attribute__((aligned(16))) char smem[];
  const lds_ptr lds = (lds_ptr)smem;
  const unsigned lds0 = (unsigned)(uintptr_t)smem;
  const int tid = threadIdx.x, lane = tid & 63;
  const int w = (int)rfl(tid >> 6);
  const int bid = blockIdx.x;
  const int tb = (bid & 7) * 4 + ((bid >> 3) & 3), nb = bid >> 5;
  const int r32 = lane & 31, h = lane >> 5;
  STAMP_DECL STAMP(0);
  if (w >= 4) {
    const int lw = w - 4;
    const int rr = lane >> 3, pos = lane & 7;
    const char* wcol = (const char*)W + nb * 128 + (pos ^ (((rr >> 1) & 1) << 2)) * 16;
    const char* wsrc = wcol + (size_t)(64 * (lw >> 1) + 8 * rr + 4 * (lw & 1)) * 1024;
    const char* wlast = wcol + (size_t)(1344 + 8 * (rr & 3) + (rr >> 2)) * 1024;
    const char* asrc = ws + WS_ACT + ((size_t)(tb * 2 + (lw >> 1)) * NKS + (lw & 1) * 4) * 1024 + lane * 16;
#define K4_ISSUE(st) do { const unsigned sl_ = lds0 + ((st) % 5) * K4_SLOT; _Pragma("unroll") for (int i = 0; i < 4; ++i) { \
    const bool oka_ = (st) < 10 || !((lw & 1) && i >= 2); \
    glds16(asrc + (oka_ ? (st) * 8192 + i * 1024 : 0), sl_ + 16384 + (lw * 4 + i) * 1024); \
    glds16(((st) < 10 || lw < 2) ? wsrc + (size_t)(st) * 131072 + i * 1024 : wlast + i * 2048, sl_ + (lw * 4 + i) * 1024); } } while (0)
    if (MODE == 0) {
      K4_ISSUE(0); K4_ISSUE(1);
      WAIT_VM(8); RAWBAR(); K4_ISSUE(2);
      WAIT_VM(8); RAWBAR(); K4_ISSUE(3);
      WAIT_VM(8); RAWBAR();
      WAIT_VM(0); RAWBAR();
    } else {
      K4_ISSUE(0); K4_ISSUE(1); K4_ISSUE(2);
      WAIT_VM(16); RAWBAR(); K4_ISSUE(3); K4_ISSUE(4);
      WAIT_VM(24); RAWBAR(); K4_ISSUE(5);
      WAIT_VM(24); RAWBAR(); K4_ISSUE(6);
      WAIT_VM(24); RAWBAR(); K4_ISSUE(7);
      WAIT_VM(24); RAWBAR(); K4_ISSUE(8);
      WAIT_VM(24); RAWBAR(); K4_ISSUE(9);
      WAIT_VM(24); RAWBAR(); K4_ISSUE(10);
      WAIT_VM(24); RAWBAR();
      WAIT_VM(16); RAWBAR();
      WAIT_VM(8); RAWBAR();
      WAIT_VM(0); RAWBAR();
    }
    STAMP(7); STAMP_FLUSH(MODE ? 4 : 2);
    return;
  }
  const int nt = w & 1, tt = w >> 1;
  const int tok = tb * 64 + tt * 32 + r32;
  const int ncol = nb * 64 + nt * 32 + h * 16;
  const int blk = (lane >> 4) & 1, q = (lane & 15) >> 2, p = lane & 3;
  char* hfrag = ws + WS_H + (size_t)(tok >> 5) * 32768 + (ncol >> 3) * 512 + r32 * 16;
  u32x4 h0, h1;
  if (MODE) { h0 = *(const u32x4*)(hfrag); h1 = *(const u32x4*)(hfrag + 512); }
  else { h0 = *(const u32x4*)(x + (size_t)tok * DM + ncol); h1 = *(const u32x4*)(x + (size_t)tok * DM + ncol + 8); }
  f32x16 acc = {};
  const int cbyte = nt * 64 + (16 * (p & 1) + 8 * blk + 4 * (p >> 1)) * 2;
  lds_ptr ap[3], bp[3];
#pragma unroll
  for (int u = 0; u < 3; ++u) { ap[u] = lds + u * 2 * K4_SLOT + (4 * h + q) * 128 + (cbyte ^ ((q >> 1) << 6)); bp[u] = lds + u * 2 * K4_SLOT + 16384 + tt * 8192 + lane * 16; }
  s16x4 fa[2][4][2]; bf16x8 fb_[2][4];
#define K4_U(st) (((st) % 5) >> 1)
#define K4_O(st) ((((st) % 5) & 1) * K4_SLOT)
#define K4_LD1(bu, st, k0, k) do { fa[bu][k][0] = lds_tr(ap[K4_U(st)] + K4_O(st) + ((k0) + (k)) * 2048); fa[bu][k][1] = lds_tr(ap[K4_U(st)] + K4_O(st) + ((k0) + (k)) * 2048 + 1024); \
    fb_[bu][k] = lds_b128(bp[K4_U(st)] + K4_O(st) + ((k0) + (k)) * 1024); } while (0)
#define K4_STEP(bu, NM, NL, st, k0) do { _Pragma("unroll") for (int k = 0; k < 4; ++k) { \
    if (k < (NM)) { acc = MFMA32(cat8(fa[bu][k][0], fa[bu][k][1]), fb_[bu][k], acc); PINM(acc); } \
    if (k < (NL)) K4_LD1(1 - (bu), st, k0, k); SB(); } } while (0)
#define K4_STAGE(st, NK2) do { BAR(); K4_STEP(1, (st) > 0 ? 4 : 0, 4, st, 0); K4_STEP(0, 4, NK2, st, 4); } while (0)
  STAMP(1);
  K4_STAGE(0, 4); STAMP(2); K4_STAGE(1, 4); STAMP(3); K4_STAGE(2, 4); K4_STAGE(3, 4);
  if (MODE) {
    K4_STAGE(4, 4); STAMP(4); K4_STAGE(5, 4); K4_STAGE(6, 4); K4_STAGE(7, 4); K4_STAGE(8, 4); K4_STAGE(9, 4); STAMP(5); K4_STAGE(10, 2);
    K4_STEP(1, 2, 0, 0, 0);
  } else {
    K4_STEP(1, 4, 0, 0, 0);
  }
  STAMP(6);
  u32x4 o0, o1;
#pragma unroll
  for (int i = 0; i < 4; ++i) {
    o0[i] = pk2(acc[2 * i] + bflo(h0[i]), acc[2 * i + 1] + bfhi(h0[i]));
    o1[i] = pk2(acc[8 + 2 * i] + bflo(h1[i]), acc[9 + 2 * i] + bfhi(h1[i]));
  }
  if (MODE) {
    u16* dst = out + (size_t)tok * DM + ncol;
    *(u32x4*)dst = o0;
    *(u32x4*)(dst + 8) = o1;
  } else {
    *(u32x4*)hfrag = o0;
    *(u32x4*)(hfrag + 512) = o1;
    float v[16], s = 0.f;
#pragma unroll
    for (int i = 0; i < 4; ++i) { v[2 * i] = bflo(o0[i]); v[2 * i + 1] = bfhi(o0[i]); v[8 + 2 * i] = bflo(o1[i]); v[9 + 2 * i] = bfhi(o1[i]); }
#pragma unroll
    for (int i = 0; i < 16; ++i) s += v[i];
    const float m32 = swap32_sum(s) * (1.f / 32);
    float ss = 0.f;
#pragma unroll
    for (int i = 0; i < 16; ++i) ss += (v[i] - m32) * (v[i] - m32);
    f32x2 st; st[0] = m32; st[1] = swap32_sum(ss);
    if (h == 0) *(f32x2*)(ws + WS_ST + ((size_t)((tok >> 5) * 16 + nb * 2 + nt) * 32 + r32) * 8) = st;
  }
  STAMP(7); STAMP_FLUSH(MODE ? 4 : 2);
}

constexpr int K3_SLOT = 12288, K3_FLG = 13 * K3_SLOT, K3_SC = K3_FLG + 64, K3_LDS = K3_SC + 768;

extern "C" __global__ __launch_bounds__(512) void k3_gateup(const u16* __restrict__ wd, char* __restrict__ ws) {
  extern __shared__ __attribute__((aligned(16))) char smem[];
  const lds_ptr lds = (lds_ptr)smem;
  const unsigned lds0 = (unsigned)(uintptr_t)smem;
  const int tid = threadIdx.x, lane = tid & 63;
  const int w = (int)rfl(tid >> 6);
  const int bid = blockIdx.x, jx = bid >> 3;
  if (jx >= 30) {
#define K3_NW 6
    const char* src = (const char*)wd + (size_t)(((jx & 1) * 8 + w) * 64 + lane) * 128;
#pragma unroll
    for (int i = 0; i < K3_NW; ++i) glds4(src + i * 131072, lds0 + w * 256);
    return;
  }
  const int rb = (bid & 7) * 2 + (jx & 1), cb = jx >> 1;
  const int at0 = cb * 6 > 80 ? 80 : cb * 6;
  STAMP_DECL STAMP(0);
  if (w >= 4) {
    const int lw = w - 4;
    const char* wsrc[3];
#pragma unroll
    for (int i = 0; i < 3; ++i) wsrc[i] = ws + WS_WGU + (size_t)(at0 + ((lw * 3 + i) >> 1)) * 32768 + ((lw * 3 + i) & 1) * 1024 + lane * 16;
#define K3_ISSUE(st) do { if ((st) >= 13) WAIT_CNT(lds + K3_FLG + 16, (st) - 12); \
    _Pragma("unroll") for (int i = 0; i < 3; ++i) glds16(wsrc[i] + (st) * 2048, lds0 + ((st) % 13) * K3_SLOT + (lw * 3 + i) * 1024); } while (0)
#define K3_PUB(n) LDS_VU32(lds + K3_FLG + lw * 4) = (n)
    K3_ISSUE(0); K3_ISSUE(1);
    RAWBAR();

    K3_ISSUE(2); WAIT_VM(6); K3_PUB(1);
    K3_ISSUE(3); WAIT_VM(6); K3_PUB(2);
    K3_ISSUE(4); WAIT_VM(6); K3_PUB(3);
    K3_ISSUE(5); WAIT_VM(6); K3_PUB(4);
    K3_ISSUE(6); WAIT_VM(6); K3_PUB(5);
    K3_ISSUE(7); WAIT_VM(6); K3_PUB(6);
    K3_ISSUE(8); WAIT_VM(6); K3_PUB(7);
    K3_ISSUE(9); WAIT_VM(6); K3_PUB(8);
    K3_ISSUE(10); WAIT_VM(6); K3_PUB(9);
    K3_ISSUE(11); WAIT_VM(6); K3_PUB(10);
    K3_ISSUE(12); WAIT_VM(6); K3_PUB(11);
    K3_ISSUE(13); WAIT_VM(6); K3_PUB(12);
    K3_ISSUE(14); WAIT_VM(6); K3_PUB(13);
    K3_ISSUE(15); WAIT_VM(6); K3_PUB(14);
    WAIT_VM(3); K3_PUB(15);
    WAIT_VM(0); K3_PUB(16);
    if (lw < 3) {
      const int i = lw * 64 + lane;
      const float* cp = (const float*)(ws + WS_CS) + ((i >> 4) & 1) * (8 * 1408) + ffeat(at0 + (i >> 5), i & 15);
      float c[8];
#pragma unroll
      for (int j = 0; j < 8; ++j) c[j] = cp[j * 1408];
      LDS_F32(lds + K3_SC + i * 4) = ((c[0] + c[1]) + (c[2] + c[3])) + ((c[4] + c[5]) + (c[6] + c[7]));
    }
    BAR();
    return;
  }
  const int r32 = lane & 31, h = lane >> 5;
  const char* bsrc = ws + WS_H + (size_t)(rb * 4 + w) * 32768 + lane * 16;
#define K3_BD 8
  bf16x8 fb_[K3_BD];
#define K3_LDB(kk) fb_[(kk) % K3_BD] = *(const bf16x8*)(bsrc + (kk) * 1024)
#pragma unroll
  for (int i = 0; i < 4; ++i) K3_LDB(i);
  if (tid < 8) LDS_VU32(lds + K3_FLG + tid * 4) = 0u;
  BAR();
#pragma unroll
  for (int i = 4; i < K3_BD; ++i) K3_LDB(i);
  f32x16 acc[6] = {};
  lds_ptr ap[3];
#pragma unroll
  for (int u = 0; u < 3; ++u) { ap[u] = lds + u * 5 * K3_SLOT + lane * 16; PIN(ap[u]); }
  bf16x8 fa[2][6];
  u32x4 rdy;
#define K3_A(kk, i) lds_b128(ap[(((kk) >> 1) % 13) / 5] + ((((kk) >> 1) % 13) % 5) * K3_SLOT + (i) * 2048 + ((kk) & 1) * 1024)
#define K3_STEP(kk) do { constexpr int n_ = (kk) + 2; \
    if (n_ < 32 && (n_ & 1) == 0 && __builtin_expect(rfl(min4(rdy)) < n_ / 2u + 1u, 0)) WAIT_CNT(lds + K3_FLG, n_ / 2 + 1); \
    _Pragma("unroll") for (int i = 0; i < 6; ++i) { \
      acc[i] = MFMA32(fa[(kk) & 1][i], fb_[(kk) % K3_BD], acc[i]); PINM(acc[i]); \
      if (i == 0 && (kk) > 0 && (kk) - 1 + K3_BD < 32) K3_LDB((kk) - 1 + K3_BD); \
      if (n_ < 32) fa[(kk) & 1][i] = K3_A(n_, i); \
      if ((n_ & 1) == 1 && i == 2) rdy = LDS_VU32X4(lds + K3_FLG); \
      SB(); } \
    if (((kk) & 1) == 1 && (kk) < 6) LDS_VU32(lds + K3_FLG + 16 + w * 4) = ((kk) + 1) / 2;   } while (0)
#define K3_STEP4(kk) do { K3_STEP(kk); K3_STEP((kk) + 1); K3_STEP((kk) + 2); K3_STEP((kk) + 3); } while (0)
  STAMP(1);
  WAIT_CNT(lds + K3_FLG, 1);
#pragma unroll
  for (int i = 0; i < 6; ++i) { fa[0][i] = K3_A(0, i); fa[1][i] = K3_A(1, i); }
  rdy = LDS_VU32X4(lds + K3_FLG);
  K3_STEP4(0); STAMP(2); K3_STEP4(4); STAMP(3);
  const f32x2 lp0 = *(const f32x2*)(ws + WS_ST + ((size_t)((rb * 4 + w) * 4 + h * 2) * 32 + r32) * 8), lp1 = *(const f32x2*)(ws + WS_ST + ((size_t)((rb * 4 + w) * 4 + h * 2 + 1) * 32 + r32) * 8);
  K3_STEP4(8); K3_STEP4(12); STAMP(4); K3_STEP4(16); K3_STEP4(20); K3_STEP4(24); STAMP(5);
  float mean = swap32_sum(lp0[0] + lp1[0]) * 0.25f;
  float rstd = ln_rstd(swap32_sum(lp0[1] + lp1[1] + 128.f * ((lp0[0] - mean) * (lp0[0] - mean) + (lp1[0] - mean) * (lp1[0] - mean))) * (1.f / DM));
  mean = rbf(mean);
  PIN(mean); PIN(rstd);
  BAR();
  K3_STEP4(28);
  STAMP(6);
  const float ke = -1.4426950408889634f * rstd, ir2 = __builtin_amdgcn_rcpf(rstd * rstd);
  const f32x2 nm2 = {-mean, -mean}, ke2 = {ke, ke}, ir22 = {ir2, ir2};
  f32x4 call[6][4];
#pragma unroll
  for (int i = 0; i < 6; ++i)
#pragma unroll
    for (int g = 0; g < 4; ++g) call[i][g] = LDS_F32X4(lds + K3_SC + i * 128 + (8 * g + 4 * h) * 4);
  SB();
#pragma unroll
  for (int i = 0; i < 6; ++i) {
    const f32x4 (&c4)[4] = call[i];
    f32x2 tg[4], tu[4], e[4];
#pragma unroll
    for (int j = 0; j < 4; ++j) {
      const f32x2 ag2 = {acc[i][2 * j], acc[i][2 * j + 1]}, au2 = {acc[i][8 + 2 * j], acc[i][9 + 2 * j]};
      const f32x2 cg2 = {c4[j >> 1][2 * (j & 1)], c4[j >> 1][2 * (j & 1) + 1]}, cu2 = {c4[2 + (j >> 1)][2 * (j & 1)], c4[2 + (j >> 1)][2 * (j & 1) + 1]};
      tg[j] = __builtin_elementwise_fma(nm2, cg2, ag2); tu[j] = __builtin_elementwise_fma(nm2, cu2, au2);
      e[j] = tg[j] * ke2;
    }
#pragma unroll
    for (int j = 0; j < 4; ++j) { e[j][0] = __builtin_amdgcn_exp2f(e[j][0]); e[j][1] = __builtin_amdgcn_exp2f(e[j][1]); tg[j] = tg[j] * tu[j]; }
#pragma unroll
    for (int j = 0; j < 4; ++j) e[j] = __builtin_elementwise_fma(e[j], ir22, ir22);
#pragma unroll
    for (int j = 0; j < 4; ++j) { e[j][0] = __builtin_amdgcn_rcpf(e[j][0]); e[j][1] = __builtin_amdgcn_rcpf(e[j][1]); }
    u32x4 pk;
#pragma unroll
    for (int j = 0; j < 4; ++j) { const f32x2 a2 = tg[j] * e[j]; pk[j] = pk2(a2[0], a2[1]); }
    *(u32x4*)(ws + WS_ACT + ((size_t)(rb * 4 + w) * NAT + at0 + i) * 1024 + lane * 16) = pk;
  }
  STAMP(7); STAMP_FLUSH(3);
}

extern "C" __global__ void k_end_marker() {}

extern "C" void kernel_launch(void* const* d_in, const int* in_sizes, int n_in, void* d_out, int out_size, void* d_ws, size_t ws_size,
                              hipStream_t stream) {
  const u16* x = (const u16*)d_in[0];
  const u16* wq = (const u16*)d_in[1];
  const u16* wk = (const u16*)d_in[2];
  const u16* wv = (const u16*)d_in[3];
  const u16* wo = (const u16*)d_in[4];
  const u16* wg = (const u16*)d_in[6];
  const u16* wu = (const u16*)d_in[7];
  const u16* wd = (const u16*)d_in[8];
  char* ws = (char*)d_ws;
  (void)hipFuncSetAttribute((const void*)k1_qkv, hipFuncAttributeMaxDynamicSharedMemorySize, K1_LDS);
  (void)hipFuncSetAttribute((const void*)k2_attn, hipFuncAttributeMaxDynamicSharedMemorySize, K2_LDS);
  (void)hipFuncSetAttribute((const void*)k3_gateup, hipFuncAttributeMaxDynamicSharedMemorySize, K3_LDS);
  (void)hipFuncSetAttribute((const void*)k4_down, hipFuncAttributeMaxDynamicSharedMemorySize, K4_LDS);
  k1_qkv<<<256, 640, K1_LDS, stream>>>(x, wq, wk, wv, wo, wg, wu, ws);
  k2_attn<<<256, 512, K2_LDS, stream>>>(x, wd, ws);
  k3_gateup<<<256, 512, K3_LDS, stream>>>(wd, ws);
  k4_down<<<256, 512, K4_LDS, stream>>>(wd, x, ws, (u16*)d_out);
}
```

```cpp
#include <hip/hip_runtime.h>
#include <hip/hip_bf16.h>
#include <stdint.h>

typedef __attribute__((ext_vector_type(8))) short bf16x8;
typedef __attribute__((ext_vector_type(4))) short s16x4;
typedef __attribute__((ext_vector_type(2))) __bf16 bf16x2;
typedef __attribute__((ext_vector_type(4))) unsigned u32x4;
typedef __attribute__((ext_vector_type(2))) unsigned u32x2;
typedef __attribute__((ext_vector_type(16))) float f32x16;
typedef __attribute__((ext_vector_type(4))) float f32x4;
typedef __attribute__((ext_vector_type(2))) float f32x2;
typedef __attribute__((address_space(3))) char* lds_ptr;
typedef unsigned short u16;

#define DI __device__ __forceinline__
#define MFMA32(a, b, c) __builtin_amdgcn_mfma_f32_32x32x16_bf16((a), (b), (c), 0, 0, 0)
#define MFMA16(a, b, c) __builtin_amdgcn_mfma_f32_16x16x32_bf16((a), (b), (c), 0, 0, 0)
#define WAIT_VM_BAR(N) asm volatile("s_waitcnt vmcnt(" #N ") lgkmcnt(0)\n\ts_barrier" ::: "memory")
#define BAR() asm volatile("s_waitcnt lgkmcnt(0)\n\ts_barrier" ::: "memory")

constexpr int SEQ = 2048, DM = 512, FF = 1376, NAT = 86;
constexpr float QSCALE = 0.25f * 1.4426950408889634f;
constexpr float LN_EPS = 1e-5f;

constexpr size_t WS_Q = 0;
constexpr size_t WS_K = WS_Q + (size_t)SEQ * DM * 2;
constexpr size_t WS_V = WS_K + 65536;
constexpr size_t WS_KMAX = WS_V + 65536;
constexpr size_t WS_WO = WS_KMAX + 4096;
constexpr size_t WS_H = WS_WO + (size_t)DM * DM * 2;
constexpr size_t WS_ST = WS_H + (size_t)SEQ * DM * 2;
constexpr size_t WS_CS = WS_ST + (size_t)SEQ * 16 * 8;
constexpr size_t WS_WGU = WS_CS + 2 * 8 * 1408 * 4;
constexpr size_t WS_ACT = WS_WGU + (size_t)NAT * 32768;

#define STAMP_DECL
#define STAMP(i) do { } while (0)
#define STAMP_FLUSH(kid) do { } while (0)
#define ACC_BEGIN() do { } while (0)
#define ACC_END(i) do { } while (0)
DI void glds16(const void* g, unsigned lds_base) {
  unsigned sv;
  asm volatile("s_mov_b32 %0, m0\n\ts_mov_b32 m0, %2\n\ts_nop 0\n\tglobal_load_lds_dwordx4 %1, off\n\ts_mov_b32 m0, %0"
               : "=&s"(sv) : "v"(g), "s"(lds_base) : "memory");
}
DI void glds4(const void* g, unsigned lds_base) {
  unsigned sv;
  asm volatile("s_mov_b32 %0, m0\n\ts_mov_b32 m0, %2\n\ts_nop 0\n\tglobal_load_lds_dword %1, off\n\ts_mov_b32 m0, %0"
               : "=&s"(sv) : "v"(g), "s"(lds_base) : "memory");
}
#define CODE_ADDR(sym) ({ unsigned lo_, hi_; asm volatile("s_getpc_b64 s[40:41]\n\ts_add_u32 %0, s40, " #sym "@rel32@lo+4\n\ts_addc_u32 %1, s41, " #sym "@rel32@hi+12" \
    : "=s"(lo_), "=s"(hi_) :: "s40", "s41", "scc"); (const char*)(((unsigned long long)hi_ << 32) | lo_); })
DI unsigned rfl(unsigned v) { return (unsigned)__builtin_amdgcn_readfirstlane((int)v); }
DI unsigned pk2(float lo, float hi) {
  bf16x2 v; v[0] = (__bf16)lo; v[1] = (__bf16)hi;
  return __builtin_bit_cast(unsigned, v);
}
DI int ffeat(int at, int rho) { return at < 84 ? 64 * (at >> 2) + 8 * (rho & 7) + 2 * (at & 3) + (rho >> 3) : 1344 + 8 * (rho & 3) + 4 * (at - 84) + (rho >> 2); }
DI void st16_wt(void* p, u32x4 v) { asm volatile("global_store_dwordx4 %0, %1, off sc1\n\ts_nop 1" :: "v"(p), "v"(v) : "memory"); }
DI float rbf(float v) { return (float)(__bf16)v; }
DI float ln_rstd(float var) { return __builtin_amdgcn_rcpf(rbf(__builtin_amdgcn_sqrtf(rbf(rbf(var) + LN_EPS)))); }
DI float bflo(unsigned u) { return __uint_as_float(u << 16); }
DI float bfhi(unsigned u) { return __uint_as_float(u & 0xffff0000u); }
DI float dot2(unsigned a, unsigned b, float c) {
  return __builtin_amdgcn_fdot2_f32_bf16(__builtin_bit_cast(bf16x2, a), __builtin_bit_cast(bf16x2, b), c, false);
}
DI bf16x8 lds_b128(lds_ptr p) { return *(const __attribute__((address_space(3))) bf16x8*)p; }
DI s16x4 lds_tr(lds_ptr p) {
  typedef short v4i16_t __attribute__((ext_vector_type(4)));
  return __builtin_bit_cast(s16x4, __builtin_amdgcn_ds_read_tr16_b64_v4i16((__attribute__((address_space(3))) v4i16_t*)p));
}
DI float swap32_sum(float v) {
  auto r = __builtin_amdgcn_permlane32_swap(__float_as_uint(v), __float_as_uint(v), false, false);
  return __uint_as_float(r[0]) + __uint_as_float(r[1]);
}
DI float swap16_sum(float v) {
  auto r = __builtin_amdgcn_permlane16_swap(__float_as_uint(v), __float_as_uint(v), false, false);
  return __uint_as_float(r[0]) + __uint_as_float(r[1]);
}
DI float swap32_max(float v) {
  auto r = __builtin_amdgcn_permlane32_swap(__float_as_uint(v), __float_as_uint(v), false, false);
  return fmaxf(__uint_as_float(r[0]), __uint_as_float(r[1]));
}
template <int CTRL, int RM>
DI float dpp(float old, float v) { return __uint_as_float((unsigned)__builtin_amdgcn_update_dpp((int)__float_as_uint(old), (int)__float_as_uint(v), CTRL, RM, 0xf, false)); }
DI float wave_sum(float v) {
  v += dpp<0x111, 0xf>(0.f, v); v += dpp<0x112, 0xf>(0.f, v); v += dpp<0x114, 0xf>(0.f, v); v += dpp<0x118, 0xf>(0.f, v);
  v += dpp<0x142, 0xa>(0.f, v); v += dpp<0x143, 0xc>(0.f, v);
  return __uint_as_float((unsigned)__builtin_amdgcn_readlane((int)__float_as_uint(v), 63));
}
DI float wave_max(float v) {
  v = fmaxf(v, dpp<0x111, 0xf>(0.f, v)); v = fmaxf(v, dpp<0x112, 0xf>(0.f, v)); v = fmaxf(v, dpp<0x114, 0xf>(0.f, v)); v = fmaxf(v, dpp<0x118, 0xf>(0.f, v));
  v = fmaxf(v, dpp<0x142, 0xa>(0.f, v)); v = fmaxf(v, dpp<0x143, 0xc>(0.f, v));
  return __uint_as_float((unsigned)__builtin_amdgcn_readlane((int)__float_as_uint(v), 63));
}
DI float quad_sum(float v) { v += dpp<0xb1, 0xf>(0.f, v); v += dpp<0x4e, 0xf>(0.f, v); return v; }

#define SB() __builtin_amdgcn_sched_barrier(0)
#define PIN(x) asm volatile("" : "+v"(x))
#define PINM(x) asm volatile("" : "+v"(x) :: "memory")
#define LDS_F32X4(p) (*(__attribute__((address_space(3))) f32x4*)(p))
#define LDS_F32X2(p) (*(__attribute__((address_space(3))) f32x2*)(p))
#define LDS_F32(p) (*(__attribute__((address_space(3))) float*)(p))
#define LDS_U32X2(p) (*(__attribute__((address_space(3))) u32x2*)(p))
#define LDS_U32X4(p) (*(__attribute__((address_space(3))) u32x4*)(p))
#define LDS_VU32(p) (*(volatile __attribute__((address_space(3))) unsigned*)(p))
#define WAIT_VM(N) asm volatile("s_waitcnt vmcnt(" #N ")" ::: "memory")
#define RAWBAR() asm volatile("s_barrier" ::: "memory")
#define LGKM0() asm volatile("s_waitcnt lgkmcnt(0)" ::: "memory")
DI bf16x8 cat8(s16x4 a, s16x4 b) { return __builtin_shufflevector(a, b, 0, 1, 2, 3, 4, 5, 6, 7); }
constexpr unsigned ONES = 0x3f803f80u;
#define LDS_VU32X4(p) (*(volatile __attribute__((address_space(3))) u32x4*)(p))
DI unsigned min4(u32x4 v) { return min(min(v[0], v[1]), min(v[2], v[3])); }
#define WAIT_CNT(p, n) do { while (rfl(min4(LDS_VU32X4(p))) < (unsigned)(n)) __builtin_amdgcn_s_sleep(1); asm volatile("" ::: "memory"); } while (0)

constexpr int K1_X = 0, K1_W = 65536, K1_KV = 131072, K1_SC = 147456, K1_FLG = K1_SC + 2048 + 8192, K1_JUNK = K1_FLG + 32, K1_LDS = K1_JUNK + 256;

extern "C" __global__ __launch_bounds__(640) void k1_qkv(const u16* __restrict__ x, const u16* __restrict__ wq, const u16* __restrict__ wk,
                                              const u16* __restrict__ wv, const u16* __restrict__ wo, const u16* __restrict__ wg,
                                              const u16* __restrict__ wu, char* __restrict__ ws) {
  extern __shared__ __attribute__((aligned(16))) char smem[];
  const lds_ptr lds = (lds_ptr)smem;
  const unsigned lds0 = (unsigned)(uintptr_t)smem;
  const int tid = threadIdx.x, lane = tid & 63;
  const int w = (int)rfl(tid >> 6);
  if (w < 3) {
    const char *c0 = CODE_ADDR(k1_qkv), *c1 = CODE_ADDR(k2_attn);
    const char* src = c0 + tid * 128;
    glds4(src < c1 ? src : c0, lds0 + K1_JUNK);
  }
  const int bid = blockIdx.x;
  const int tb = (bid & 7) * 4 + ((bid >> 3) & 3), fb = bid >> 5;
  const bool kvwg = fb < 2;
  STAMP_DECL STAMP(0);
  if (w >= 8) {
    RAWBAR();
    const int job = (w - 8) * 256 + (255 - bid);
    if (w == 9 && bid < 64) {
      const char *c0 = CODE_ADDR(k2_attn), *c1 = CODE_ADDR(k_end_marker);
      const char* src = c0 + ((bid >> 3) * 64 + lane) * 128;
      glds4(src < c1 ? src : c0, lds0 + K1_JUNK);
    }
    if (job < 416) {
        const bool isw = job < 64;
        const int jg = job - 64, mat = jg >= 176, jj = jg - mat * 176, ksl = jj & 7, cg = jj >> 3;
        const int col = cg * 64 + (lane & 7) * 8, kc = ksl * 8 + (lane >> 3);
        const u16* src = isw ? wo + (size_t)((job >> 3) * 64 + (job & 1) * 32 + 8 * (lane >> 4)) * DM + ((job >> 1) & 3) * 128 + (lane & 15) * 8
                             : (mat ? wu : wg) + (size_t)kc * 8 * FF + (col < FF ? col : 0);
        const int ld = isw ? DM : FF;
#define K1_SJ 2
        WAIT_CNT(lds + K1_FLG, K1_SJ);
        u32x4 R[8];
#pragma unroll
        for (int j = 0; j < 8; ++j) R[j] = *(const u32x4*)(src + (size_t)j * ld);
        char* dst = isw ? ws + WS_WO + (size_t)job * 8192 + lane * 16
                        : ws + WS_WGU + ((size_t)(cg * 128 + (kc >> 1)) * 64 + (kc & 1) * 32 + mat * 16 + (lane & 7)) * 16;
        float c[8];
#pragma unroll
        for (int m = 0; m < 4; ++m) {
          u32x4 lo, hi;
#pragma unroll
          for (int i = 0; i < 4; ++i) { lo[i] = __builtin_amdgcn_perm(R[2 * i + 1][m], R[2 * i][m], 0x05040100u); hi[i] = __builtin_amdgcn_perm(R[2 * i + 1][m], R[2 * i][m], 0x07060302u); }
          const int o0 = isw ? 2 * m * 1024 : cg < 21 ? m * 32768 : (m >> 1) * 32768 + (m & 1) * 128, o1 = o0 + (isw ? 1024 : cg < 21 ? 128 : 64);
          if (isw || col < FF) { st16_wt(dst + o0, lo); st16_wt(dst + o1, hi); }
          c[2 * m] = dot2(lo[3], ONES, dot2(lo[2], ONES, dot2(lo[1], ONES, dot2(lo[0], ONES, 0.f))));
          c[2 * m + 1] = dot2(hi[3], ONES, dot2(hi[2], ONES, dot2(hi[1], ONES, dot2(hi[0], ONES, 0.f))));
        }
        if (!isw) {
#pragma unroll
          for (int i = 0; i < 8; ++i) { c[i] += dpp<0x128, 0xf>(0.f, c[i]); c[i] = swap32_sum(swap16_sum(c[i])); }
          if (lane < 8) {
            float* cd = (float*)(ws + WS_CS) + (mat * 8 + ksl) * 1408 + cg * 64 + lane * 8;
            *(f32x4*)cd = f32x4{c[0], c[1], c[2], c[3]};
            *(f32x4*)(cd + 4) = f32x4{c[4], c[5], c[6], c[7]};
          }
        }
    }
    return;
  }
  if (w >= 4) {
    const int lw = w - 4;
    const u16* xs = x + (size_t)(tb * 64 + lw * 16 + (lane >> 4)) * DM;
    const int xc = (lane & 15) ^ (lane >> 4);
    const int rr = lane >> 3, pos = lane & 7;
    const u16* wsrc = wq + (size_t)(lw * 32 + rr) * DM + fb * 64 + ((pos ^ (((rr >> 1) & 1) << 2)) * 8);
    const u16* kvs = (fb ? wv : wk) + lw * 512 + lane * 8;
#define K1_ISSUE(st, KV) do { _Pragma("unroll") for (int i = 0; i < 4; ++i) { \
      glds16(xs + (size_t)i * 4 * DM + (st) * 128 + ((xc ^ (4 * i)) * 8), lds0 + K1_X + (st) * 16384 + (lw * 4 + i) * 1024); \
      glds16(wsrc + (size_t)((st) * 128 + i * 8) * DM, lds0 + K1_W + (st) * 16384 + (lw * 4 + i) * 1024); } \
    if (KV) glds16(kvs + (st) * 2048, lds0 + K1_KV + (st) * 4096 + lw * 1024); } while (0)
#define K1_PUB(n) LDS_VU32(lds + K1_FLG + lw * 4) = (n)
    if (kvwg) {
      K1_ISSUE(0, 1); RAWBAR(); K1_ISSUE(1, 1);
      WAIT_VM(9); K1_PUB(1); K1_ISSUE(2, 1); WAIT_VM(9); K1_PUB(2); K1_ISSUE(3, 1); WAIT_VM(9); K1_PUB(3);
    } else {
      K1_ISSUE(0, 0); RAWBAR(); K1_ISSUE(1, 0);
      WAIT_VM(8); K1_PUB(1); K1_ISSUE(2, 0); WAIT_VM(8); K1_PUB(2); K1_ISSUE(3, 0); WAIT_VM(8); K1_PUB(3);
    }
    WAIT_VM(0); K1_PUB(4);
    return;
  }
  if (tid < 8) LDS_VU32(lds + K1_FLG + tid * 4) = 0u;
  BAR();
  const int r32 = lane & 31, h = lane >> 5;
  const int blk = (lane >> 4) & 1, q = (lane & 15) >> 2, p = lane & 3;
  const int tt = w & 1, ft = w >> 1;
  const int tok = tt * 32 + r32;
  lds_ptr xb[8];
#pragma unroll
  for (int i = 0; i < 8; ++i) xb[i] = lds + K1_X + tok * 256 + (((2 * i + h) ^ (tok & 15)) * 16);
  const lds_ptr wb = lds + K1_W + (8 * h + q) * 128 + (((ft * 4 + blk * 2 + (p >> 1)) ^ ((q >> 1) << 2)) * 16) + 8 * (p & 1);
  const lds_ptr kb = lds + K1_KV + (8 * h + q) * 32 + 8 * p;
  const float piv = __uint_as_float((unsigned)x[(size_t)(tb * 64 + tok) * DM] << 16);
  f32x16 acc = {}, acc2 = {};
  float cs = 0.f, cs2 = 0.f, sx = 0.f, sxx = 0.f, sxb = 0.f, sxxb = 0.f;
  s16x4 a[2][4][2]; bf16x8 b[2][4];
#define K1_STEP(bu, MM, LD, s0, sc0) do { _Pragma("unroll") for (int i = 0; i < 4; ++i) { const int s = (s0) + i; \
      if (MM) { acc = MFMA32(cat8(a[bu][i][0], a[bu][i][1]), b[bu][i], acc); PINM(acc); } \
      if (LD) { a[1 - (bu)][i][0] = lds_tr(wb + s * 2048); a[1 - (bu)][i][1] = lds_tr(wb + s * 2048 + 512); b[1 - (bu)][i] = lds_b128(xb[s & 7] + (s >> 3) * 16384); } SB(); } \
    if (MM) { \
      if (ft == (bu)) { \
        if (kvwg) { bf16x8 ka[4]; \
          _Pragma("unroll") for (int i = 0; i < 4; ++i) ka[i] = cat8(lds_tr(kb + ((sc0) + i) * 512), lds_tr(kb + ((sc0) + i) * 512 + 128)); \
          if (fb == 0) { _Pragma("unroll") for (int i = 0; i < 4; ++i) acc2 = MFMA32(ka[i], b[bu][i], acc2); } \
          else { _Pragma("unroll") for (int i = 0; i < 4; ++i) acc2 = MFMA32(b[bu][i], ka[i], acc2); } \
          PINM(acc2); SB(); \
          _Pragma("unroll") for (int i = 0; i < 4; ++i) { const u32x4 u = __builtin_bit_cast(u32x4, ka[i]); \
            cs2 = dot2(u[0], ONES, cs2); cs2 = dot2(u[1], ONES, cs2); cs2 = dot2(u[2], ONES, cs2); cs2 = dot2(u[3], ONES, cs2); } PIN(cs2); } \
        _Pragma("unroll") for (int i = 0; i < 4; ++i) { const u32x4 u = __builtin_bit_cast(u32x4, b[bu][i]); \
          sx = dot2(u[0], ONES, sx); sxx = dot2(u[0], u[0], sxx); sxb = dot2(u[1], ONES, sxb); sxxb = dot2(u[1], u[1], sxxb); \
          sx = dot2(u[2], ONES, sx); sxx = dot2(u[2], u[2], sxx); sxb = dot2(u[3], ONES, sxb); sxxb = dot2(u[3], u[3], sxxb); } \
        PIN(sx); PIN(sxx); PIN(sxb); PIN(sxxb); } \
      if (tt == (bu)) { _Pragma("unroll") for (int i = 0; i < 4; ++i) { const u32x4 u = __builtin_bit_cast(u32x4, cat8(a[bu][i][0], a[bu][i][1])); \
          cs = dot2(u[0], ONES, cs); cs = dot2(u[1], ONES, cs); cs = dot2(u[2], ONES, cs); cs = dot2(u[3], ONES, cs); } PIN(cs); } \
      SB(); } } while (0)
  STAMP(1);
#pragma unroll
  for (int st = 0; st < 4; ++st) { WAIT_CNT(lds + K1_FLG, st + 1); K1_STEP(1, st > 0, true, st * 8, st * 8 - 4); K1_STEP(0, true, true, st * 8 + 4, st * 8); }
  K1_STEP(1, true, false, 0, 28);
  STAMP(2);
  const lds_ptr sc = lds + K1_SC;
  {
    sx += sxb; sxx += sxxb;
    f32x2 d; d[0] = swap32_sum(sx - 128.f * piv); d[1] = swap32_sum(__builtin_fmaf(-2.f * piv, sx, sxx) + 128.f * piv * piv);
    cs = swap32_sum(cs); cs2 = swap32_sum(cs2);
    if (h == 0) { LDS_F32(sc + (ft * 2 + tt) * 128 + r32 * 4) = cs; LDS_F32X2(sc + 512 + (tt * 2 + ft) * 256 + r32 * 8) = d; LDS_F32(sc + 1536 + w * 128 + r32 * 4) = cs2; }
    if (kvwg && ft == 1) {
#pragma unroll
      for (int g = 0; g < 4; ++g) { f32x4 t; t[0] = acc2[4 * g]; t[1] = acc2[4 * g + 1]; t[2] = acc2[4 * g + 2]; t[3] = acc2[4 * g + 3]; LDS_F32X4(sc + 2048 + tt * 4096 + g * 1024 + lane * 16) = t; }
    }
  }
  asm volatile("" ::: "memory");
  LDS_VU32(lds + K1_FLG + 16 + w * 4) = 1u;
  WAIT_CNT(lds + K1_FLG + 16, 1);
  float mean, rstd;
  {
    const f32x2 d = LDS_F32X2(sc + 512 + tt * 512 + r32 * 8) + LDS_F32X2(sc + 512 + tt * 512 + 256 + r32 * 8);
    const float dmn = d[0] * (1.f / DM);
    mean = rbf(piv + dmn); rstd = ln_rstd(fmaxf(d[1] * (1.f / DM) - dmn * dmn, 0.f));
  }
  {
    const float scl = rstd * QSCALE;
    float o[16];
#pragma unroll
    for (int g = 0; g < 4; ++g) {
      const f32x4 c4 = LDS_F32X4(sc + ft * 256 + (8 * g + 4 * h) * 4) + LDS_F32X4(sc + ft * 256 + 128 + (8 * g + 4 * h) * 4);
#pragma unroll
      for (int j = 0; j < 4; ++j) o[4 * g + j] = (acc[4 * g + j] - mean * c4[j]) * scl;
    }
    u32x4 p0, p1;
#pragma unroll
    for (int i = 0; i < 4; ++i) { p0[i] = pk2(o[2 * i], o[2 * i + 1]); p1[i] = pk2(o[8 + 2 * i], o[9 + 2 * i]); }
    char* dst = ws + WS_Q + (size_t)(tb * 64 + tok) * 1024 + (fb * 4 + ft * 2) * 32 + h * 16;
    *(u32x4*)dst = p0;
    *(u32x4*)(dst + 32) = p1;
  }
  if (kvwg && ft == 0) {
    const int gt = tb * 2 + tt;
#pragma unroll
    for (int g = 0; g < 4; ++g) { const f32x4 t = LDS_F32X4(sc + 2048 + tt * 4096 + g * 1024 + lane * 16);
#pragma unroll
      for (int j = 0; j < 4; ++j) acc2[4 * g + j] += t[j]; }
    if (fb == 0) {
      float o[8];
#pragma unroll
      for (int g = 0; g < 2; ++g) {
        const f32x4 c4 = LDS_F32X4(sc + 1536 + tt * 128 + (8 * g + 4 * h) * 4) + LDS_F32X4(sc + 1536 + (tt + 2) * 128 + (8 * g + 4 * h) * 4);
#pragma unroll
        for (int j = 0; j < 4; ++j) o[4 * g + j] = (acc2[4 * g + j] - mean * c4[j]) * rstd;
      }
      u32x4 p0;
#pragma unroll
      for (int i = 0; i < 4; ++i) p0[i] = pk2(o[2 * i], o[2 * i + 1]);
      *(u32x4*)(ws + WS_K + (size_t)gt * 1024 + h * 512 + r32 * 16) = p0;
      float n2 = 0.f;
#pragma unroll
      for (int i = 0; i < 4; ++i) n2 = dot2(p0[i], p0[i], n2);
      n2 = wave_max(swap32_sum(n2));
      if (lane == 0) *(float*)(ws + WS_KMAX + gt * 4) = n2;
    } else {
      const float cv = LDS_F32(sc + 1536 + tt * 128 + r32 * 4) + LDS_F32(sc + 1536 + (tt + 2) * 128 + r32 * 4);
      const lds_ptr sp = sc + 2048 + tt * 4096;
      LGKM0();
      if (h == 0) { f32x2 st; st[0] = mean; st[1] = rstd; LDS_F32X2(sp + r32 * 8) = st; }
      float o[16];
#pragma unroll
      for (int g = 0; g < 4; ++g) {
        const f32x4 s01 = LDS_F32X4(sp + (8 * g + 4 * h) * 8), s23 = LDS_F32X4(sp + (8 * g + 4 * h) * 8 + 16);
        o[4 * g + 0] = (acc2[4 * g + 0] - s01[0] * cv) * s01[1];
        o[4 * g + 1] = (acc2[4 * g + 1] - s01[2] * cv) * s01[3];
        o[4 * g + 2] = (acc2[4 * g + 2] - s23[0] * cv) * s23[1];
        o[4 * g + 3] = (acc2[4 * g + 3] - s23[2] * cv) * s23[3];
      }
      u32x4 p0, p1;
#pragma unroll
      for (int i = 0; i < 4; ++i) { p0[i] = pk2(o[2 * i], o[2 * i + 1]); p1[i] = pk2(o[8 + 2 * i], o[9 + 2 * i]); }
      if (r32 < 16) {
        char* dst = ws + WS_V + (size_t)gt * 1024 + h * 256 + r32 * 16;
        *(u32x4*)dst = p0;
        *(u32x4*)(dst + 512) = p1;
      }
    }
  }
  STAMP(3); STAMP_FLUSH(0);
}

constexpr int K2_PSZ = 2176;
constexpr int K2_RED = 0;
constexpr int K2_Q = 64 * K2_PSZ;
constexpr int K2_NM = K2_Q + 8192;
constexpr int K2_JUNK = K2_NM + 8192;
constexpr int K2_FLG = K2_JUNK + 256;
constexpr int K2_OB = K2_FLG + 512;
constexpr int K2_LDS = K2_OB + 2 * 1152;
constexpr int K2_MX = 0;

#define E_(r) "v_exp_f32 v" #r ", v" #r "\n\t"
#define C_(d, a, b) "v_cvt_pk_bf16_f32 v" #d ", v" #a ", v" #b "\n\t"
#define PVPREV_ "v_mfma_f32_32x32x16_bf16 v[200:215], %8, v[236:239], v[200:215]\n\t"
#define TILE_A1 E_(168) E_(169)
#define TILE_A2 E_(170) E_(171) E_(172) E_(173) "v_mfma_f32_32x32x16_bf16 v[184:199], %6, %7, v[216:231]\n\t" E_(174) E_(175) \
    C_(232, 168, 169) C_(233, 170, 171) C_(234, 172, 173) C_(235, 174, 175) E_(176) E_(177)
#define TILE_A3 E_(178) E_(179) E_(180) E_(181) E_(182) E_(183) C_(236, 176, 177) C_(237, 178, 179) C_(238, 180, 181) C_(239, 182, 183)
#define TILE_B1 E_(184) E_(185)
#define TILE_B2 E_(186) E_(187) E_(188) E_(189) "v_mfma_f32_32x32x16_bf16 v[168:183], %6, %7, v[216:231]\n\t" E_(190) E_(191) \
    C_(232, 184, 185) C_(233, 186, 187) C_(234, 188, 189) C_(235, 190, 191) E_(192) E_(193)
#define TILE_B3 E_(194) E_(195) E_(196) E_(197) E_(198) E_(199) C_(236, 192, 193) C_(237, 194, 195) C_(238, 196, 197) C_(239, 198, 199)
#define PV0_ "v_mfma_f32_32x32x16_bf16 v[200:215], %9, v[232:235], v[200:215]\n\t"
#define PV0Z_ "v_mfma_f32_32x32x16_bf16 v[200:215], %9, v[232:235], 0\n\t"
#define MF_(D, A, B, C) "v_mfma_f32_32x32x16_bf16 " D ", " A ", " B ", " C "\n\t"
#define XT_A(KN, QN, VP, V0) E_(168) E_(169) MF_("v[200:215]", VP, "v[236:239]", "v[200:215]") E_(170) E_(171) E_(172) E_(173) MF_("v[184:199]", KN, QN, "v[216:231]") E_(174) E_(175) \
    C_(232, 168, 169) C_(233, 170, 171) C_(234, 172, 173) C_(235, 174, 175) E_(176) E_(177) MF_("v[200:215]", V0, "v[232:235]", "v[200:215]") TILE_A3
#define XT_B(KN, QN, VP, V0) E_(184) E_(185) MF_("v[200:215]", VP, "v[236:239]", "v[200:215]") E_(186) E_(187) E_(188) E_(189) MF_("v[168:183]", KN, QN, "v[216:231]") E_(190) E_(191) \
    C_(232, 184, 185) C_(233, 186, 187) C_(234, 188, 189) C_(235, 190, 191) E_(192) E_(193) MF_("v[200:215]", V0, "v[232:235]", "v[200:215]") TILE_B3
#define K2_ASM_MID() asm volatile(MF_("v[200:215]", "%26", "v[232:235]", "0") TILE_A3 \
    XT_B("%7", "%6", "%13", "%14") XT_A("%8", "%6", "%15", "%16") XT_B("%9", "%6", "%17", "%18") XT_A("%10", "%6", "%19", "%20") XT_B("%11", "%6", "%21", "%22") XT_A("%12", "%6", "%23", "%24") \
    : "+{v[168:183]}"(sa), "+{v[184:199]}"(sb), "+{v[200:215]}"(o), "+{v[232:235]}"(p0), "+{v[236:239]}"(p1), "+{v[216:231]}"(negm) \
    : "v"(qf), "v"(kf[2]), "v"(kf[3]), "v"(kf[4]), "v"(kf[5]), "v"(kf[6]), "v"(kf[7]), "v"(vf[0][1]), "v"(vf[1][0]), "v"(vf[1][1]), "v"(vf[2][0]), \
      "v"(vf[2][1]), "v"(vf[3][0]), "v"(vf[3][1]), "v"(vf[4][0]), "v"(vf[4][1]), "v"(vf[5][0]), "v"(vf[5][1]), "v"(vf[6][0]), "v"(vf[6][0]), "v"(vf[0][0]))
#define K2_ASM(TXT, KN, QN, VP, V0) asm volatile(TXT : "+{v[168:183]}"(sa), "+{v[184:199]}"(sb), "+{v[200:215]}"(o), "+{v[232:235]}"(p0), "+{v[236:239]}"(p1), "+{v[216:231]}"(negm) \
    : "v"(KN), "v"(QN), "v"(VP), "v"(V0))

extern "C" __global__ __launch_bounds__(512) void k2_attn(const u16* __restrict__ x, const u16* __restrict__ wd, char* __restrict__ ws) {
  extern __shared__ __attribute__((aligned(16))) char smem[];
  const lds_ptr lds = (lds_ptr)smem;
  const unsigned lds0 = (unsigned)(uintptr_t)smem;
  const int tid = threadIdx.x, lane = tid & 63;
  const int w = (int)rfl(tid >> 6);
  const int g8 = (blockIdx.x & 7) * 32 + (blockIdx.x >> 3);
  const int r32 = lane & 31, h = lane >> 5;
  STAMP_DECL STAMP(0);
  const u32x4 qown = *(const u32x4*)(ws + WS_Q + (size_t)(g8 * 8 + (r32 & 7)) * 1024 + (w * 4 + (r32 >> 3)) * 32 + h * 16);
  const float km2l = *(const float*)(ws + WS_KMAX + lane * 4);
  bf16x8 kf[8], vf[8][2];
  u32x4 vr[8];
  const char* ksrc = ws + WS_K + (size_t)w * 8192 + lane * 16;
  const char* vsrc = ws + WS_V + (size_t)w * 8192 + (r32 >> 4) * 512 + h * 256 + (lane & 15) * 16;
#define K2_LOADKV(j) do { kf[j] = *(const bf16x8*)(ksrc + (j) * 1024); vr[j] = *(const u32x4*)(vsrc + (j) * 1024); } while (0)
#define K2_SPLITV(j) do { u32x4 a_, b_; _Pragma("unroll") for (int i_ = 0; i_ < 4; ++i_) { \
    const auto r_ = __builtin_amdgcn_permlane16_swap(vr[j][i_], vones, false, false); a_[i_] = r_[0]; b_[i_] = r_[1]; } \
    vf[j][0] = __builtin_bit_cast(bf16x8, a_); vf[j][1] = __builtin_bit_cast(bf16x8, b_); } while (0)
  const unsigned vones = (lane & 11) == 0 ? ONES : 0u;
  K2_LOADKV(0); K2_LOADKV(1);
  if (w < 4) __builtin_amdgcn_s_setprio(2); else __builtin_amdgcn_s_setprio(1);
  if (tid >= 8 && tid < 128) LDS_VU32(lds + K2_FLG + tid * 4) = 0u;
  {
    float n2 = 0.f;
#pragma unroll
    for (int i = 0; i < 4; ++i) n2 = dot2(qown[i], qown[i], n2);
    const float mref = sqrtf(swap32_sum(n2) * wave_max(km2l));
    const f32x4 nm = {-mref, -mref, -mref, -mref};
    LDS_U32X4(lds + K2_Q + w * 1024 + lane * 16) = qown;
    LDS_F32X4(lds + K2_NM + w * 1024 + lane * 16) = nm;
    LDS_VU32(lds + K2_FLG + w * 4) = __any(!(mref <= 40.f)) ? 1u : 0u;
  }
  STAMP(1);
  BAR();
  STAMP(2);
  K2_LOADKV(2); K2_LOADKV(3); K2_LOADKV(4); K2_LOADKV(5); K2_LOADKV(6); K2_LOADKV(7);
  const lds_ptr qp = lds + K2_Q + lane * 16;
  f32x16 negm = {};
  const bool slow = __any(LDS_VU32(lds + K2_FLG + (lane & 7) * 4) != 0u);
  if (slow) {
    const f32x16 zero = {};
    for (int g = 0; g < 8; ++g) {
      const bf16x8 qf = lds_b128(qp + g * 1024);
      float mx = -3.0e38f;
#pragma unroll
      for (int j = 0; j < 8; ++j) {
        const f32x16 s = MFMA32(kf[j], qf, zero);
#pragma unroll
        for (int i = 0; i < 16; ++i) mx = fmaxf(mx, s[i]);
      }
      mx = swap32_max(mx);
      if (h == 0) LDS_F32(lds + K2_MX + (g * 8 + w) * 128 + r32 * 4) = mx;
    }
    BAR();
    float mx = LDS_F32(lds + K2_MX + w * 1024 + r32 * 4);
#pragma unroll
    for (int i = 1; i < 8; ++i) mx = fmaxf(mx, LDS_F32(lds + K2_MX + w * 1024 + i * 128 + r32 * 4));
    const f32x4 nm = {-mx, -mx, -mx, -mx};
    LDS_F32X4(lds + K2_NM + w * 1024 + lane * 16) = nm;
    BAR();
  }
  else {
    float nm = LDS_F32(lds + K2_NM + lane * 16);
#pragma unroll
    for (int g = 1; g < 8; ++g) nm = fminf(nm, LDS_F32(lds + K2_NM + g * 1024 + lane * 16));
#pragma unroll
    for (int i = 0; i < 16; ++i) negm[i] = nm;
  }
#define K2_LOAD_NEGM(g) asm volatile("ds_read_b128 v[216:219], %1\n\tds_read_b128 v[220:223], %1\n\tds_read_b128 v[224:227], %1\n\tds_read_b128 v[228:231], %1\n\ts_waitcnt lgkmcnt(0)" \
    : "+{v[216:231]}"(negm) : "v"(lds0 + K2_NM + (g) * 1024 + lane * 16) : "memory")
  if (slow) K2_LOAD_NEGM(0);
  f32x16 o = {}, sa, sb = {};
  u32x4 p0 = {}, p1 = {};
  bf16x8 qf = lds_b128(qp);
  sa = MFMA32(kf[0], qf, negm);
  const lds_ptr redw = lds + K2_RED + w * K2_PSZ + lane * 16;
#define K2_WRITE_PARTIAL(g) do { const lds_ptr rp_ = redw + (g) * (8 * K2_PSZ); f32x4 t0_, t1_; \
    t0_[0] = o[0]; t0_[1] = o[1]; t0_[2] = o[2]; t0_[3] = o[3]; t1_[0] = o[4]; t1_[1] = o[5]; t1_[2] = o[6]; t1_[3] = o[7]; \
    LDS_F32X4(rp_) = t0_; LDS_F32X4(rp_ + 1024) = t1_; LDS_F32(rp_ + 2048 - lane * 16 + r32 * 4) = o[8]; \
    asm volatile("" ::: "memory"); LDS_VU32(lds + K2_FLG + 32 + ((g) * 8 + w) * 4) = 1u; } while (0)
#define VMWAIT0() __builtin_amdgcn_s_waitcnt(0x0F70)
#define SPL(j) do { if (FIRST_) { K2_SPLITV(j); asm volatile("s_nop 1" : "+v"(vf[j][0]), "+v"(vf[j][1])); } } while (0)
#define K2_ITER(g, FIRST) do { constexpr bool FIRST_ = FIRST; \
    const bf16x8 qn = lds_b128(qp + (((g) + 1) & 7) * 1024); \
    if (FIRST) { \
      SPL(0); K2_ASM(TILE_A1 TILE_A2 PV0Z_ TILE_A3, kf[1], qf, vf[0][0], vf[0][0]); \
      SPL(1); K2_ASM(TILE_B1 PVPREV_ TILE_B2 PV0_ TILE_B3, kf[2], qf, vf[0][1], vf[1][0]); \
      SPL(2); K2_ASM(TILE_A1 PVPREV_ TILE_A2 PV0_ TILE_A3, kf[3], qf, vf[1][1], vf[2][0]); \
      SPL(3); K2_ASM(TILE_B1 PVPREV_ TILE_B2 PV0_ TILE_B3, kf[4], qf, vf[2][1], vf[3][0]); \
      SPL(4); K2_ASM(TILE_A1 PVPREV_ TILE_A2 PV0_ TILE_A3, kf[5], qf, vf[3][1], vf[4][0]); \
      SPL(5); K2_ASM(TILE_B1 PVPREV_ TILE_B2 PV0_ TILE_B3, kf[6], qf, vf[4][1], vf[5][0]); \
      SPL(6); K2_ASM(TILE_A1 PVPREV_ TILE_A2 PV0_ TILE_A3, kf[7], qf, vf[5][1], vf[6][0]); \
      SPL(7); \
    } else { K2_ASM(TILE_A1 PVPREV_ TILE_A2, kf[1], qf, vf[7][1], vf[0][0]); K2_WRITE_PARTIAL((g) - 1); K2_ASM_MID(); } \
    if (slow) K2_LOAD_NEGM(((g) + 1) & 7); \
    K2_ASM(TILE_B1 PVPREV_ TILE_B2 PV0_ TILE_B3, kf[0], qn, vf[6][1], vf[7][0]); \
    qf = qn; } while (0)

  STAMP(3);
  asm volatile("s_nop 15" : "+{v[168:183]}"(sa));
  K2_ITER(0, true);
  VMWAIT0();
#pragma unroll
  for (int g = 1; g < 8; ++g) {
    if (g <= 5 && w < 4) {
      const int ln = ((g - 1) * 128 + (blockIdx.x >> 3) * 4 + w) * 64 + lane;
      const char* src = ln < 11008 ? (const char*)wd + ln * 128 : ln < 15104 ? ws + WS_WO + (ln - 11008) * 128 : ws + WS_WGU + (ln < 37120 ? ln - 15104 : 0) * 128;
      glds4(src, lds0 + K2_JUNK);
    }
    if (g == 7 && w == 0 && blockIdx.x < 32) {
      const char *c0 = CODE_ADDR(k4_down), *c1 = CODE_ADDR(k_end_marker);
      const char* src = c0 + ((blockIdx.x >> 3) * 64 + lane) * 128;
      glds4(src < c1 ? src : c0, lds0 + K2_JUNK);
    }
    K2_ITER(g, false);
  }
  STAMP(4);
  K2_ASM("s_nop 1\n\t" PVPREV_ "s_nop 15", kf[0], qf, vf[7][1], vf[7][0]);
  K2_WRITE_PARTIAL(7);
  if (w >= 4) { STAMP(5); STAMP_FLUSH(1); return; }
#define K2_P2 2
  __builtin_amdgcn_s_setprio(K2_P2);
  asm volatile("s_waitcnt vmcnt(0)" ::: "memory");
  VMWAIT0();
  const int g4 = lane >> 4, tok8 = lane & 7;
  const char* wsrc = ws + WS_WO + (size_t)w * 16384 + lane * 16;
  bf16x8 wr[5][8];
#define K2_WLOAD(hh) do { if ((hh) < 16) { _Pragma("unroll") for (int i_ = 0; i_ < 8; ++i_) wr[(hh) % 5][i_] = *(const bf16x8*)(wsrc + (size_t)((hh) >> 1) * 65536 + ((hh) & 1) * 8192 + i_ * 1024); } } while (0)
  f32x4 oacc[8];
  const float piv = __uint_as_float((unsigned)x[(size_t)(g8 * 8 + tok8) * DM + w * 128] << 16);
  {
    u32x4 xr[4];
#pragma unroll
    for (int r = 0; r < 4; ++r) xr[r] = *(const u32x4*)(x + (size_t)(g8 * 8 + tok8) * DM + w * 128 + g4 * 32 + r * 8);
    K2_WLOAD(0); K2_WLOAD(1);
#pragma unroll
    for (int r = 0; r < 4; ++r)
#pragma unroll
      for (int i = 0; i < 4; ++i) { oacc[2 * i][r] = bflo(xr[r][i]); oacc[2 * i + 1][r] = bfhi(xr[r][i]); }
  }
#define SLPV 2
#define GAPV 2
#define K2_GAP(g, f) do { if ((g) < 7 && ((f) & 1)) __builtin_amdgcn_s_sleep(1); } while (0)
#define K2_PROJ(g) do { \
    while (!__all(LDS_VU32(lds + K2_FLG + 32 + ((g) * 8 + tok8) * 4) != 0u)) __builtin_amdgcn_s_sleep((g) < 7 ? SLPV : 1); \
    asm volatile("" ::: "memory"); \
    { const lds_ptr rp_ = lds + K2_RED + (g) * (8 * K2_PSZ) + (w >> 1) * 1024 + lane * 16 + (w & 1) * 8, lp_ = lds + K2_RED + (g) * (8 * K2_PSZ) + 2048 + r32 * 4; \
      f32x2 t_[8]; float l_[8]; \
      _Pragma("unroll") for (int i_ = 0; i_ < 8; ++i_) { t_[i_] = LDS_F32X2(rp_ + i_ * K2_PSZ); l_[i_] = LDS_F32(lp_ + i_ * K2_PSZ); } \
      SB(); K2_WLOAD(2 * (g) + 2); SB(); \
      const f32x2 ts_ = ((t_[0] + t_[1]) + (t_[2] + t_[3])) + ((t_[4] + t_[5]) + (t_[6] + t_[7])); \
      const float inv_ = __builtin_amdgcn_rcpf(((l_[0] + l_[1]) + (l_[2] + l_[3])) + ((l_[4] + l_[5]) + (l_[6] + l_[7]))); \
      LDS_VU32(lds + K2_OB + ((g) & 1) * 1152 + (r32 & 7) * 144 + (r32 >> 3) * 32 + h * 8 + (w >> 1) * 16 + (w & 1) * 4) = pk2(ts_[0] * inv_, ts_[1] * inv_); \
      LDS_VU32(lds + K2_FLG + 288 + ((g) * 4 + w) * 4) = 1u; } \
    SB(); K2_WLOAD(2 * (g) + 3); SB(); \
    while (!__all(LDS_VU32(lds + K2_FLG + 288 + ((g) * 4 + (lane & 3)) * 4) != 0u)) __builtin_amdgcn_s_sleep(1); \
    asm volatile("" ::: "memory"); \
    const bf16x8 b0_ = lds_b128(lds + K2_OB + ((g) & 1) * 1152 + tok8 * 144 + g4 * 16), b1_ = lds_b128(lds + K2_OB + ((g) & 1) * 1152 + tok8 * 144 + 64 + g4 * 16); \
    _Pragma("unroll") for (int f_ = 0; f_ < 8; ++f_) { oacc[f_] = MFMA16(wr[(2 * (g)) % 5][f_], b0_, oacc[f_]); PINM(oacc[f_]); K2_GAP(g, f_); } \
    _Pragma("unroll") for (int f_ = 0; f_ < 8; ++f_) { oacc[f_] = MFMA16(wr[(2 * (g) + 1) % 5][f_], b1_, oacc[f_]); PINM(oacc[f_]); K2_GAP(g, f_); } } while (0)
  STAMP(5);
  K2_PROJ(0); K2_PROJ(1); K2_PROJ(2); K2_PROJ(3); K2_PROJ(4); K2_PROJ(5); K2_PROJ(6); K2_PROJ(7);
  STAMP(6);
  {
    const int tok = g8 * 8 + tok8;
    u32x4 hb[4];
#pragma unroll
    for (int r = 0; r < 4; ++r)
#pragma unroll
      for (int i = 0; i < 4; ++i) hb[r][i] = pk2(oacc[2 * i][r], oacc[2 * i + 1][r]);
    if ((lane & 8) == 0) {
#pragma unroll
      for (int r = 0; r < 4; ++r) *(u32x4*)(ws + WS_H + (size_t)(tok >> 5) * 32768 + (w * 16 + g4 * 4 + r) * 512 + (tok & 31) * 16) = hb[r];
    }
    float sx = 0.f, sxx = 0.f;
#pragma unroll
    for (int r = 0; r < 4; ++r)
#pragma unroll
      for (int i = 0; i < 4; ++i) { sx = dot2(hb[r][i], ONES, sx); sxx = dot2(hb[r][i], hb[r][i], sxx); }
    const float d1 = swap32_sum(swap16_sum(sx - 32.f * piv)), d2 = swap32_sum(swap16_sum(__builtin_fmaf(-2.f * piv, sx, sxx) + 32.f * piv * piv));
    f32x2 st; st[0] = piv + d1 * (1.f / 128); st[1] = fmaxf(d2 - d1 * d1 * (1.f / 128), 0.f);
    if (lane < 8) *(f32x2*)(ws + WS_ST + ((size_t)((tok >> 5) * 4 + w) * 32 + (tok & 31)) * 8) = st;
  }
  STAMP(7); STAMP_FLUSH(1);
}

constexpr int K4_SLOT = 32768, K4_LDS = 5 * K4_SLOT;

extern "C" __global__ __launch_bounds__(512) void k4_down(const u16* __restrict__ W, const u16* __restrict__ x, char* __restrict__ ws, u16* __restrict__ out) {
  constexpr int MODE = 1, NKS = NAT;
  extern __shared__ __attribute__((aligned(16))) char smem[];
  const lds_ptr lds = (lds_ptr)smem;
  const unsigned lds0 = (unsigned)(uintptr_t)smem;
  const int tid = threadIdx.x, lane = tid & 63;
  const int w = (int)rfl(tid >> 6);
  const int bid = blockIdx.x;
  const int tb = (bid & 7) * 4 + ((bid >> 3) & 3), nb = bid >> 5;
  const int r32 = lane & 31, h = lane >> 5;
  STAMP_DECL STAMP(0);
  if (w >= 4) {
    const int lw = w - 4;
    const int rr = lane >> 3, pos = lane & 7;
    const char* wcol = (const char*)W + nb * 128 + (pos ^ (((rr >> 1) & 1) << 2)) * 16;
    const char* wsrc = wcol + (size_t)(64 * (lw >> 1) + 8 * rr + 4 * (lw & 1)) * 1024;
    const char* wlast = wcol + (size_t)(1344 + 8 * (rr & 3) + (rr >> 2)) * 1024;
    const char* asrc = ws + WS_ACT + ((size_t)(tb * 2 + (lw >> 1)) * NKS + (lw & 1) * 4) * 1024 + lane * 16;
#define K4_ISSUE(st) do { const unsigned sl_ = lds0 + ((st) % 5) * K4_SLOT; _Pragma("unroll") for (int i = 0; i < 4; ++i) { \
    const bool oka_ = (st) < 10 || !((lw & 1) && i >= 2); \
    glds16(asrc + (oka_ ? (st) * 8192 + i * 1024 : 0), sl_ + 16384 + (lw * 4 + i) * 1024); \
    glds16(((st) < 10 || lw < 2) ? wsrc + (size_t)(st) * 131072 + i * 1024 : wlast + i * 2048, sl_ + (lw * 4 + i) * 1024); } } while (0)
    if (MODE == 0) {
      K4_ISSUE(0); K4_ISSUE(1);
      WAIT_VM(8); RAWBAR(); K4_ISSUE(2);
      WAIT_VM(8); RAWBAR(); K4_ISSUE(3);
      WAIT_VM(8); RAWBAR();
      WAIT_VM(0); RAWBAR();
    } else {
      K4_ISSUE(0); K4_ISSUE(1); K4_ISSUE(2);
      WAIT_VM(16); RAWBAR(); K4_ISSUE(3); K4_ISSUE(4);
      WAIT_VM(24); RAWBAR(); K4_ISSUE(5);
      WAIT_VM(24); RAWBAR(); K4_ISSUE(6);
      WAIT_VM(24); RAWBAR(); K4_ISSUE(7);
      WAIT_VM(24); RAWBAR(); K4_ISSUE(8);
      WAIT_VM(24); RAWBAR(); K4_ISSUE(9);
      WAIT_VM(24); RAWBAR(); K4_ISSUE(10);
      WAIT_VM(24); RAWBAR();
      WAIT_VM(16); RAWBAR();
      WAIT_VM(8); RAWBAR();
      WAIT_VM(0); RAWBAR();
    }
    STAMP(7); STAMP_FLUSH(MODE ? 4 : 2);
    return;
  }
  const int nt = w & 1, tt = w >> 1;
  const int tok = tb * 64 + tt * 32 + r32;
  const int ncol = nb * 64 + nt * 32 + h * 16;
  const int blk = (lane >> 4) & 1, q = (lane & 15) >> 2, p = lane & 3;
  char* hfrag = ws + WS_H + (size_t)(tok >> 5) * 32768 + (ncol >> 3) * 512 + r32 * 16;
  u32x4 h0, h1;
  if (MODE) { h0 = *(const u32x4*)(hfrag); h1 = *(const u32x4*)(hfrag + 512); }
  else { h0 = *(const u32x4*)(x + (size_t)tok * DM + ncol); h1 = *(const u32x4*)(x + (size_t)tok * DM + ncol + 8); }
  f32x16 acc = {};
  const int cbyte = nt * 64 + (16 * (p & 1) + 8 * blk + 4 * (p >> 1)) * 2;
  lds_ptr ap[3], bp[3];
#pragma unroll
  for (int u = 0; u < 3; ++u) { ap[u] = lds + u * 2 * K4_SLOT + (4 * h + q) * 128 + (cbyte ^ ((q >> 1) << 6)); bp[u] = lds + u * 2 * K4_SLOT + 16384 + tt * 8192 + lane * 16; }
  s16x4 fa[2][4][2]; bf16x8 fb_[2][4];
#define K4_U(st) (((st) % 5) >> 1)
#define K4_O(st) ((((st) % 5) & 1) * K4_SLOT)
#define K4_LD1(bu, st, k0, k) do { fa[bu][k][0] = lds_tr(ap[K4_U(st)] + K4_O(st) + ((k0) + (k)) * 2048); fa[bu][k][1] = lds_tr(ap[K4_U(st)] + K4_O(st) + ((k0) + (k)) * 2048 + 1024); \
    fb_[bu][k] = lds_b128(bp[K4_U(st)] + K4_O(st) + ((k0) + (k)) * 1024); } while (0)
#define K4_STEP(bu, NM, NL, st, k0) do { _Pragma("unroll") for (int k = 0; k < 4; ++k) { \
    if (k < (NM)) { acc = MFMA32(cat8(fa[bu][k][0], fa[bu][k][1]), fb_[bu][k], acc); PINM(acc); } \
    if (k < (NL)) K4_LD1(1 - (bu), st, k0, k); SB(); } } while (0)
#define K4_STAGE(st, NK2) do { BAR(); K4_STEP(1, (st) > 0 ? 4 : 0, 4, st, 0); K4_STEP(0, 4, NK2, st, 4); } while (0)
  STAMP(1);
  K4_STAGE(0, 4); STAMP(2); K4_STAGE(1, 4); STAMP(3); K4_STAGE(2, 4); K4_STAGE(3, 4);
  if (MODE) {
    K4_STAGE(4, 4); STAMP(4); K4_STAGE(5, 4); K4_STAGE(6, 4); K4_STAGE(7, 4); K4_STAGE(8, 4); K4_STAGE(9, 4); STAMP(5); K4_STAGE(10, 2);
    K4_STEP(1, 2, 0, 0, 0);
  } else {
    K4_STEP(1, 4, 0, 0, 0);
  }
  STAMP(6);
  u32x4 o0, o1;
#pragma unroll
  for (int i = 0; i < 4; ++i) {
    o0[i] = pk2(acc[2 * i] + bflo(h0[i]), acc[2 * i + 1] + bfhi(h0[i]));
    o1[i] = pk2(acc[8 + 2 * i] + bflo(h1[i]), acc[9 + 2 * i] + bfhi(h1[i]));
  }
  if (MODE) {
    u16* dst = out + (size_t)tok * DM + ncol;
    *(u32x4*)dst = o0;
    *(u32x4*)(dst + 8) = o1;
  } else {
    *(u32x4*)hfrag = o0;
    *(u32x4*)(hfrag + 512) = o1;
    float v[16], s = 0.f;
#pragma unroll
    for (int i = 0; i < 4; ++i) { v[2 * i] = bflo(o0[i]); v[2 * i + 1] = bfhi(o0[i]); v[8 + 2 * i] = bflo(o1[i]); v[9 + 2 * i] = bfhi(o1[i]); }
#pragma unroll
    for (int i = 0; i < 16; ++i) s += v[i];
    const float m32 = swap32_sum(s) * (1.f / 32);
    float ss = 0.f;
#pragma unroll
    for (int i = 0; i < 16; ++i) ss += (v[i] - m32) * (v[i] - m32);
    f32x2 st; st[0] = m32; st[1] = swap32_sum(ss);
    if (h == 0) *(f32x2*)(ws + WS_ST + ((size_t)((tok >> 5) * 16 + nb * 2 + nt) * 32 + r32) * 8) = st;
  }
  STAMP(7); STAMP_FLUSH(MODE ? 4 : 2);
}

constexpr int K3_SLOT = 12288, K3_FLG = 13 * K3_SLOT, K3_SC = K3_FLG + 64, K3_LDS = K3_SC + 768;

extern "C" __global__ __launch_bounds__(512) void k3_gateup(const u16* __restrict__ wd, char* __restrict__ ws) {
  extern __shared__ __attribute__((aligned(16))) char smem[];
  const lds_ptr lds = (lds_ptr)smem;
  const unsigned lds0 = (unsigned)(uintptr_t)smem;
  const int tid = threadIdx.x, lane = tid & 63;
  const int w = (int)rfl(tid >> 6);
  const int bid = blockIdx.x, jx = bid >> 3;
  if (jx >= 30) {
#define K3_NW 6
    const char* src = (const char*)wd + (size_t)(((jx & 1) * 8 + w) * 64 + lane) * 128;
#pragma unroll
    for (int i = 0; i < K3_NW; ++i) glds4(src + i * 131072, lds0 + w * 256);
    return;
  }
  const int rb = (bid & 7) * 2 + (jx & 1), cb = jx >> 1;
  const int at0 = cb * 6 > 80 ? 80 : cb * 6;
  STAMP_DECL STAMP(0);
  if (w >= 4) {
    const int lw = w - 4;
    const char* wsrc[3];
#pragma unroll
    for (int i = 0; i < 3; ++i) wsrc[i] = ws + WS_WGU + (size_t)(at0 + ((lw * 3 + i) >> 1)) * 32768 + ((lw * 3 + i) & 1) * 1024 + lane * 16;
#define K3_ISSUE(st) do { if ((st) >= 13) WAIT_CNT(lds + K3_FLG + 16, (st) - 12); \
    _Pragma("unroll") for (int i = 0; i < 3; ++i) glds16(wsrc[i] + (st) * 2048, lds0 + ((st) % 13) * K3_SLOT + (lw * 3 + i) * 1024); } while (0)
#define K3_PUB(n) LDS_VU32(lds + K3_FLG + lw * 4) = (n)
    K3_ISSUE(0); K3_ISSUE(1);
    RAWBAR();

    K3_ISSUE(2); WAIT_VM(6); K3_PUB(1);
    K3_ISSUE(3); WAIT_VM(6); K3_PUB(2);
    K3_ISSUE(4); WAIT_VM(6); K3_PUB(3);
    K3_ISSUE(5); WAIT_VM(6); K3_PUB(4);
    K3_ISSUE(6); WAIT_VM(6); K3_PUB(5);
    K3_ISSUE(7); WAIT_VM(6); K3_PUB(6);
    K3_ISSUE(8); WAIT_VM(6); K3_PUB(7);
    K3_ISSUE(9); WAIT_VM(6); K3_PUB(8);
    K3_ISSUE(10); WAIT_VM(6); K3_PUB(9);
    K3_ISSUE(11); WAIT_VM(6); K3_PUB(10);
    K3_ISSUE(12); WAIT_VM(6); K3_PUB(11);
    K3_ISSUE(13); WAIT_VM(6); K3_PUB(12);
    K3_ISSUE(14); WAIT_VM(6); K3_PUB(13);
    K3_ISSUE(15); WAIT_VM(6); K3_PUB(14);
    WAIT_VM(3); K3_PUB(15);
    WAIT_VM(0); K3_PUB(16);
    if (lw < 3) {
      const int i = lw * 64 + lane;
      const float* cp = (const float*)(ws + WS_CS) + ((i >> 4) & 1) * (8 * 1408) + ffeat(at0 + (i >> 5), i & 15);
      float c[8];
#pragma unroll
      for (int j = 0; j < 8; ++j) c[j] = cp[j * 1408];
      LDS_F32(lds + K3_SC + i * 4) = ((c[0] + c[1]) + (c[2] + c[3])) + ((c[4] + c[5]) + (c[6] + c[7]));
    }
    BAR();
    return;
  }
  const int r32 = lane & 31, h = lane >> 5;
  const char* bsrc = ws + WS_H + (size_t)(rb * 4 + w) * 32768 + lane * 16;
#define K3_BD 8
  bf16x8 fb_[K3_BD];
#define K3_LDB(kk) fb_[(kk) % K3_BD] = *(const bf16x8*)(bsrc + (kk) * 1024)
#pragma unroll
  for (int i = 0; i < 4; ++i) K3_LDB(i);
  if (tid < 8) LDS_VU32(lds + K3_FLG + tid * 4) = 0u;
  BAR();
#pragma unroll
  for (int i = 4; i < K3_BD; ++i) K3_LDB(i);
  f32x16 acc[6] = {};
  lds_ptr ap[3];
#pragma unroll
  for (int u = 0; u < 3; ++u) { ap[u] = lds + u * 5 * K3_SLOT + lane * 16; PIN(ap[u]); }
  bf16x8 fa[2][6];
  u32x4 rdy;
#define K3_A(kk, i) lds_b128(ap[(((kk) >> 1) % 13) / 5] + ((((kk) >> 1) % 13) % 5) * K3_SLOT + (i) * 2048 + ((kk) & 1) * 1024)
#define K3_STEP(kk) do { constexpr int n_ = (kk) + 2; \
    if (n_ < 32 && (n_ & 1) == 0 && __builtin_expect(rfl(min4(rdy)) < n_ / 2u + 1u, 0)) WAIT_CNT(lds + K3_FLG, n_ / 2 + 1); \
    _Pragma("unroll") for (int i = 0; i < 6; ++i) { \
      acc[i] = MFMA32(fa[(kk) & 1][i], fb_[(kk) % K3_BD], acc[i]); PINM(acc[i]); \
      if (i == 0 && (kk) > 0 && (kk) - 1 + K3_BD < 32) K3_LDB((kk) - 1 + K3_BD); \
      if (n_ < 32) fa[(kk) & 1][i] = K3_A(n_, i); \
      if ((n_ & 1) == 1 && i == 2) rdy = LDS_VU32X4(lds + K3_FLG); \
      SB(); } \
    if (((kk) & 1) == 1 && (kk) < 6) LDS_VU32(lds + K3_FLG + 16 + w * 4) = ((kk) + 1) / 2;   } while (0)
#define K3_STEP4(kk) do { K3_STEP(kk); K3_STEP((kk) + 1); K3_STEP((kk) + 2); K3_STEP((kk) + 3); } while (0)
  STAMP(1);
  WAIT_CNT(lds + K3_FLG, 1);
#pragma unroll
  for (int i = 0; i < 6; ++i) { fa[0][i] = K3_A(0, i); fa[1][i] = K3_A(1, i); }
  rdy = LDS_VU32X4(lds + K3_FLG);
  K3_STEP4(0); STAMP(2); K3_STEP4(4); STAMP(3);
  const f32x2 lp0 = *(const f32x2*)(ws + WS_ST + ((size_t)((rb * 4 + w) * 4 + h * 2) * 32 + r32) * 8), lp1 = *(const f32x2*)(ws + WS_ST + ((size_t)((rb * 4 + w) * 4 + h * 2 + 1) * 32 + r32) * 8);
  K3_STEP4(8); K3_STEP4(12); STAMP(4); K3_STEP4(16); K3_STEP4(20); K3_STEP4(24); STAMP(5);
  float mean = swap32_sum(lp0[0] + lp1[0]) * 0.25f;
  float rstd = ln_rstd(swap32_sum(lp0[1] + lp1[1] + 128.f * ((lp0[0] - mean) * (lp0[0] - mean) + (lp1[0] - mean) * (lp1[0] - mean))) * (1.f / DM));
  mean = rbf(mean);
  PIN(mean); PIN(rstd);
  BAR();
  K3_STEP4(28);
  STAMP(6);
  const float ke = -1.4426950408889634f * rstd, ir2 = __builtin_amdgcn_rcpf(rstd * rstd);
  const f32x2 nm2 = {-mean, -mean}, ke2 = {ke, ke}, ir22 = {ir2, ir2};
  f32x4 call[6][4];
#pragma unroll
  for (int i = 0; i < 6; ++i)
#pragma unroll
    for (int g = 0; g < 4; ++g) call[i][g] = LDS_F32X4(lds + K3_SC + i * 128 + (8 * g + 4 * h) * 4);
  SB();
#pragma unroll
  for (int i = 0; i < 6; ++i) {
    const f32x4 (&c4)[4] = call[i];
    f32x2 tg[4], tu[4], e[4];
#pragma unroll
    for (int j = 0; j < 4; ++j) {
      const f32x2 ag2 = {acc[i][2 * j], acc[i][2 * j + 1]}, au2 = {acc[i][8 + 2 * j], acc[i][9 + 2 * j]};
      const f32x2 cg2 = {c4[j >> 1][2 * (j & 1)], c4[j >> 1][2 * (j & 1) + 1]}, cu2 = {c4[2 + (j >> 1)][2 * (j & 1)], c4[2 + (j >> 1)][2 * (j & 1) + 1]};
      tg[j] = __builtin_elementwise_fma(nm2, cg2, ag2); tu[j] = __builtin_elementwise_fma(nm2, cu2, au2);
      e[j] = tg[j] * ke2;
    }
#pragma unroll
    for (int j = 0; j < 4; ++j) { e[j][0] = __builtin_amdgcn_exp2f(e[j][0]); e[j][1] = __builtin_amdgcn_exp2f(e[j][1]); tg[j] = tg[j] * tu[j]; }
#pragma unroll
    for (int j = 0; j < 4; ++j) e[j] = __builtin_elementwise_fma(e[j], ir22, ir22);
#pragma unroll
    for (int j = 0; j < 4; ++j) { e[j][0] = __builtin_amdgcn_rcpf(e[j][0]); e[j][1] = __builtin_amdgcn_rcpf(e[j][1]); }
    u32x4 pk;
#pragma unroll
    for (int j = 0; j < 4; ++j) { const f32x2 a2 = tg[j] * e[j]; pk[j] = pk2(a2[0], a2[1]); }
    *(u32x4*)(ws + WS_ACT + ((size_t)(rb * 4 + w) * NAT + at0 + i) * 1024 + lane * 16) = pk;
  }
  STAMP(7); STAMP_FLUSH(3);
}

extern "C" __global__ void k_end_marker() {}

extern "C" void kernel_launch(void* const* d_in, const int* in_sizes, int n_in, void* d_out, int out_size, void* d_ws, size_t ws_size,
                              hipStream_t stream) {
  const u16* x = (const u16*)d_in[0];
  const u16* wq = (const u16*)d_in[1];
  const u16* wk = (const u16*)d_in[2];
  const u16* wv = (const u16*)d_in[3];
  const u16* wo = (const u16*)d_in[4];
  const u16* wg = (const u16*)d_in[6];
  const u16* wu = (const u16*)d_in[7];
  const u16* wd = (const u16*)d_in[8];
  char* ws = (char*)d_ws;
  (void)hipFuncSetAttribute((const void*)k1_qkv, hipFuncAttributeMaxDynamicSharedMemorySize, K1_LDS);
  (void)hipFuncSetAttribute((const void*)k2_attn, hipFuncAttributeMaxDynamicSharedMemorySize, K2_LDS);
  (void)hipFuncSetAttribute((const void*)k3_gateup, hipFuncAttributeMaxDynamicSharedMemorySize, K3_LDS);
  (void)hipFuncSetAttribute((const void*)k4_down, hipFuncAttributeMaxDynamicSharedMemorySize, K4_LDS);
  k1_qkv<<<256, 640, K1_LDS, stream>>>(x, wq, wk, wv, wo, wg, wu, ws);
  k2_attn<<<256, 512, K2_LDS, stream>>>(x, wd, ws);
  k3_gateup<<<256, 512, K3_LDS, stream>>>(wd, ws);
  k4_down<<<256, 512, K4_LDS, stream>>>(wd, x, ws, (u16*)d_out);
}
```

```cpp
#include <hip/hip_runtime.h>
#include <hip/hip_bf16.h>
#include <stdint.h>

typedef __attribute__((ext_vector_type(8))) short bf16x8;
typedef __attribute__((ext_vector_type(4))) short s16x4;
typedef __attribute__((ext_vector_type(2))) __bf16 bf16x2;
typedef __attribute__((ext_vector_type(4))) unsigned u32x4;
typedef __attribute__((ext_vector_type(2))) unsigned u32x2;
typedef __attribute__((ext_vector_type(16))) float f32x16;
typedef __attribute__((ext_vector_type(4))) float f32x4;
typedef __attribute__((ext_vector_type(2))) float f32x2;
typedef __attribute__((address_space(3))) char* lds_ptr;
typedef unsigned short u16;

#define DI __device__ __forceinline__
#define MFMA32(a, b, c) __builtin_amdgcn_mfma_f32_32x32x16_bf16((a), (b), (c), 0, 0, 0)
#define MFMA16(a, b, c) __builtin_amdgcn_mfma_f32_16x16x32_bf16((a), (b), (c), 0, 0, 0)
#define WAIT_VM_BAR(N) asm volatile("s_waitcnt vmcnt(" #N ") lgkmcnt(0)\n\ts_barrier" ::: "memory")
#define BAR() asm volatile("s_waitcnt lgkmcnt(0)\n\ts_barrier" ::: "memory")

constexpr int SEQ = 2048, DM = 512, FF = 1376, NAT = 86;
constexpr float QSCALE = 0.25f * 1.4426950408889634f;
constexpr float LN_EPS = 1e-5f;

constexpr size_t WS_Q = 0;
constexpr size_t WS_K = WS_Q + (size_t)SEQ * DM * 2;
constexpr size_t WS_V = WS_K + 65536;
constexpr size_t WS_KMAX = WS_V + 65536;
constexpr size_t WS_WO = WS_KMAX + 4096;
constexpr size_t WS_H = WS_WO + (size_t)DM * DM * 2;
constexpr size_t WS_ST = WS_H + (size_t)SEQ * DM * 2;
constexpr size_t WS_CS = WS_ST + (size_t)SEQ * 16 * 8;
constexpr size_t WS_WGU = WS_CS + 2 * 8 * 1408 * 4;
constexpr size_t WS_ACT = WS_WGU + (size_t)NAT * 32768;

DI void glds16(const void* g, unsigned lds_base) {
  unsigned sv;
  asm volatile("s_mov_b32 %0, m0\n\ts_mov_b32 m0, %2\n\ts_nop 0\n\tglobal_load_lds_dwordx4 %1, off\n\ts_mov_b32 m0, %0"
               : "=&s"(sv) : "v"(g), "s"(lds_base) : "memory");
}
DI void glds4(const void* g, unsigned lds_base) {
  unsigned sv;
  asm volatile("s_mov_b32 %0, m0\n\ts_mov_b32 m0, %2\n\ts_nop 0\n\tglobal_load_lds_dword %1, off\n\ts_mov_b32 m0, %0"
               : "=&s"(sv) : "v"(g), "s"(lds_base) : "memory");
}
#define CODE_ADDR(sym) ({ unsigned lo_, hi_; asm volatile("s_getpc_b64 s[40:41]\n\ts_add_u32 %0, s40, " #sym "@rel32@lo+4\n\ts_addc_u32 %1, s41, " #sym "@rel32@hi+12" \
    : "=s"(lo_), "=s"(hi_) :: "s40", "s41", "scc"); (const char*)(((unsigned long long)hi_ << 32) | lo_); })
DI unsigned rfl(unsigned v) { return (unsigned)__builtin_amdgcn_readfirstlane((int)v); }
DI unsigned pk2(float lo, float hi) {
  bf16x2 v; v[0] = (__bf16)lo; v[1] = (__bf16)hi;
  return __builtin_bit_cast(unsigned, v);
}
DI int ffeat(int at, int rho) { return at < 84 ? 64 * (at >> 2) + 8 * (rho & 7) + 2 * (at & 3) + (rho >> 3) : 1344 + 8 * (rho & 3) + 4 * (at - 84) + (rho >> 2); }
DI void st16_wt(void* p, u32x4 v) { asm volatile("global_store_dwordx4 %0, %1, off sc1\n\ts_nop 1" :: "v"(p), "v"(v) : "memory"); }
DI float rbf(float v) { return (float)(__bf16)v; }
DI float ln_rstd(float var) { return __builtin_amdgcn_rcpf(rbf(__builtin_amdgcn_sqrtf(rbf(rbf(var) + LN_EPS)))); }
DI float bflo(unsigned u) { return __uint_as_float(u << 16); }
DI float bfhi(unsigned u) { return __uint_as_float(u & 0xffff0000u); }
DI float dot2(unsigned a, unsigned b, float c) {
  return __builtin_amdgcn_fdot2_f32_bf16(__builtin_bit_cast(bf16x2, a), __builtin_bit_cast(bf16x2, b), c, false);
}
DI bf16x8 lds_b128(lds_ptr p) { return *(const __attribute__((address_space(3))) bf16x8*)p; }
DI s16x4 lds_tr(lds_ptr p) {
  typedef short v4i16_t __attribute__((ext_vector_type(4)));
  return __builtin_bit_cast(s16x4, __builtin_amdgcn_ds_read_tr16_b64_v4i16((__attribute__((address_space(3))) v4i16_t*)p));
}
DI float swap32_sum(float v) {
  auto r = __builtin_amdgcn_permlane32_swap(__float_as_uint(v), __float_as_uint(v), false, false);
  return __uint_as_float(r[0]) + __uint_as_float(r[1]);
}
DI float swap16_sum(float v) {
  auto r = __builtin_amdgcn_permlane16_swap(__float_as_uint(v), __float_as_uint(v), false, false);
  return __uint_as_float(r[0]) + __uint_as_float(r[1]);
}
DI float swap32_max(float v) {
  auto r = __builtin_amdgcn_permlane32_swap(__float_as_uint(v), __float_as_uint(v), false, false);
  return fmaxf(__uint_as_float(r[0]), __uint_as_float(r[1]));
}
template <int CTRL, int RM>
DI float dpp(float old, float v) { return __uint_as_float((unsigned)__builtin_amdgcn_update_dpp((int)__float_as_uint(old), (int)__float_as_uint(v), CTRL, RM, 0xf, false)); }
DI float wave_sum(float v) {
  v += dpp<0x111, 0xf>(0.f, v); v += dpp<0x112, 0xf>(0.f, v); v += dpp<0x114, 0xf>(0.f, v); v += dpp<0x118, 0xf>(0.f, v);
  v += dpp<0x142, 0xa>(0.f, v); v += dpp<0x143, 0xc>(0.f, v);
  return __uint_as_float((unsigned)__builtin_amdgcn_readlane((int)__float_as_uint(v), 63));
}
DI float wave_max(float v) {
  v = fmaxf(v, dpp<0x111, 0xf>(0.f, v)); v = fmaxf(v, dpp<0x112, 0xf>(0.f, v)); v = fmaxf(v, dpp<0x114, 0xf>(0.f, v)); v = fmaxf(v, dpp<0x118, 0xf>(0.f, v));
  v = fmaxf(v, dpp<0x142, 0xa>(0.f, v)); v = fmaxf(v, dpp<0x143, 0xc>(0.f, v));
  return __uint_as_float((unsigned)__builtin_amdgcn_readlane((int)__float_as_uint(v), 63));
}
DI float quad_sum(float v) { v += dpp<0xb1, 0xf>(0.f, v); v += dpp<0x4e, 0xf>(0.f, v); return v; }

#define SB() __builtin_amdgcn_sched_barrier(0)
#define PIN(x) asm volatile("" : "+v"(x))
#define PINM(x) asm volatile("" : "+v"(x) :: "memory")
#define LDS_F32X4(p) (*(__attribute__((address_space(3))) f32x4*)(p))
#define LDS_F32X2(p) (*(__attribute__((address_space(3))) f32x2*)(p))
#define LDS_F32(p) (*(__attribute__((address_space(3))) float*)(p))
#define LDS_U32X2(p) (*(__attribute__((address_space(3))) u32x2*)(p))
#define LDS_U32X4(p) (*(__attribute__((address_space(3))) u32x4*)(p))
#define LDS_VU32(p) (*(volatile __attribute__((address_space(3))) unsigned*)(p))
#define WAIT_VM(N) asm volatile("s_waitcnt vmcnt(" #N ")" ::: "memory")
#define RAWBAR() asm volatile("s_barrier" ::: "memory")
#define LGKM0() asm volatile("s_waitcnt lgkmcnt(0)" ::: "memory")
DI bf16x8 cat8(s16x4 a, s16x4 b) { return __builtin_shufflevector(a, b, 0, 1, 2, 3, 4, 5, 6, 7); }
constexpr unsigned ONES = 0x3f803f80u;
#define LDS_VU32X4(p) (*(volatile __attribute__((address_space(3))) u32x4*)(p))
DI unsigned min4(u32x4 v) { return min(min(v[0], v[1]), min(v[2], v[3])); }
#define WAIT_CNT(p, n) do { while (rfl(min4(LDS_VU32X4(p))) < (unsigned)(n)) __builtin_amdgcn_s_sleep(1); asm volatile("" ::: "memory"); } while (0)

constexpr int K1_X = 0, K1_W = 65536, K1_KV = 131072, K1_SC = 147456, K1_FLG = K1_SC + 2048 + 8192, K1_JUNK = K1_FLG + 32, K1_LDS = K1_JUNK + 256;

extern "C" __global__ __launch_bounds__(640) void k1_qkv(const u16* __restrict__ x, const u16* __restrict__ wq, const u16* __restrict__ wk,
                                              const u16* __restrict__ wv, const u16* __restrict__ wo, const u16* __restrict__ wg,
                                              const u16* __restrict__ wu, char* __restrict__ ws) {
  extern __shared__ __attribute__((aligned(16))) char smem[];
  const lds_ptr lds = (lds_ptr)smem;
  const unsigned lds0 = (unsigned)(uintptr_t)smem;
  const int tid = threadIdx.x, lane = tid & 63;
  const int w = (int)rfl(tid >> 6);
  if (w < 3) {
    const char *c0 = CODE_ADDR(k1_qkv), *c1 = CODE_ADDR(k2_attn);
    const char* src = c0 + tid * 128;
    glds4(src < c1 ? src : c0, lds0 + K1_JUNK);
  }
  const int bid = blockIdx.x;
  const int tb = (bid & 7) * 4 + ((bid >> 3) & 3), fb = bid >> 5;
  const bool kvwg = fb < 2;
  if (w >= 8) {
    RAWBAR();
    const int job = (w - 8) * 256 + (255 - bid);
    if (w == 9 && bid < 64) {
      const char *c0 = CODE_ADDR(k2_attn), *c1 = CODE_ADDR(k_end_marker);
      const char* src = c0 + ((bid >> 3) * 64 + lane) * 128;
      glds4(src < c1 ? src : c0, lds0 + K1_JUNK);
    }
    if (job < 416) {
        const bool isw = job < 64;
        const int jg = job - 64, mat = jg >= 176, jj = jg - mat * 176, ksl = jj & 7, cg = jj >> 3;
        const int col = cg * 64 + (lane & 7) * 8, kc = ksl * 8 + (lane >> 3);
        const u16* src = isw ? wo + (size_t)((job >> 3) * 64 + (job & 1) * 32 + 8 * (lane >> 4)) * DM + ((job >> 1) & 3) * 128 + (lane & 15) * 8
                             : (mat ? wu : wg) + (size_t)kc * 8 * FF + (col < FF ? col : 0);
        const int ld = isw ? DM : FF;
        WAIT_CNT(lds + K1_FLG, 2);
        u32x4 R[8];
#pragma unroll
        for (int j = 0; j < 8; ++j) R[j] = *(const u32x4*)(src + (size_t)j * ld);
        char* dst = isw ? ws + WS_WO + (size_t)job * 8192 + lane * 16
                        : ws + WS_WGU + ((size_t)(cg * 128 + (kc >> 1)) * 64 + (kc & 1) * 32 + mat * 16 + (lane & 7)) * 16;
        float c[8];
#pragma unroll
        for (int m = 0; m < 4; ++m) {
          u32x4 lo, hi;
#pragma unroll
          for (int i = 0; i < 4; ++i) { lo[i] = __builtin_amdgcn_perm(R[2 * i + 1][m], R[2 * i][m], 0x05040100u); hi[i] = __builtin_amdgcn_perm(R[2 * i + 1][m], R[2 * i][m], 0x07060302u); }
          const int o0 = isw ? 2 * m * 1024 : cg < 21 ? m * 32768 : (m >> 1) * 32768 + (m & 1) * 128, o1 = o0 + (isw ? 1024 : cg < 21 ? 128 : 64);
          if (isw || col < FF) { st16_wt(dst + o0, lo); st16_wt(dst + o1, hi); }
          c[2 * m] = dot2(lo[3], ONES, dot2(lo[2], ONES, dot2(lo[1], ONES, dot2(lo[0], ONES, 0.f))));
          c[2 * m + 1] = dot2(hi[3], ONES, dot2(hi[2], ONES, dot2(hi[1], ONES, dot2(hi[0], ONES, 0.f))));
        }
        if (!isw) {
#pragma unroll
          for (int i = 0; i < 8; ++i) { c[i] += dpp<0x128, 0xf>(0.f, c[i]); c[i] = swap32_sum(swap16_sum(c[i])); }
          if (lane < 8) {
            float* cd = (float*)(ws + WS_CS) + (mat * 8 + ksl) * 1408 + cg * 64 + lane * 8;
            *(f32x4*)cd = f32x4{c[0], c[1], c[2], c[3]};
            *(f32x4*)(cd + 4) = f32x4{c[4], c[5], c[6], c[7]};
          }
        }
    }
    return;
  }
  if (w >= 4) {
    const int lw = w - 4;
    const u16* xs = x + (size_t)(tb * 64 + lw * 16 + (lane >> 4)) * DM;
    const int xc = (lane & 15) ^ (lane >> 4);
    const int rr = lane >> 3, pos = lane & 7;
    const u16* wsrc = wq + (size_t)(lw * 32 + rr) * DM + fb * 64 + ((pos ^ (((rr >> 1) & 1) << 2)) * 8);
    const u16* kvs = (fb ? wv : wk) + lw * 512 + lane * 8;
#define K1_ISSUE(st, KV) do { _Pragma("unroll") for (int i = 0; i < 4; ++i) { \
      glds16(xs + (size_t)i * 4 * DM + (st) * 128 + ((xc ^ (4 * i)) * 8), lds0 + K1_X + (st) * 16384 + (lw * 4 + i) * 1024); \
      glds16(wsrc + (size_t)((st) * 128 + i * 8) * DM, lds0 + K1_W + (st) * 16384 + (lw * 4 + i) * 1024); } \
    if (KV) glds16(kvs + (st) * 2048, lds0 + K1_KV + (st) * 4096 + lw * 1024); } while (0)
#define K1_PUB(n) LDS_VU32(lds + K1_FLG + lw * 4) = (n)
    if (kvwg) {
      K1_ISSUE(0, 1); RAWBAR(); K1_ISSUE(1, 1);
      WAIT_VM(9); K1_PUB(1); K1_ISSUE(2, 1); WAIT_VM(9); K1_PUB(2); K1_ISSUE(3, 1); WAIT_VM(9); K1_PUB(3);
    } else {
      K1_ISSUE(0, 0); RAWBAR(); K1_ISSUE(1, 0);
      WAIT_VM(8); K1_PUB(1); K1_ISSUE(2, 0); WAIT_VM(8); K1_PUB(2); K1_ISSUE(3, 0); WAIT_VM(8); K1_PUB(3);
    }
    WAIT_VM(0); K1_PUB(4);
    return;
  }
  if (tid < 8) LDS_VU32(lds + K1_FLG + tid * 4) = 0u;
  BAR();
  const int r32 = lane & 31, h = lane >> 5;
  const int blk = (lane >> 4) & 1, q = (lane & 15) >> 2, p = lane & 3;
  const int tt = w & 1, ft = w >> 1;
  const int tok = tt * 32 + r32;
  lds_ptr xb[8];
#pragma unroll
  for (int i = 0; i < 8; ++i) xb[i] = lds + K1_X + tok * 256 + (((2 * i + h) ^ (tok & 15)) * 16);
  const lds_ptr wb = lds + K1_W + (8 * h + q) * 128 + (((ft * 4 + blk * 2 + (p >> 1)) ^ ((q >> 1) << 2)) * 16) + 8 * (p & 1);
  const lds_ptr kb = lds + K1_KV + (8 * h + q) * 32 + 8 * p;
  const float piv = __uint_as_float((unsigned)x[(size_t)(tb * 64 + tok) * DM] << 16);
  f32x16 acc = {}, acc2 = {};
  float cs = 0.f, cs2 = 0.f, sx = 0.f, sxx = 0.f, sxb = 0.f, sxxb = 0.f;
  s16x4 a[2][4][2]; bf16x8 b[2][4];
#define K1_STEP(bu, MM, LD, s0, sc0) do { _Pragma("unroll") for (int i = 0; i < 4; ++i) { const int s = (s0) + i; \
      if (MM) { acc = MFMA32(cat8(a[bu][i][0], a[bu][i][1]), b[bu][i], acc); PINM(acc); } \
      if (LD) { a[1 - (bu)][i][0] = lds_tr(wb + s * 2048); a[1 - (bu)][i][1] = lds_tr(wb + s * 2048 + 512); b[1 - (bu)][i] = lds_b128(xb[s & 7] + (s >> 3) * 16384); } SB(); } \
    if (MM) { \
      if (ft == (bu)) { \
        if (kvwg) { bf16x8 ka[4]; \
          _Pragma("unroll") for (int i = 0; i < 4; ++i) ka[i] = cat8(lds_tr(kb + ((sc0) + i) * 512), lds_tr(kb + ((sc0) + i) * 512 + 128)); \
          if (fb == 0) { _Pragma("unroll") for (int i = 0; i < 4; ++i) acc2 = MFMA32(ka[i], b[bu][i], acc2); } \
          else { _Pragma("unroll") for (int i = 0; i < 4; ++i) acc2 = MFMA32(b[bu][i], ka[i], acc2); } \
          PINM(acc2); SB(); \
          _Pragma("unroll") for (int i = 0; i < 4; ++i) { const u32x4 u = __builtin_bit_cast(u32x4, ka[i]); \
            cs2 = dot2(u[0], ONES, cs2); cs2 = dot2(u[1], ONES, cs2); cs2 = dot2(u[2], ONES, cs2); cs2 = dot2(u[3], ONES, cs2); } PIN(cs2); } \
        _Pragma("unroll") for (int i = 0; i < 4; ++i) { const u32x4 u = __builtin_bit_cast(u32x4, b[bu][i]); \
          sx = dot2(u[0], ONES, sx); sxx = dot2(u[0], u[0], sxx); sxb = dot2(u[1], ONES, sxb); sxxb = dot2(u[1], u[1], sxxb); \
          sx = dot2(u[2], ONES, sx); sxx = dot2(u[2], u[2], sxx); sxb = dot2(u[3], ONES, sxb); sxxb = dot2(u[3], u[3], sxxb); } \
        PIN(sx); PIN(sxx); PIN(sxb); PIN(sxxb); } \
      if (tt == (bu)) { _Pragma("unroll") for (int i = 0; i < 4; ++i) { const u32x4 u = __builtin_bit_cast(u32x4, cat8(a[bu][i][0], a[bu][i][1])); \
          cs = dot2(u[0], ONES, cs); cs = dot2(u[1], ONES, cs); cs = dot2(u[2], ONES, cs); cs = dot2(u[3], ONES, cs); } PIN(cs); } \
      SB(); } } while (0)
#pragma unroll
  for (int st = 0; st < 4; ++st) { WAIT_CNT(lds + K1_FLG, st + 1); K1_STEP(1, st > 0, true, st * 8, st * 8 - 4); K1_STEP(0, true, true, st * 8 + 4, st * 8); }
  K1_STEP(1, true, false, 0, 28);
  const lds_ptr sc = lds + K1_SC;
  {
    sx += sxb; sxx += sxxb;
    f32x2 d; d[0] = swap32_sum(sx - 128.f * piv); d[1] = swap32_sum(__builtin_fmaf(-2.f * piv, sx, sxx) + 128.f * piv * piv);
    cs = swap32_sum(cs); cs2 = swap32_sum(cs2);
    if (h == 0) { LDS_F32(sc + (ft * 2 + tt) * 128 + r32 * 4) = cs; LDS_F32X2(sc + 512 + (tt * 2 + ft) * 256 + r32 * 8) = d; LDS_F32(sc + 1536 + w * 128 + r32 * 4) = cs2; }
    if (kvwg && ft == 1) {
#pragma unroll
      for (int g = 0; g < 4; ++g) { f32x4 t; t[0] = acc2[4 * g]; t[1] = acc2[4 * g + 1]; t[2] = acc2[4 * g + 2]; t[3] = acc2[4 * g + 3]; LDS_F32X4(sc + 2048 + tt * 4096 + g * 1024 + lane * 16) = t; }
    }
  }
  asm volatile("" ::: "memory");
  LDS_VU32(lds + K1_FLG + 16 + w * 4) = 1u;
  WAIT_CNT(lds + K1_FLG + 16, 1);
  float mean, rstd;
  {
    const f32x2 d = LDS_F32X2(sc + 512 + tt * 512 + r32 * 8) + LDS_F32X2(sc + 512 + tt * 512 + 256 + r32 * 8);
    const float dmn = d[0] * (1.f / DM);
    mean = rbf(piv + dmn); rstd = ln_rstd(fmaxf(d[1] * (1.f / DM) - dmn * dmn, 0.f));
  }
  {
    const float scl = rstd * QSCALE;
    float o[16];
#pragma unroll
    for (int g = 0; g < 4; ++g) {
      const f32x4 c4 = LDS_F32X4(sc + ft * 256 + (8 * g + 4 * h) * 4) + LDS_F32X4(sc + ft * 256 + 128 + (8 * g + 4 * h) * 4);
#pragma unroll
      for (int j = 0; j < 4; ++j) o[4 * g + j] = (acc[4 * g + j] - mean * c4[j]) * scl;
    }
    u32x4 p0, p1;
#pragma unroll
    for (int i = 0; i < 4; ++i) { p0[i] = pk2(o[2 * i], o[2 * i + 1]); p1[i] = pk2(o[8 + 2 * i], o[9 + 2 * i]); }
    char* dst = ws + WS_Q + (size_t)(tb * 64 + tok) * 1024 + (fb * 4 + ft * 2) * 32 + h * 16;
    *(u32x4*)dst = p0;
    *(u32x4*)(dst + 32) = p1;
  }
  if (kvwg && ft == 0) {
    const int gt = tb * 2 + tt;
#pragma unroll
    for (int g = 0; g < 4; ++g) { const f32x4 t = LDS_F32X4(sc + 2048 + tt * 4096 + g * 1024 + lane * 16);
#pragma unroll
      for (int j = 0; j < 4; ++j) acc2[4 * g + j] += t[j]; }
    if (fb == 0) {
      float o[8];
#pragma unroll
      for (int g = 0; g < 2; ++g) {
        const f32x4 c4 = LDS_F32X4(sc + 1536 + tt * 128 + (8 * g + 4 * h) * 4) + LDS_F32X4(sc + 1536 + (tt + 2) * 128 + (8 * g + 4 * h) * 4);
#pragma unroll
        for (int j = 0; j < 4; ++j) o[4 * g + j] = (acc2[4 * g + j] - mean * c4[j]) * rstd;
      }
      u32x4 p0;
#pragma unroll
      for (int i = 0; i < 4; ++i) p0[i] = pk2(o[2 * i], o[2 * i + 1]);
      *(u32x4*)(ws + WS_K + (size_t)gt * 1024 + h * 512 + r32 * 16) = p0;
      float n2 = 0.f;
#pragma unroll
      for (int i = 0; i < 4; ++i) n2 = dot2(p0[i], p0[i], n2);
      n2 = wave_max(swap32_sum(n2));
      if (lane == 0) *(float*)(ws + WS_KMAX + gt * 4) = n2;
    } else {
      const float cv = LDS_F32(sc + 1536 + tt * 128 + r32 * 4) + LDS_F32(sc + 1536 + (tt + 2) * 128 + r32 * 4);
      const lds_ptr sp = sc + 2048 + tt * 4096;
      LGKM0();
      if (h == 0) { f32x2 st; st[0] = mean; st[1] = rstd; LDS_F32X2(sp + r32 * 8) = st; }
      float o[16];
#pragma unroll
      for (int g = 0; g < 4; ++g) {
        const f32x4 s01 = LDS_F32X4(sp + (8 * g + 4 * h) * 8), s23 = LDS_F32X4(sp + (8 * g + 4 * h) * 8 + 16);
        o[4 * g + 0] = (acc2[4 * g + 0] - s01[0] * cv) * s01[1];
        o[4 * g + 1] = (acc2[4 * g + 1] - s01[2] * cv) * s01[3];
        o[4 * g + 2] = (acc2[4 * g + 2] - s23[0] * cv) * s23[1];
        o[4 * g + 3] = (acc2[4 * g + 3] - s23[2] * cv) * s23[3];
      }
      u32x4 p0, p1;
#pragma unroll
      for (int i = 0; i < 4; ++i) { p0[i] = pk2(o[2 * i], o[2 * i + 1]); p1[i] = pk2(o[8 + 2 * i], o[9 + 2 * i]); }
      if (r32 < 16) {
        char* dst = ws + WS_V + (size_t)gt * 1024 + h * 256 + r32 * 16;
        *(u32x4*)dst = p0;
        *(u32x4*)(dst + 512) = p1;
      }
    }
  }
}

constexpr int K2_PSZ = 2176;
constexpr int K2_RED = 0;
constexpr int K2_Q = 64 * K2_PSZ;
constexpr int K2_NM = K2_Q + 8192;
constexpr int K2_JUNK = K2_NM + 8192;
constexpr int K2_FLG = K2_JUNK + 256;
constexpr int K2_OB = K2_FLG + 512;
constexpr int K2_LDS = K2_OB + 2 * 1152;
constexpr int K2_MX = 0;

#define E_(r) "v_exp_f32 v" #r ", v" #r "\n\t"
#define C_(d, a, b) "v_cvt_pk_bf16_f32 v" #d ", v" #a ", v" #b "\n\t"
#define PVPREV_ "v_mfma_f32_32x32x16_bf16 v[200:215], %8, v[236:239], v[200:215]\n\t"
#define TILE_A1 E_(168) E_(169)
#define TILE_A2 E_(170) E_(171) E_(172) E_(173) "v_mfma_f32_32x32x16_bf16 v[184:199], %6, %7, v[216:231]\n\t" E_(174) E_(175) \
    C_(232, 168, 169) C_(233, 170, 171) C_(234, 172, 173) C_(235, 174, 175) E_(176) E_(177)
#define TILE_A3 E_(178) E_(179) E_(180) E_(181) E_(182) E_(183) C_(236, 176, 177) C_(237, 178, 179) C_(238, 180, 181) C_(239, 182, 183)
#define TILE_B1 E_(184) E_(185)
#define TILE_B2 E_(186) E_(187) E_(188) E_(189) "v_mfma_f32_32x32x16_bf16 v[168:183], %6, %7, v[216:231]\n\t" E_(190) E_(191) \
    C_(232, 184, 185) C_(233, 186, 187) C_(234, 188, 189) C_(235, 190, 191) E_(192) E_(193)
#define TILE_B3 E_(194) E_(195) E_(196) E_(197) E_(198) E_(199) C_(236, 192, 193) C_(237, 194, 195) C_(238, 196, 197) C_(239, 198, 199)
#define PV0_ "v_mfma_f32_32x32x16_bf16 v[200:215], %9, v[232:235], v[200:215]\n\t"
#define PV0Z_ "v_mfma_f32_32x32x16_bf16 v[200:215], %9, v[232:235], 0\n\t"
#define MF_(D, A, B, C) "v_mfma_f32_32x32x16_bf16 " D ", " A ", " B ", " C "\n\t"
#define XT_A(KN, QN, VP, V0) E_(168) E_(169) MF_("v[200:215]", VP, "v[236:239]", "v[200:215]") E_(170) E_(171) E_(172) E_(173) MF_("v[184:199]", KN, QN, "v[216:231]") E_(174) E_(175) \
    C_(232, 168, 169) C_(233, 170, 171) C_(234, 172, 173) C_(235, 174, 175) E_(176) E_(177) MF_("v[200:215]", V0, "v[232:235]", "v[200:215]") TILE_A3
#define XT_B(KN, QN, VP, V0) E_(184) E_(185) MF_("v[200:215]", VP, "v[236:239]", "v[200:215]") E_(186) E_(187) E_(188) E_(189) MF_("v[168:183]", KN, QN, "v[216:231]") E_(190) E_(191) \
    C_(232, 184, 185) C_(233, 186, 187) C_(234, 188, 189) C_(235, 190, 191) E_(192) E_(193) MF_("v[200:215]", V0, "v[232:235]", "v[200:215]") TILE_B3
#define K2_ASM_MID() asm volatile(MF_("v[200:215]", "%26", "v[232:235]", "0") TILE_A3 \
    XT_B("%7", "%6", "%13", "%14") XT_A("%8", "%6", "%15", "%16") XT_B("%9", "%6", "%17", "%18") XT_A("%10", "%6", "%19", "%20") XT_B("%11", "%6", "%21", "%22") XT_A("%12", "%6", "%23", "%24") \
    : "+{v[168:183]}"(sa), "+{v[184:199]}"(sb), "+{v[200:215]}"(o), "+{v[232:235]}"(p0), "+{v[236:239]}"(p1), "+{v[216:231]}"(negm) \
    : "v"(qf), "v"(kf[2]), "v"(kf[3]), "v"(kf[4]), "v"(kf[5]), "v"(kf[6]), "v"(kf[7]), "v"(vf[0][1]), "v"(vf[1][0]), "v"(vf[1][1]), "v"(vf[2][0]), \
      "v"(vf[2][1]), "v"(vf[3][0]), "v"(vf[3][1]), "v"(vf[4][0]), "v"(vf[4][1]), "v"(vf[5][0]), "v"(vf[5][1]), "v"(vf[6][0]), "v"(vf[6][0]), "v"(vf[0][0]))
#define K2_ASM(TXT, KN, QN, VP, V0) asm volatile(TXT : "+{v[168:183]}"(sa), "+{v[184:199]}"(sb), "+{v[200:215]}"(o), "+{v[232:235]}"(p0), "+{v[236:239]}"(p1), "+{v[216:231]}"(negm) \
    : "v"(KN), "v"(QN), "v"(VP), "v"(V0))

extern "C" __global__ __launch_bounds__(512) void k2_attn(const u16* __restrict__ x, const u16* __restrict__ wd, char* __restrict__ ws) {
  extern __shared__ __attribute__((aligned(16))) char smem[];
  const lds_ptr lds = (lds_ptr)smem;
  const unsigned lds0 = (unsigned)(uintptr_t)smem;
  const int tid = threadIdx.x, lane = tid & 63;
  const int w = (int)rfl(tid >> 6);
  const int g8 = (blockIdx.x & 7) * 32 + (blockIdx.x >> 3);
  const int r32 = lane & 31, h = lane >> 5;
  const u32x4 qown = *(const u32x4*)(ws + WS_Q + (size_t)(g8 * 8 + (r32 & 7)) * 1024 + (w * 4 + (r32 >> 3)) * 32 + h * 16);
  const float km2l = *(const float*)(ws + WS_KMAX + lane * 4);
  bf16x8 kf[8], vf[8][2];
  u32x4 vr[8];
  const char* ksrc = ws + WS_K + (size_t)w * 8192 + lane * 16;
  const char* vsrc = ws + WS_V + (size_t)w * 8192 + (r32 >> 4) * 512 + h * 256 + (lane & 15) * 16;
#define K2_LOADKV(j) do { kf[j] = *(const bf16x8*)(ksrc + (j) * 1024); vr[j] = *(const u32x4*)(vsrc + (j) * 1024); } while (0)
#define K2_SPLITV(j) do { u32x4 a_, b_; _Pragma("unroll") for (int i_ = 0; i_ < 4; ++i_) { \
    const auto r_ = __builtin_amdgcn_permlane16_swap(vr[j][i_], vones, false, false); a_[i_] = r_[0]; b_[i_] = r_[1]; } \
    vf[j][0] = __builtin_bit_cast(bf16x8, a_); vf[j][1] = __builtin_bit_cast(bf16x8, b_); } while (0)
  const unsigned vones = (lane & 11) == 0 ? ONES : 0u;
  K2_LOADKV(0); K2_LOADKV(1);
  if (w < 4) __builtin_amdgcn_s_setprio(2); else __builtin_amdgcn_s_setprio(1);
  if (tid >= 8 && tid < 128) LDS_VU32(lds + K2_FLG + tid * 4) = 0u;
  {
    float n2 = 0.f;
#pragma unroll
    for (int i = 0; i < 4; ++i) n2 = dot2(qown[i], qown[i], n2);
    const float mref = sqrtf(swap32_sum(n2) * wave_max(km2l));
    const f32x4 nm = {-mref, -mref, -mref, -mref};
    LDS_U32X4(lds + K2_Q + w * 1024 + lane * 16) = qown;
    LDS_F32X4(lds + K2_NM + w * 1024 + lane * 16) = nm;
    LDS_VU32(lds + K2_FLG + w * 4) = __any(!(mref <= 40.f)) ? 1u : 0u;
  }
  BAR();
  K2_LOADKV(2); K2_LOADKV(3); K2_LOADKV(4); K2_LOADKV(5); K2_LOADKV(6); K2_LOADKV(7);
  const lds_ptr qp = lds + K2_Q + lane * 16;
  f32x16 negm = {};
  const bool slow = __any(LDS_VU32(lds + K2_FLG + (lane & 7) * 4) != 0u);
  if (slow) {
    const f32x16 zero = {};
    for (int g = 0; g < 8; ++g) {
      const bf16x8 qf = lds_b128(qp + g * 1024);
      float mx = -3.0e38f;
#pragma unroll
      for (int j = 0; j < 8; ++j) {
        const f32x16 s = MFMA32(kf[j], qf, zero);
#pragma unroll
        for (int i = 0; i < 16; ++i) mx = fmaxf(mx, s[i]);
      }
      mx = swap32_max(mx);
      if (h == 0) LDS_F32(lds + K2_MX + (g * 8 + w) * 128 + r32 * 4) = mx;
    }
    BAR();
    float mx = LDS_F32(lds + K2_MX + w * 1024 + r32 * 4);
#pragma unroll
    for (int i = 1; i < 8; ++i) mx = fmaxf(mx, LDS_F32(lds + K2_MX + w * 1024 + i * 128 + r32 * 4));
    const f32x4 nm = {-mx, -mx, -mx, -mx};
    LDS_F32X4(lds + K2_NM + w * 1024 + lane * 16) = nm;
    BAR();
  }
  else {
    float nm = LDS_F32(lds + K2_NM + lane * 16);
#pragma unroll
    for (int g = 1; g < 8; ++g) nm = fminf(nm, LDS_F32(lds + K2_NM + g * 1024 + lane * 16));
#pragma unroll
    for (int i = 0; i < 16; ++i) negm[i] = nm;
  }
#define K2_LOAD_NEGM(g) asm volatile("ds_read_b128 v[216:219], %1\n\tds_read_b128 v[220:223], %1\n\tds_read_b128 v[224:227], %1\n\tds_read_b128 v[228:231], %1\n\ts_waitcnt lgkmcnt(0)" \
    : "+{v[216:231]}"(negm) : "v"(lds0 + K2_NM + (g) * 1024 + lane * 16) : "memory")
  if (slow) K2_LOAD_NEGM(0);
  f32x16 o = {}, sa, sb = {};
  u32x4 p0 = {}, p1 = {};
  bf16x8 qf = lds_b128(qp);
  sa = MFMA32(kf[0], qf, negm);
  const lds_ptr redw = lds + K2_RED + w * K2_PSZ + lane * 16;
#define K2_WRITE_PARTIAL(g) do { const lds_ptr rp_ = redw + (g) * (8 * K2_PSZ); f32x4 t0_, t1_; \
    t0_[0] = o[0]; t0_[1] = o[1]; t0_[2] = o[2]; t0_[3] = o[3]; t1_[0] = o[4]; t1_[1] = o[5]; t1_[2] = o[6]; t1_[3] = o[7]; \
    LDS_F32X4(rp_) = t0_; LDS_F32X4(rp_ + 1024) = t1_; LDS_F32(rp_ + 2048 - lane * 16 + r32 * 4) = o[8]; \
    asm volatile("" ::: "memory"); LDS_VU32(lds + K2_FLG + 32 + ((g) * 8 + w) * 4) = 1u; } while (0)
#define VMWAIT0() __builtin_amdgcn_s_waitcnt(0x0F70)
#define SPL(j) do { if (FIRST_) { K2_SPLITV(j); asm volatile("s_nop 1" : "+v"(vf[j][0]), "+v"(vf[j][1])); } } while (0)
#define K2_ITER(g, FIRST) do { constexpr bool FIRST_ = FIRST; \
    const bf16x8 qn = lds_b128(qp + (((g) + 1) & 7) * 1024); \
    if (FIRST) { \
      SPL(0); K2_ASM(TILE_A1 TILE_A2 PV0Z_ TILE_A3, kf[1], qf, vf[0][0], vf[0][0]); \
      SPL(1); K2_ASM(TILE_B1 PVPREV_ TILE_B2 PV0_ TILE_B3, kf[2], qf, vf[0][1], vf[1][0]); \
      SPL(2); K2_ASM(TILE_A1 PVPREV_ TILE_A2 PV0_ TILE_A3, kf[3], qf, vf[1][1], vf[2][0]); \
      SPL(3); K2_ASM(TILE_B1 PVPREV_ TILE_B2 PV0_ TILE_B3, kf[4], qf, vf[2][1], vf[3][0]); \
      SPL(4); K2_ASM(TILE_A1 PVPREV_ TILE_A2 PV0_ TILE_A3, kf[5], qf, vf[3][1], vf[4][0]); \
      SPL(5); K2_ASM(TILE_B1 PVPREV_ TILE_B2 PV0_ TILE_B3, kf[6], qf, vf[4][1], vf[5][0]); \
      SPL(6); K2_ASM(TILE_A1 PVPREV_ TILE_A2 PV0_ TILE_A3, kf[7], qf, vf[5][1], vf[6][0]); \
      SPL(7); \
    } else { K2_ASM(TILE_A1 PVPREV_ TILE_A2, kf[1], qf, vf[7][1], vf[0][0]); K2_WRITE_PARTIAL((g) - 1); K2_ASM_MID(); } \
    if (slow) K2_LOAD_NEGM(((g) + 1) & 7); \
    K2_ASM(TILE_B1 PVPREV_ TILE_B2 PV0_ TILE_B3, kf[0], qn, vf[6][1], vf[7][0]); \
    qf = qn; } while (0)

  asm volatile("s_nop 15" : "+{v[168:183]}"(sa));
  K2_ITER(0, true);
  VMWAIT0();
#pragma unroll
  for (int g = 1; g < 8; ++g) {
    if (g <= 5 && w < 4) {
      const int ln = ((g - 1) * 128 + (blockIdx.x >> 3) * 4 + w) * 64 + lane;
      const char* src = ln < 11008 ? (const char*)wd + ln * 128 : ln < 15104 ? ws + WS_WO + (ln - 11008) * 128 : ws + WS_WGU + (ln < 37120 ? ln - 15104 : 0) * 128;
      glds4(src, lds0 + K2_JUNK);
    }
    if (g == 7 && w == 0 && blockIdx.x < 32) {
      const char *c0 = CODE_ADDR(k4_down), *c1 = CODE_ADDR(k_end_marker);
      const char* src = c0 + ((blockIdx.x >> 3) * 64 + lane) * 128;
      glds4(src < c1 ? src : c0, lds0 + K2_JUNK);
    }
    K2_ITER(g, false);
  }
  K2_ASM("s_nop 1\n\t" PVPREV_ "s_nop 15", kf[0], qf, vf[7][1], vf[7][0]);
  K2_WRITE_PARTIAL(7);
  if (w >= 4) return;
  asm volatile("s_waitcnt vmcnt(0)" ::: "memory");
  VMWAIT0();
  const int g4 = lane >> 4, tok8 = lane & 7;
  const char* wsrc = ws + WS_WO + (size_t)w * 16384 + lane * 16;
  bf16x8 wr[5][8];
#define K2_WLOAD(hh) do { if ((hh) < 16) { _Pragma("unroll") for (int i_ = 0; i_ < 8; ++i_) wr[(hh) % 5][i_] = *(const bf16x8*)(wsrc + (size_t)((hh) >> 1) * 65536 + ((hh) & 1) * 8192 + i_ * 1024); } } while (0)
  f32x4 oacc[8];
  const float piv = __uint_as_float((unsigned)x[(size_t)(g8 * 8 + tok8) * DM + w * 128] << 16);
  {
    u32x4 xr[4];
#pragma unroll
    for (int r = 0; r < 4; ++r) xr[r] = *(const u32x4*)(x + (size_t)(g8 * 8 + tok8) * DM + w * 128 + g4 * 32 + r * 8);
    K2_WLOAD(0); K2_WLOAD(1);
#pragma unroll
    for (int r = 0; r < 4; ++r)
#pragma unroll
      for (int i = 0; i < 4; ++i) { oacc[2 * i][r] = bflo(xr[r][i]); oacc[2 * i + 1][r] = bfhi(xr[r][i]); }
  }
#define K2_GAP(g, f) do { if ((g) < 7 && ((f) & 1)) __builtin_amdgcn_s_sleep(1); } while (0)
#define K2_PROJ(g) do { \
    while (!__all(LDS_VU32(lds + K2_FLG + 32 + ((g) * 8 + tok8) * 4) != 0u)) __builtin_amdgcn_s_sleep((g) < 7 ? 2 : 1); \
    asm volatile("" ::: "memory"); \
    { const lds_ptr rp_ = lds + K2_RED + (g) * (8 * K2_PSZ) + (w >> 1) * 1024 + lane * 16 + (w & 1) * 8, lp_ = lds + K2_RED + (g) * (8 * K2_PSZ) + 2048 + r32 * 4; \
      f32x2 t_[8]; float l_[8]; \
      _Pragma("unroll") for (int i_ = 0; i_ < 8; ++i_) { t_[i_] = LDS_F32X2(rp_ + i_ * K2_PSZ); l_[i_] = LDS_F32(lp_ + i_ * K2_PSZ); } \
      SB(); K2_WLOAD(2 * (g) + 2); SB(); \
      const f32x2 ts_ = ((t_[0] + t_[1]) + (t_[2] + t_[3])) + ((t_[4] + t_[5]) + (t_[6] + t_[7])); \
      const float inv_ = __builtin_amdgcn_rcpf(((l_[0] + l_[1]) + (l_[2] + l_[3])) + ((l_[4] + l_[5]) + (l_[6] + l_[7]))); \
      LDS_VU32(lds + K2_OB + ((g) & 1) * 1152 + (r32 & 7) * 144 + (r32 >> 3) * 32 + h * 8 + (w >> 1) * 16 + (w & 1) * 4) = pk2(ts_[0] * inv_, ts_[1] * inv_); \
      LDS_VU32(lds + K2_FLG + 288 + ((g) * 4 + w) * 4) = 1u; } \
    SB(); K2_WLOAD(2 * (g) + 3); SB(); \
    while (!__all(LDS_VU32(lds + K2_FLG + 288 + ((g) * 4 + (lane & 3)) * 4) != 0u)) __builtin_amdgcn_s_sleep(1); \
    asm volatile("" ::: "memory"); \
    const bf16x8 b0_ = lds_b128(lds + K2_OB + ((g) & 1) * 1152 + tok8 * 144 + g4 * 16), b1_ = lds_b128(lds + K2_OB + ((g) & 1) * 1152 + tok8 * 144 + 64 + g4 * 16); \
    _Pragma("unroll") for (int f_ = 0; f_ < 8; ++f_) { oacc[f_] = MFMA16(wr[(2 * (g)) % 5][f_], b0_, oacc[f_]); PINM(oacc[f_]); K2_GAP(g, f_); } \
    _Pragma("unroll") for (int f_ = 0; f_ < 8; ++f_) { oacc[f_] = MFMA16(wr[(2 * (g) + 1) % 5][f_], b1_, oacc[f_]); PINM(oacc[f_]); K2_GAP(g, f_); } } while (0)
  K2_PROJ(0); K2_PROJ(1); K2_PROJ(2); K2_PROJ(3); K2_PROJ(4); K2_PROJ(5); K2_PROJ(6); K2_PROJ(7);
  {
    const int tok = g8 * 8 + tok8;
    u32x4 hb[4];
#pragma unroll
    for (int r = 0; r < 4; ++r)
#pragma unroll
      for (int i = 0; i < 4; ++i) hb[r][i] = pk2(oacc[2 * i][r], oacc[2 * i + 1][r]);
    if ((lane & 8) == 0) {
#pragma unroll
      for (int r = 0; r < 4; ++r) *(u32x4*)(ws + WS_H + (size_t)(tok >> 5) * 32768 + (w * 16 + g4 * 4 + r) * 512 + (tok & 31) * 16) = hb[r];
    }
    float sx = 0.f, sxx = 0.f;
#pragma unroll
    for (int r = 0; r < 4; ++r)
#pragma unroll
      for (int i = 0; i < 4; ++i) { sx = dot2(hb[r][i], ONES, sx); sxx = dot2(hb[r][i], hb[r][i], sxx); }
    const float d1 = swap32_sum(swap16_sum(sx - 32.f * piv)), d2 = swap32_sum(swap16_sum(__builtin_fmaf(-2.f * piv, sx, sxx) + 32.f * piv * piv));
    f32x2 st; st[0] = piv + d1 * (1.f / 128); st[1] = fmaxf(d2 - d1 * d1 * (1.f / 128), 0.f);
    if (lane < 8) *(f32x2*)(ws + WS_ST + ((size_t)((tok >> 5) * 4 + w) * 32 + (tok & 31)) * 8) = st;
  }
}

constexpr int K4_SLOT = 32768, K4_LDS = 5 * K4_SLOT;

extern "C" __global__ __launch_bounds__(512) void k4_down(const u16* __restrict__ W, char* __restrict__ ws, u16* __restrict__ out) {
  constexpr int NKS = NAT;
  extern __shared__ __attribute__((aligned(16))) char smem[];
  const lds_ptr lds = (lds_ptr)smem;
  const unsigned lds0 = (unsigned)(uintptr_t)smem;
  const int tid = threadIdx.x, lane = tid & 63;
  const int w = (int)rfl(tid >> 6);
  const int bid = blockIdx.x;
  const int tb = (bid & 7) * 4 + ((bid >> 3) & 3), nb = bid >> 5;
  const int r32 = lane & 31, h = lane >> 5;
  if (w >= 4) {
    const int lw = w - 4;
    const int rr = lane >> 3, pos = lane & 7;
    const char* wcol = (const char*)W + nb * 128 + (pos ^ (((rr >> 1) & 1) << 2)) * 16;
    const char* wsrc = wcol + (size_t)(64 * (lw >> 1) + 8 * rr + 4 * (lw & 1)) * 1024;
    const char* wlast = wcol + (size_t)(1344 + 8 * (rr & 3) + (rr >> 2)) * 1024;
    const char* asrc = ws + WS_ACT + ((size_t)(tb * 2 + (lw >> 1)) * NKS + (lw & 1) * 4) * 1024 + lane * 16;
#define K4_ISSUE(st) do { const unsigned sl_ = lds0 + ((st) % 5) * K4_SLOT; _Pragma("unroll") for (int i = 0; i < 4; ++i) { \
    const bool oka_ = (st) < 10 || !((lw & 1) && i >= 2); \
    glds16(asrc + (oka_ ? (st) * 8192 + i * 1024 : 0), sl_ + 16384 + (lw * 4 + i) * 1024); \
    glds16(((st) < 10 || lw < 2) ? wsrc + (size_t)(st) * 131072 + i * 1024 : wlast + i * 2048, sl_ + (lw * 4 + i) * 1024); } } while (0)
    {
      K4_ISSUE(0); K4_ISSUE(1); K4_ISSUE(2);
      WAIT_VM(16); RAWBAR(); K4_ISSUE(3); K4_ISSUE(4);
      WAIT_VM(24); RAWBAR(); K4_ISSUE(5);
      WAIT_VM(24); RAWBAR(); K4_ISSUE(6);
      WAIT_VM(24); RAWBAR(); K4_ISSUE(7);
      WAIT_VM(24); RAWBAR(); K4_ISSUE(8);
      WAIT_VM(24); RAWBAR(); K4_ISSUE(9);
      WAIT_VM(24); RAWBAR(); K4_ISSUE(10);
      WAIT_VM(24); RAWBAR();
      WAIT_VM(16); RAWBAR();
      WAIT_VM(8); RAWBAR();
      WAIT_VM(0); RAWBAR();
    }
    return;
  }
  const int nt = w & 1, tt = w >> 1;
  const int tok = tb * 64 + tt * 32 + r32;
  const int ncol = nb * 64 + nt * 32 + h * 16;
  const int blk = (lane >> 4) & 1, q = (lane & 15) >> 2, p = lane & 3;
  const char* hfrag = ws + WS_H + (size_t)(tok >> 5) * 32768 + (ncol >> 3) * 512 + r32 * 16;
  const u32x4 h0 = *(const u32x4*)(hfrag), h1 = *(const u32x4*)(hfrag + 512);
  f32x16 acc = {};
  const int cbyte = nt * 64 + (16 * (p & 1) + 8 * blk + 4 * (p >> 1)) * 2;
  lds_ptr ap[3], bp[3];
#pragma unroll
  for (int u = 0; u < 3; ++u) { ap[u] = lds + u * 2 * K4_SLOT + (4 * h + q) * 128 + (cbyte ^ ((q >> 1) << 6)); bp[u] = lds + u * 2 * K4_SLOT + 16384 + tt * 8192 + lane * 16; }
  s16x4 fa[2][4][2]; bf16x8 fb_[2][4];
#define K4_U(st) (((st) % 5) >> 1)
#define K4_O(st) ((((st) % 5) & 1) * K4_SLOT)
#define K4_LD1(bu, st, k0, k) do { fa[bu][k][0] = lds_tr(ap[K4_U(st)] + K4_O(st) + ((k0) + (k)) * 2048); fa[bu][k][1] = lds_tr(ap[K4_U(st)] + K4_O(st) + ((k0) + (k)) * 2048 + 1024); \
    fb_[bu][k] = lds_b128(bp[K4_U(st)] + K4_O(st) + ((k0) + (k)) * 1024); } while (0)
#define K4_STEP(bu, NM, NL, st, k0) do { _Pragma("unroll") for (int k = 0; k < 4; ++k) { \
    if (k < (NM)) { acc = MFMA32(cat8(fa[bu][k][0], fa[bu][k][1]), fb_[bu][k], acc); PINM(acc); } \
    if (k < (NL)) K4_LD1(1 - (bu), st, k0, k); SB(); } } while (0)
#define K4_STAGE(st, NK2) do { BAR(); K4_STEP(1, (st) > 0 ? 4 : 0, 4, st, 0); K4_STEP(0, 4, NK2, st, 4); } while (0)
  K4_STAGE(0, 4); K4_STAGE(1, 4); K4_STAGE(2, 4); K4_STAGE(3, 4);
  K4_STAGE(4, 4); K4_STAGE(5, 4); K4_STAGE(6, 4); K4_STAGE(7, 4); K4_STAGE(8, 4); K4_STAGE(9, 4); K4_STAGE(10, 2);
  K4_STEP(1, 2, 0, 0, 0);
  u32x4 o0, o1;
#pragma unroll
  for (int i = 0; i < 4; ++i) {
    o0[i] = pk2(acc[2 * i] + bflo(h0[i]), acc[2 * i + 1] + bfhi(h0[i]));
    o1[i] = pk2(acc[8 + 2 * i] + bflo(h1[i]), acc[9 + 2 * i] + bfhi(h1[i]));
  }
  u16* dst = out + (size_t)tok * DM + ncol;
  *(u32x4*)dst = o0;
  *(u32x4*)(dst + 8) = o1;
}

constexpr int K3_SLOT = 12288, K3_FLG = 13 * K3_SLOT, K3_SC = K3_FLG + 64, K3_LDS = K3_SC + 768;

extern "C" __global__ __launch_bounds__(512) void k3_gateup(const u16* __restrict__ wd, char* __restrict__ ws) {
  extern __shared__ __attribute__((aligned(16))) char smem[];
  const lds_ptr lds = (lds_ptr)smem;
  const unsigned lds0 = (unsigned)(uintptr_t)smem;
  const int tid = threadIdx.x, lane = tid & 63;
  const int w = (int)rfl(tid >> 6);
  const int bid = blockIdx.x, jx = bid >> 3;
  if (jx >= 30) {
    const char* src = (const char*)wd + (size_t)(((jx & 1) * 8 + w) * 64 + lane) * 128;
#pragma unroll
    for (int i = 0; i < 6; ++i) glds4(src + i * 131072, lds0 + w * 256);
    return;
  }
  const int rb = (bid & 7) * 2 + (jx & 1), cb = jx >> 1;
  const int at0 = cb * 6 > 80 ? 80 : cb * 6;
  if (w >= 4) {
    const int lw = w - 4;
    const char* wsrc[3];
#pragma unroll
    for (int i = 0; i < 3; ++i) wsrc[i] = ws + WS_WGU + (size_t)(at0 + ((lw * 3 + i) >> 1)) * 32768 + ((lw * 3 + i) & 1) * 1024 + lane * 16;
#define K3_ISSUE(st) do { if ((st) >= 13) WAIT_CNT(lds + K3_FLG + 16, (st) - 12); \
    _Pragma("unroll") for (int i = 0; i < 3; ++i) glds16(wsrc[i] + (st) * 2048, lds0 + ((st) % 13) * K3_SLOT + (lw * 3 + i) * 1024); } while (0)
#define K3_PUB(n) LDS_VU32(lds + K3_FLG + lw * 4) = (n)
    K3_ISSUE(0); K3_ISSUE(1);
    RAWBAR();

    K3_ISSUE(2); WAIT_VM(6); K3_PUB(1);
    K3_ISSUE(3); WAIT_VM(6); K3_PUB(2);
    K3_ISSUE(4); WAIT_VM(6); K3_PUB(3);
    K3_ISSUE(5); WAIT_VM(6); K3_PUB(4);
    K3_ISSUE(6); WAIT_VM(6); K3_PUB(5);
    K3_ISSUE(7); WAIT_VM(6); K3_PUB(6);
    K3_ISSUE(8); WAIT_VM(6); K3_PUB(7);
    K3_ISSUE(9); WAIT_VM(6); K3_PUB(8);
    K3_ISSUE(10); WAIT_VM(6); K3_PUB(9);
    K3_ISSUE(11); WAIT_VM(6); K3_PUB(10);
    K3_ISSUE(12); WAIT_VM(6); K3_PUB(11);
    K3_ISSUE(13); WAIT_VM(6); K3_PUB(12);
    K3_ISSUE(14); WAIT_VM(6); K3_PUB(13);
    K3_ISSUE(15); WAIT_VM(6); K3_PUB(14);
    WAIT_VM(3); K3_PUB(15);
    WAIT_VM(0); K3_PUB(16);
    if (lw < 3) {
      const int i = lw * 64 + lane;
      const float* cp = (const float*)(ws + WS_CS) + ((i >> 4) & 1) * (8 * 1408) + ffeat(at0 + (i >> 5), i & 15);
      float c[8];
#pragma unroll
      for (int j = 0; j < 8; ++j) c[j] = cp[j * 1408];
      LDS_F32(lds + K3_SC + i * 4) = ((c[0] + c[1]) + (c[2] + c[3])) + ((c[4] + c[5]) + (c[6] + c[7]));
    }
    BAR();
    return;
  }
  const int r32 = lane & 31, h = lane >> 5;
  const char* bsrc = ws + WS_H + (size_t)(rb * 4 + w) * 32768 + lane * 16;
  constexpr int K3_BD = 8;
  bf16x8 fb_[K3_BD];
#define K3_LDB(kk) fb_[(kk) % K3_BD] = *(const bf16x8*)(bsrc + (kk) * 1024)
#pragma unroll
  for (int i = 0; i < 4; ++i) K3_LDB(i);
  if (tid < 8) LDS_VU32(lds + K3_FLG + tid * 4) = 0u;
  BAR();
#pragma unroll
  for (int i = 4; i < K3_BD; ++i) K3_LDB(i);
  f32x16 acc[6] = {};
  lds_ptr ap[3];
#pragma unroll
  for (int u = 0; u < 3; ++u) { ap[u] = lds + u * 5 * K3_SLOT + lane * 16; PIN(ap[u]); }
  bf16x8 fa[2][6];
  u32x4 rdy;
#define K3_A(kk, i) lds_b128(ap[(((kk) >> 1) % 13) / 5] + ((((kk) >> 1) % 13) % 5) * K3_SLOT + (i) * 2048 + ((kk) & 1) * 1024)
#define K3_STEP(kk) do { constexpr int n_ = (kk) + 2; \
    if (n_ < 32 && (n_ & 1) == 0 && __builtin_expect(rfl(min4(rdy)) < n_ / 2u + 1u, 0)) WAIT_CNT(lds + K3_FLG, n_ / 2 + 1); \
    _Pragma("unroll") for (int i = 0; i < 6; ++i) { \
      acc[i] = MFMA32(fa[(kk) & 1][i], fb_[(kk) % K3_BD], acc[i]); PINM(acc[i]); \
      if (i == 0 && (kk) > 0 && (kk) - 1 + K3_BD < 32) K3_LDB((kk) - 1 + K3_BD); \
      if (n_ < 32) fa[(kk) & 1][i] = K3_A(n_, i); \
      if ((n_ & 1) == 1 && i == 2) rdy = LDS_VU32X4(lds + K3_FLG); \
      SB(); } \
    if (((kk) & 1) == 1 && (kk) < 6) LDS_VU32(lds + K3_FLG + 16 + w * 4) = ((kk) + 1) / 2;   } while (0)
#define K3_STEP4(kk) do { K3_STEP(kk); K3_STEP((kk) + 1); K3_STEP((kk) + 2); K3_STEP((kk) + 3); } while (0)
  WAIT_CNT(lds + K3_FLG, 1);
#pragma unroll
  for (int i = 0; i < 6; ++i) { fa[0][i] = K3_A(0, i); fa[1][i] = K3_A(1, i); }
  rdy = LDS_VU32X4(lds + K3_FLG);
  K3_STEP4(0); K3_STEP4(4);
  const f32x2 lp0 = *(const f32x2*)(ws + WS_ST + ((size_t)((rb * 4 + w) * 4 + h * 2) * 32 + r32) * 8), lp1 = *(const f32x2*)(ws + WS_ST + ((size_t)((rb * 4 + w) * 4 + h * 2 + 1) * 32 + r32) * 8);
  K3_STEP4(8); K3_STEP4(12); K3_STEP4(16); K3_STEP4(20); K3_STEP4(24);
  float mean = swap32_sum(lp0[0] + lp1[0]) * 0.25f;
  float rstd = ln_rstd(swap32_sum(lp0[1] + lp1[1] + 128.f * ((lp0[0] - mean) * (lp0[0] - mean) + (lp1[0] - mean) * (lp1[0] - mean))) * (1.f / DM));
  mean = rbf(mean);
  PIN(mean); PIN(rstd);
  BAR();
  K3_STEP4(28);
  const float ke = -1.4426950408889634f * rstd, ir2 = __builtin_amdgcn_rcpf(rstd * rstd);
  const f32x2 nm2 = {-mean, -mean}, ke2 = {ke, ke}, ir22 = {ir2, ir2};
  f32x4 call[6][4];
#pragma unroll
  for (int i = 0; i < 6; ++i)
#pragma unroll
    for (int g = 0; g < 4; ++g) call[i][g] = LDS_F32X4(lds + K3_SC + i * 128 + (8 * g + 4 * h) * 4);
  SB();
#pragma unroll
  for (int i = 0; i < 6; ++i) {
    const f32x4 (&c4)[4] = call[i];
    f32x2 tg[4], tu[4], e[4];
#pragma unroll
    for (int j = 0; j < 4; ++j) {
      const f32x2 ag2 = {acc[i][2 * j], acc[i][2 * j + 1]}, au2 = {acc[i][8 + 2 * j], acc[i][9 + 2 * j]};
      const f32x2 cg2 = {c4[j >> 1][2 * (j & 1)], c4[j >> 1][2 * (j & 1) + 1]}, cu2 = {c4[2 + (j >> 1)][2 * (j & 1)], c4[2 + (j >> 1)][2 * (j & 1) + 1]};
      tg[j] = __builtin_elementwise_fma(nm2, cg2, ag2); tu[j] = __builtin_elementwise_fma(nm2, cu2, au2);
      e[j] = tg[j] * ke2;
    }
#pragma unroll
    for (int j = 0; j < 4; ++j) { e[j][0] = __builtin_amdgcn_exp2f(e[j][0]); e[j][1] = __builtin_amdgcn_exp2f(e[j][1]); tg[j] = tg[j] * tu[j]; }
#pragma unroll
    for (int j = 0; j < 4; ++j) e[j] = __builtin_elementwise_fma(e[j], ir22, ir22);
#pragma unroll
    for (int j = 0; j < 4; ++j) { e[j][0] = __builtin_amdgcn_rcpf(e[j][0]); e[j][1] = __builtin_amdgcn_rcpf(e[j][1]); }
    u32x4 pk;
#pragma unroll
    for (int j = 0; j < 4; ++j) { const f32x2 a2 = tg[j] * e[j]; pk[j] = pk2(a2[0], a2[1]); }
    *(u32x4*)(ws + WS_ACT + ((size_t)(rb * 4 + w) * NAT + at0 + i) * 1024 + lane * 16) = pk;
  }
}

extern "C" __global__ void k_end_marker() {}

extern "C" void kernel_launch(void* const* d_in, const int* in_sizes, int n_in, void* d_out, int out_size, void* d_ws, size_t ws_size,
                              hipStream_t stream) {
  const u16* x = (const u16*)d_in[0];
  const u16* wq = (const u16*)d_in[1];
  const u16* wk = (const u16*)d_in[2];
  const u16* wv = (const u16*)d_in[3];
  const u16* wo = (const u16*)d_in[4];
  const u16* wg = (const u16*)d_in[6];
  const u16* wu = (const u16*)d_in[7];
  const u16* wd = (const u16*)d_in[8];
  char* ws = (char*)d_ws;
  (void)hipFuncSetAttribute((const void*)k1_qkv, hipFuncAttributeMaxDynamicSharedMemorySize, K1_LDS);
  (void)hipFuncSetAttribute((const void*)k2_attn, hipFuncAttributeMaxDynamicSharedMemorySize, K2_LDS);
  (void)hipFuncSetAttribute((const void*)k3_gateup, hipFuncAttributeMaxDynamicSharedMemorySize, K3_LDS);
  (void)hipFuncSetAttribute((const void*)k4_down, hipFuncAttributeMaxDynamicSharedMemorySize, K4_LDS);
  k1_qkv<<<256, 640, K1_LDS, stream>>>(x, wq, wk, wv, wo, wg, wu, ws);
  k2_attn<<<256, 512, K2_LDS, stream>>>(x, wd, ws);
  k3_gateup<<<256, 512, K3_LDS, stream>>>(wd, ws);
  k4_down<<<256, 512, K4_LDS, stream>>>(wd, ws, (u16*)d_out);
}
```

```cpp
#define SEL -1
#include <hip/hip_runtime.h>
#include <hip/hip_bf16.h>
#include <stdint.h>

typedef __attribute__((ext_vector_type(8))) short bf16x8;
typedef __attribute__((ext_vector_type(4))) short s16x4;
typedef __attribute__((ext_vector_type(2))) __bf16 bf16x2;
typedef __attribute__((ext_vector_type(4))) unsigned u32x4;
typedef __attribute__((ext_vector_type(2))) unsigned u32x2;
typedef __attribute__((ext_vector_type(16))) float f32x16;
typedef __attribute__((ext_vector_type(4))) float f32x4;
typedef __attribute__((ext_vector_type(2))) float f32x2;
typedef __attribute__((address_space(3))) char* lds_ptr;
typedef unsigned short u16;

#define DI __device__ __forceinline__
#define MFMA32(a, b, c) __builtin_amdgcn_mfma_f32_32x32x16_bf16((a), (b), (c), 0, 0, 0)
#define MFMA16(a, b, c) __builtin_amdgcn_mfma_f32_16x16x32_bf16((a), (b), (c), 0, 0, 0)
#define WAIT_VM_BAR(N) asm volatile("s_waitcnt vmcnt(" #N ") lgkmcnt(0)\n\ts_barrier" ::: "memory")
#define BAR() asm volatile("s_waitcnt lgkmcnt(0)\n\ts_barrier" ::: "memory")

constexpr int SEQ = 2048, DM = 512, FF = 1376, NAT = 86;
constexpr float QSCALE = 0.25f * 1.4426950408889634f;
constexpr float LN_EPS = 1e-5f;

constexpr size_t WS_Q = 0;
constexpr size_t WS_K = WS_Q + (size_t)SEQ * DM * 2;
constexpr size_t WS_V = WS_K + 65536;
constexpr size_t WS_KMAX = WS_V + 65536;
constexpr size_t WS_WO = WS_KMAX + 4096;
constexpr size_t WS_H = WS_WO + (size_t)DM * DM * 2;
constexpr size_t WS_ST = WS_H + (size_t)SEQ * DM * 2;
constexpr size_t WS_CS = WS_ST + (size_t)SEQ * 16 * 8;
constexpr size_t WS_WGU = WS_CS + 2 * 8 * 1408 * 4;
constexpr size_t WS_ACT = WS_WGU + (size_t)NAT * 32768;

DI void glds16(const void* g, unsigned lds_base) {
  unsigned sv;
  asm volatile("s_mov_b32 %0, m0\n\ts_mov_b32 m0, %2\n\ts_nop 0\n\tglobal_load_lds_dwordx4 %1, off\n\ts_mov_b32 m0, %0"
               : "=&s"(sv) : "v"(g), "s"(lds_base) : "memory");
}
DI void glds4(const void* g, unsigned lds_base) {
  unsigned sv;
  asm volatile("s_mov_b32 %0, m0\n\ts_mov_b32 m0, %2\n\ts_nop 0\n\tglobal_load_lds_dword %1, off\n\ts_mov_b32 m0, %0"
               : "=&s"(sv) : "v"(g), "s"(lds_base) : "memory");
}
#define CODE_ADDR(sym) ({ unsigned lo_, hi_; asm volatile("s_getpc_b64 s[40:41]\n\ts_add_u32 %0, s40, " #sym "@rel32@lo+4\n\ts_addc_u32 %1, s41, " #sym "@rel32@hi+12" \
    : "=s"(lo_), "=s"(hi_) :: "s40", "s41", "scc"); (const char*)(((unsigned long long)hi_ << 32) | lo_); })
DI unsigned rfl(unsigned v) { return (unsigned)__builtin_amdgcn_readfirstlane((int)v); }
DI unsigned pk2(float lo, float hi) {
  bf16x2 v; v[0] = (__bf16)lo; v[1] = (__bf16)hi;
  return __builtin_bit_cast(unsigned, v);
}
DI int ffeat(int at, int rho) { return at < 84 ? 64 * (at >> 2) + 8 * (rho & 7) + 2 * (at & 3) + (rho >> 3) : 1344 + 8 * (rho & 3) + 4 * (at - 84) + (rho >> 2); }
DI void st16_wt(void* p, u32x4 v) { asm volatile("global_store_dwordx4 %0, %1, off sc1\n\ts_nop 1" :: "v"(p), "v"(v) : "memory"); }
DI float rbf(float v) { return (float)(__bf16)v; }
DI float ln_rstd(float var) { return __builtin_amdgcn_rcpf(rbf(__builtin_amdgcn_sqrtf(rbf(rbf(var) + LN_EPS)))); }
DI float bflo(unsigned u) { return __uint_as_float(u << 16); }
DI float bfhi(unsigned u) { return __uint_as_float(u & 0xffff0000u); }
DI float dot2(unsigned a, unsigned b, float c) {
  return __builtin_amdgcn_fdot2_f32_bf16(__builtin_bit_cast(bf16x2, a), __builtin_bit_cast(bf16x2, b), c, false);
}
DI bf16x8 lds_b128(lds_ptr p) { return *(const __attribute__((address_space(3))) bf16x8*)p; }
DI s16x4 lds_tr(lds_ptr p) {
  typedef short v4i16_t __attribute__((ext_vector_type(4)));
  return __builtin_bit_cast(s16x4, __builtin_amdgcn_ds_read_tr16_b64_v4i16((__attribute__((address_space(3))) v4i16_t*)p));
}
DI float swap32_sum(float v) {
  auto r = __builtin_amdgcn_permlane32_swap(__float_as_uint(v), __float_as_uint(v), false, false);
  return __uint_as_float(r[0]) + __uint_as_float(r[1]);
}
DI float swap16_sum(float v) {
  auto r = __builtin_amdgcn_permlane16_swap(__float_as_uint(v), __float_as_uint(v), false, false);
  return __uint_as_float(r[0]) + __uint_as_float(r[1]);
}
DI float swap32_max(float v) {
  auto r = __builtin_amdgcn_permlane32_swap(__float_as_uint(v), __float_as_uint(v), false, false);
  return fmaxf(__uint_as_float(r[0]), __uint_as_float(r[1]));
}
template <int CTRL, int RM>
DI float dpp(float old, float v) { return __uint_as_float((unsigned)__builtin_amdgcn_update_dpp((int)__float_as_uint(old), (int)__float_as_uint(v), CTRL, RM, 0xf, false)); }
DI float wave_sum(float v) {
  v += dpp<0x111, 0xf>(0.f, v); v += dpp<0x112, 0xf>(0.f, v); v += dpp<0x114, 0xf>(0.f, v); v += dpp<0x118, 0xf>(0.f, v);
  v += dpp<0x142, 0xa>(0.f, v); v += dpp<0x143, 0xc>(0.f, v);
  return __uint_as_float((unsigned)__builtin_amdgcn_readlane((int)__float_as_uint(v), 63));
}
DI float wave_max(float v) {
  v = fmaxf(v, dpp<0x111, 0xf>(0.f, v)); v = fmaxf(v, dpp<0x112, 0xf>(0.f, v)); v = fmaxf(v, dpp<0x114, 0xf>(0.f, v)); v = fmaxf(v, dpp<0x118, 0xf>(0.f, v));
  v = fmaxf(v, dpp<0x142, 0xa>(0.f, v)); v = fmaxf(v, dpp<0x143, 0xc>(0.f, v));
  return __uint_as_float((unsigned)__builtin_amdgcn_readlane((int)__float_as_uint(v), 63));
}
DI float quad_sum(float v) { v += dpp<0xb1, 0xf>(0.f, v); v += dpp<0x4e, 0xf>(0.f, v); return v; }

#define SB() __builtin_amdgcn_sched_barrier(0)
#define PIN(x) asm volatile("" : "+v"(x))
#define PINM(x) asm volatile("" : "+v"(x) :: "memory")
#define LDS_F32X4(p) (*(__attribute__((address_space(3))) f32x4*)(p))
#define LDS_F32X2(p) (*(__attribute__((address_space(3))) f32x2*)(p))
#define LDS_F32(p) (*(__attribute__((address_space(3))) float*)(p))
#define LDS_U32X2(p) (*(__attribute__((address_space(3))) u32x2*)(p))
#define LDS_U32X4(p) (*(__attribute__((address_space(3))) u32x4*)(p))
#define LDS_VU32(p) (*(volatile __attribute__((address_space(3))) unsigned*)(p))
#define WAIT_VM(N) asm volatile("s_waitcnt vmcnt(" #N ")" ::: "memory")
#define RAWBAR() asm volatile("s_barrier" ::: "memory")
#define LGKM0() asm volatile("s_waitcnt lgkmcnt(0)" ::: "memory")
DI bf16x8 cat8(s16x4 a, s16x4 b) { return __builtin_shufflevector(a, b, 0, 1, 2, 3, 4, 5, 6, 7); }
constexpr unsigned ONES = 0x3f803f80u;
#define LDS_VU32X4(p) (*(volatile __attribute__((address_space(3))) u32x4*)(p))
DI unsigned min4(u32x4 v) { return min(min(v[0], v[1]), min(v[2], v[3])); }
#define WAIT_CNT(p, n) do { while (rfl(min4(LDS_VU32X4(p))) < (unsigned)(n)) __builtin_amdgcn_s_sleep(1); asm volatile("" ::: "memory"); } while (0)

constexpr int K1_X = 0, K1_W = 65536, K1_KV = 131072, K1_SC = 147456, K1_FLG = K1_SC + 2048 + 8192, K1_JUNK = K1_FLG + 32, K1_LDS = K1_JUNK + 256;

extern "C" __global__ __launch_bounds__(640) void k1_qkv(const u16* __restrict__ x, const u16* __restrict__ wq, const u16* __restrict__ wk,
                                              const u16* __restrict__ wv, const u16* __restrict__ wo, const u16* __restrict__ wg,
                                              const u16* __restrict__ wu, char* __restrict__ ws) {
  extern __shared__ __attribute__((aligned(16))) char smem[];
  const lds_ptr lds = (lds_ptr)smem;
  if (blockIdx.x == 0 && threadIdx.x == 0) *(unsigned long long*)(ws + ((size_t)64 << 20) + 0 * 8) = __builtin_amdgcn_s_memrealtime();
  const unsigned lds0 = (unsigned)(uintptr_t)smem;
  const int tid = threadIdx.x, lane = tid & 63;
  const int w = (int)rfl(tid >> 6);
  if (w < 3) {
    const char *c0 = CODE_ADDR(k1_qkv), *c1 = CODE_ADDR(k2_attn);
    const char* src = c0 + tid * 128;
    glds4(src < c1 ? src : c0, lds0 + K1_JUNK);
  }
  const int bid = blockIdx.x;
  const int tb = (bid & 7) * 4 + ((bid >> 3) & 3), fb = bid >> 5;
  const bool kvwg = fb < 2;
  if (w >= 8) {
    RAWBAR();
    const int job = (w - 8) * 256 + (255 - bid);
    if (w == 9 && bid < 64) {
      const char *c0 = CODE_ADDR(k2_attn), *c1 = CODE_ADDR(k_end_marker);
      const char* src = c0 + ((bid >> 3) * 64 + lane) * 128;
      glds4(src < c1 ? src : c0, lds0 + K1_JUNK);
    }
    if (job < 416) {
        const bool isw = job < 64;
        const int jg = job - 64, mat = jg >= 176, jj = jg - mat * 176, ksl = jj & 7, cg = jj >> 3;
        const int col = cg * 64 + (lane & 7) * 8, kc = ksl * 8 + (lane >> 3);
        const u16* src = isw ? wo + (size_t)((job >> 3) * 64 + (job & 1) * 32 + 8 * (lane >> 4)) * DM + ((job >> 1) & 3) * 128 + (lane & 15) * 8
                             : (mat ? wu : wg) + (size_t)kc * 8 * FF + (col < FF ? col : 0);
        const int ld = isw ? DM : FF;
        WAIT_CNT(lds + K1_FLG, 2);
        u32x4 R[8];
#pragma unroll
        for (int j = 0; j < 8; ++j) R[j] = *(const u32x4*)(src + (size_t)j * ld);
        char* dst = isw ? ws + WS_WO + (size_t)job * 8192 + lane * 16
                        : ws + WS_WGU + ((size_t)(cg * 128 + (kc >> 1)) * 64 + (kc & 1) * 32 + mat * 16 + (lane & 7)) * 16;
        float c[8];
#pragma unroll
        for (int m = 0; m < 4; ++m) {
          u32x4 lo, hi;
#pragma unroll
          for (int i = 0; i < 4; ++i) { lo[i] = __builtin_amdgcn_perm(R[2 * i + 1][m], R[2 * i][m], 0x05040100u); hi[i] = __builtin_amdgcn_perm(R[2 * i + 1][m], R[2 * i][m], 0x07060302u); }
          const int o0 = isw ? 2 * m * 1024 : cg < 21 ? m * 32768 : (m >> 1) * 32768 + (m & 1) * 128, o1 = o0 + (isw ? 1024 : cg < 21 ? 128 : 64);
          if (isw || col < FF) { st16_wt(dst + o0, lo); st16_wt(dst + o1, hi); }
          c[2 * m] = dot2(lo[3], ONES, dot2(lo[2], ONES, dot2(lo[1], ONES, dot2(lo[0], ONES, 0.f))));
          c[2 * m + 1] = dot2(hi[3], ONES, dot2(hi[2], ONES, dot2(hi[1], ONES, dot2(hi[0], ONES, 0.f))));
        }
        if (!isw) {
#pragma unroll
          for (int i = 0; i < 8; ++i) { c[i] += dpp<0x128, 0xf>(0.f, c[i]); c[i] = swap32_sum(swap16_sum(c[i])); }
          if (lane < 8) {
            float* cd = (float*)(ws + WS_CS) + (mat * 8 + ksl) * 1408 + cg * 64 + lane * 8;
            *(f32x4*)cd = f32x4{c[0], c[1], c[2], c[3]};
            *(f32x4*)(cd + 4) = f32x4{c[4], c[5], c[6], c[7]};
          }
        }
    }
    return;
  }
  if (w >= 4) {
    const int lw = w - 4;
    const u16* xs = x + (size_t)(tb * 64 + lw * 16 + (lane >> 4)) * DM;
    const int xc = (lane & 15) ^ (lane >> 4);
    const int rr = lane >> 3, pos = lane & 7;
    const u16* wsrc = wq + (size_t)(lw * 32 + rr) * DM + fb * 64 + ((pos ^ (((rr >> 1) & 1) << 2)) * 8);
    const u16* kvs = (fb ? wv : wk) + lw * 512 + lane * 8;
#define K1_ISSUE(st, KV) do { _Pragma("unroll") for (int i = 0; i < 4; ++i) { \
      glds16(xs + (size_t)i * 4 * DM + (st) * 128 + ((xc ^ (4 * i)) * 8), lds0 + K1_X + (st) * 16384 + (lw * 4 + i) * 1024); \
      glds16(wsrc + (size_t)((st) * 128 + i * 8) * DM, lds0 + K1_W + (st) * 16384 + (lw * 4 + i) * 1024); } \
    if (KV) glds16(kvs + (st) * 2048, lds0 + K1_KV + (st) * 4096 + lw * 1024); } while (0)
#define K1_PUB(n) LDS_VU32(lds + K1_FLG + lw * 4) = (n)
    if (kvwg) {
      K1_ISSUE(0, 1); RAWBAR(); K1_ISSUE(1, 1);
      WAIT_VM(9); K1_PUB(1); K1_ISSUE(2, 1); WAIT_VM(9); K1_PUB(2); K1_ISSUE(3, 1); WAIT_VM(9); K1_PUB(3);
    } else {
      K1_ISSUE(0, 0); RAWBAR(); K1_ISSUE(1, 0);
      WAIT_VM(8); K1_PUB(1); K1_ISSUE(2, 0); WAIT_VM(8); K1_PUB(2); K1_ISSUE(3, 0); WAIT_VM(8); K1_PUB(3);
    }
    WAIT_VM(0); K1_PUB(4);
    return;
  }
  if (tid < 8) LDS_VU32(lds + K1_FLG + tid * 4) = 0u;
  BAR();
  const int r32 = lane & 31, h = lane >> 5;
  const int blk = (lane >> 4) & 1, q = (lane & 15) >> 2, p = lane & 3;
  const int tt = w & 1, ft = w >> 1;
  const int tok = tt * 32 + r32;
  lds_ptr xb[8];
#pragma unroll
  for (int i = 0; i < 8; ++i) xb[i] = lds + K1_X + tok * 256 + (((2 * i + h) ^ (tok & 15)) * 16);
  const lds_ptr wb = lds + K1_W + (8 * h + q) * 128 + (((ft * 4 + blk * 2 + (p >> 1)) ^ ((q >> 1) << 2)) * 16) + 8 * (p & 1);
  const lds_ptr kb = lds + K1_KV + (8 * h + q) * 32 + 8 * p;
  const float piv = __uint_as_float((unsigned)x[(size_t)(tb * 64 + tok) * DM] << 16);
  f32x16 acc = {}, acc2 = {};
  float cs = 0.f, cs2 = 0.f, sx = 0.f, sxx = 0.f, sxb = 0.f, sxxb = 0.f;
  s16x4 a[2][4][2]; bf16x8 b[2][4];
#define K1_STEP(bu, MM, LD, s0, sc0) do { _Pragma("unroll") for (int i = 0; i < 4; ++i) { const int s = (s0) + i; \
      if (MM) { acc = MFMA32(cat8(a[bu][i][0], a[bu][i][1]), b[bu][i], acc); PINM(acc); } \
      if (LD) { a[1 - (bu)][i][0] = lds_tr(wb + s * 2048); a[1 - (bu)][i][1] = lds_tr(wb + s * 2048 + 512); b[1 - (bu)][i] = lds_b128(xb[s & 7] + (s >> 3) * 16384); } SB(); } \
    if (MM) { \
      if (ft == (bu)) { \
        if (kvwg) { bf16x8 ka[4]; \
          _Pragma("unroll") for (int i = 0; i < 4; ++i) ka[i] = cat8(lds_tr(kb + ((sc0) + i) * 512), lds_tr(kb + ((sc0) + i) * 512 + 128)); \
          if (fb == 0) { _Pragma("unroll") for (int i = 0; i < 4; ++i) acc2 = MFMA32(ka[i], b[bu][i], acc2); } \
          else { _Pragma("unroll") for (int i = 0; i < 4; ++i) acc2 = MFMA32(b[bu][i], ka[i], acc2); } \
          PINM(acc2); SB(); \
          _Pragma("unroll") for (int i = 0; i < 4; ++i) { const u32x4 u = __builtin_bit_cast(u32x4, ka[i]); \
            cs2 = dot2(u[0], ONES, cs2); cs2 = dot2(u[1], ONES, cs2); cs2 = dot2(u[2], ONES, cs2); cs2 = dot2(u[3], ONES, cs2); } PIN(cs2); } \
        _Pragma("unroll") for (int i = 0; i < 4; ++i) { const u32x4 u = __builtin_bit_cast(u32x4, b[bu][i]); \
          sx = dot2(u[0], ONES, sx); sxx = dot2(u[0], u[0], sxx); sxb = dot2(u[1], ONES, sxb); sxxb = dot2(u[1], u[1], sxxb); \
          sx = dot2(u[2], ONES, sx); sxx = dot2(u[2], u[2], sxx); sxb = dot2(u[3], ONES, sxb); sxxb = dot2(u[3], u[3], sxxb); } \
        PIN(sx); PIN(sxx); PIN(sxb); PIN(sxxb); } \
      if (tt == (bu)) { _Pragma("unroll") for (int i = 0; i < 4; ++i) { const u32x4 u = __builtin_bit_cast(u32x4, cat8(a[bu][i][0], a[bu][i][1])); \
          cs = dot2(u[0], ONES, cs); cs = dot2(u[1], ONES, cs); cs = dot2(u[2], ONES, cs); cs = dot2(u[3], ONES, cs); } PIN(cs); } \
      SB(); } } while (0)
#pragma unroll
  for (int st = 0; st < 4; ++st) { WAIT_CNT(lds + K1_FLG, st + 1); K1_STEP(1, st > 0, true, st * 8, st * 8 - 4); K1_STEP(0, true, true, st * 8 + 4, st * 8); }
  K1_STEP(1, true, false, 0, 28);
  const lds_ptr sc = lds + K1_SC;
  {
    sx += sxb; sxx += sxxb;
    f32x2 d; d[0] = swap32_sum(sx - 128.f * piv); d[1] = swap32_sum(__builtin_fmaf(-2.f * piv, sx, sxx) + 128.f * piv * piv);
    cs = swap32_sum(cs); cs2 = swap32_sum(cs2);
    if (h == 0) { LDS_F32(sc + (ft * 2 + tt) * 128 + r32 * 4) = cs; LDS_F32X2(sc + 512 + (tt * 2 + ft) * 256 + r32 * 8) = d; LDS_F32(sc + 1536 + w * 128 + r32 * 4) = cs2; }
    if (kvwg && ft == 1) {
#pragma unroll
      for (int g = 0; g < 4; ++g) { f32x4 t; t[0] = acc2[4 * g]; t[1] = acc2[4 * g + 1]; t[2] = acc2[4 * g + 2]; t[3] = acc2[4 * g + 3]; LDS_F32X4(sc + 2048 + tt * 4096 + g * 1024 + lane * 16) = t; }
    }
  }
  asm volatile("" ::: "memory");
  LDS_VU32(lds + K1_FLG + 16 + w * 4) = 1u;
  WAIT_CNT(lds + K1_FLG + 16, 1);
  float mean, rstd;
  {
    const f32x2 d = LDS_F32X2(sc + 512 + tt * 512 + r32 * 8) + LDS_F32X2(sc + 512 + tt * 512 + 256 + r32 * 8);
    const float dmn = d[0] * (1.f / DM);
    mean = rbf(piv + dmn); rstd = ln_rstd(fmaxf(d[1] * (1.f / DM) - dmn * dmn, 0.f));
  }
  {
    const float scl = rstd * QSCALE;
    float o[16];
#pragma unroll
    for (int g = 0; g < 4; ++g) {
      const f32x4 c4 = LDS_F32X4(sc + ft * 256 + (8 * g + 4 * h) * 4) + LDS_F32X4(sc + ft * 256 + 128 + (8 * g + 4 * h) * 4);
#pragma unroll
      for (int j = 0; j < 4; ++j) o[4 * g + j] = (acc[4 * g + j] - mean * c4[j]) * scl;
    }
    u32x4 p0, p1;
#pragma unroll
    for (int i = 0; i < 4; ++i) { p0[i] = pk2(o[2 * i], o[2 * i + 1]); p1[i] = pk2(o[8 + 2 * i], o[9 + 2 * i]); }
    char* dst = ws + WS_Q + (size_t)(tb * 64 + tok) * 1024 + (fb * 4 + ft * 2) * 32 + h * 16;
    *(u32x4*)dst = p0;
    *(u32x4*)(dst + 32) = p1;
  }
  if (kvwg && ft == 0) {
    const int gt = tb * 2 + tt;
#pragma unroll
    for (int g = 0; g < 4; ++g) { const f32x4 t = LDS_F32X4(sc + 2048 + tt * 4096 + g * 1024 + lane * 16);
#pragma unroll
      for (int j = 0; j < 4; ++j) acc2[4 * g + j] += t[j]; }
    if (fb == 0) {
      float o[8];
#pragma unroll
      for (int g = 0; g < 2; ++g) {
        const f32x4 c4 = LDS_F32X4(sc + 1536 + tt * 128 + (8 * g + 4 * h) * 4) + LDS_F32X4(sc + 1536 + (tt + 2) * 128 + (8 * g + 4 * h) * 4);
#pragma unroll
        for (int j = 0; j < 4; ++j) o[4 * g + j] = (acc2[4 * g + j] - mean * c4[j]) * rstd;
      }
      u32x4 p0;
#pragma unroll
      for (int i = 0; i < 4; ++i) p0[i] = pk2(o[2 * i], o[2 * i + 1]);
      *(u32x4*)(ws + WS_K + (size_t)gt * 1024 + h * 512 + r32 * 16) = p0;
      float n2 = 0.f;
#pragma unroll
      for (int i = 0; i < 4; ++i) n2 = dot2(p0[i], p0[i], n2);
      n2 = wave_max(swap32_sum(n2));
      if (lane == 0) *(float*)(ws + WS_KMAX + gt * 4) = n2;
    } else {
      const float cv = LDS_F32(sc + 1536 + tt * 128 + r32 * 4) + LDS_F32(sc + 1536 + (tt + 2) * 128 + r32 * 4);
      const lds_ptr sp = sc + 2048 + tt * 4096;
      LGKM0();
      if (h == 0) { f32x2 st; st[0] = mean; st[1] = rstd; LDS_F32X2(sp + r32 * 8) = st; }
      float o[16];
#pragma unroll
      for (int g = 0; g < 4; ++g) {
        const f32x4 s01 = LDS_F32X4(sp + (8 * g + 4 * h) * 8), s23 = LDS_F32X4(sp + (8 * g + 4 * h) * 8 + 16);
        o[4 * g + 0] = (acc2[4 * g + 0] - s01[0] * cv) * s01[1];
        o[4 * g + 1] = (acc2[4 * g + 1] - s01[2] * cv) * s01[3];
        o[4 * g + 2] = (acc2[4 * g + 2] - s23[0] * cv) * s23[1];
        o[4 * g + 3] = (acc2[4 * g + 3] - s23[2] * cv) * s23[3];
      }
      u32x4 p0, p1;
#pragma unroll
      for (int i = 0; i < 4; ++i) { p0[i] = pk2(o[2 * i], o[2 * i + 1]); p1[i] = pk2(o[8 + 2 * i], o[9 + 2 * i]); }
      if (r32 < 16) {
        char* dst = ws + WS_V + (size_t)gt * 1024 + h * 256 + r32 * 16;
        *(u32x4*)dst = p0;
        *(u32x4*)(dst + 512) = p1;
      }
    }
  }
}

constexpr int K2_PSZ = 2176;
constexpr int K2_RED = 0;
constexpr int K2_Q = 64 * K2_PSZ;
constexpr int K2_NM = K2_Q + 8192;
constexpr int K2_JUNK = K2_NM + 8192;
constexpr int K2_FLG = K2_JUNK + 256;
constexpr int K2_OB = K2_FLG + 512;
constexpr int K2_LDS = K2_OB + 2 * 1152;
constexpr int K2_MX = 0;

#define E_(r) "v_exp_f32 v" #r ", v" #r "\n\t"
#define C_(d, a, b) "v_cvt_pk_bf16_f32 v" #d ", v" #a ", v" #b "\n\t"
#define PVPREV_ "v_mfma_f32_32x32x16_bf16 v[200:215], %8, v[236:239], v[200:215]\n\t"
#define TILE_A1 E_(168) E_(169)
#define TILE_A2 E_(170) E_(171) E_(172) E_(173) "v_mfma_f32_32x32x16_bf16 v[184:199], %6, %7, v[216:231]\n\t" E_(174) E_(175) \
    C_(232, 168, 169) C_(233, 170, 171) C_(234, 172, 173) C_(235, 174, 175) E_(176) E_(177)
#define TILE_A3 E_(178) E_(179) E_(180) E_(181) E_(182) E_(183) C_(236, 176, 177) C_(237, 178, 179) C_(238, 180, 181) C_(239, 182, 183)
#define TILE_B1 E_(184) E_(185)
#define TILE_B2 E_(186) E_(187) E_(188) E_(189) "v_mfma_f32_32x32x16_bf16 v[168:183], %6, %7, v[216:231]\n\t" E_(190) E_(191) \
    C_(232, 184, 185) C_(233, 186, 187) C_(234, 188, 189) C_(235, 190, 191) E_(192) E_(193)
#define TILE_B3 E_(194) E_(195) E_(196) E_(197) E_(198) E_(199) C_(236, 192, 193) C_(237, 194, 195) C_(238, 196, 197) C_(239, 198, 199)
#define PV0_ "v_mfma_f32_32x32x16_bf16 v[200:215], %9, v[232:235], v[200:215]\n\t"
#define PV0Z_ "v_mfma_f32_32x32x16_bf16 v[200:215], %9, v[232:235], 0\n\t"
#define MF_(D, A, B, C) "v_mfma_f32_32x32x16_bf16 " D ", " A ", " B ", " C "\n\t"
#define XT_A(KN, QN, VP, V0) E_(168) E_(169) MF_("v[200:215]", VP, "v[236:239]", "v[200:215]") E_(170) E_(171) E_(172) E_(173) MF_("v[184:199]", KN, QN, "v[216:231]") E_(174) E_(175) \
    C_(232, 168, 169) C_(233, 170, 171) C_(234, 172, 173) C_(235, 174, 175) E_(176) E_(177) MF_("v[200:215]", V0, "v[232:235]", "v[200:215]") TILE_A3
#define XT_B(KN, QN, VP, V0) E_(184) E_(185) MF_("v[200:215]", VP, "v[236:239]", "v[200:215]") E_(186) E_(187) E_(188) E_(189) MF_("v[168:183]", KN, QN, "v[216:231]") E_(190) E_(191) \
    C_(232, 184, 185) C_(233, 186, 187) C_(234, 188, 189) C_(235, 190, 191) E_(192) E_(193) MF_("v[200:215]", V0, "v[232:235]", "v[200:215]") TILE_B3
#define K2_ASM_MID() asm volatile(MF_("v[200:215]", "%26", "v[232:235]", "0") TILE_A3 \
    XT_B("%7", "%6", "%13", "%14") XT_A("%8", "%6", "%15", "%16") XT_B("%9", "%6", "%17", "%18") XT_A("%10", "%6", "%19", "%20") XT_B("%11", "%6", "%21", "%22") XT_A("%12", "%6", "%23", "%24") \
    : "+{v[168:183]}"(sa), "+{v[184:199]}"(sb), "+{v[200:215]}"(o), "+{v[232:235]}"(p0), "+{v[236:239]}"(p1), "+{v[216:231]}"(negm) \
    : "v"(qf), "v"(kf[2]), "v"(kf[3]), "v"(kf[4]), "v"(kf[5]), "v"(kf[6]), "v"(kf[7]), "v"(vf[0][1]), "v"(vf[1][0]), "v"(vf[1][1]), "v"(vf[2][0]), \
      "v"(vf[2][1]), "v"(vf[3][0]), "v"(vf[3][1]), "v"(vf[4][0]), "v"(vf[4][1]), "v"(vf[5][0]), "v"(vf[5][1]), "v"(vf[6][0]), "v"(vf[6][0]), "v"(vf[0][0]))
#define K2_ASM(TXT, KN, QN, VP, V0) asm volatile(TXT : "+{v[168:183]}"(sa), "+{v[184:199]}"(sb), "+{v[200:215]}"(o), "+{v[232:235]}"(p0), "+{v[236:239]}"(p1), "+{v[216:231]}"(negm) \
    : "v"(KN), "v"(QN), "v"(VP), "v"(V0))

extern "C" __global__ __launch_bounds__(512) void k2_attn(const u16* __restrict__ x, const u16* __restrict__ wd, char* __restrict__ ws) {
  extern __shared__ __attribute__((aligned(16))) char smem[];
  const lds_ptr lds = (lds_ptr)smem;
  if (blockIdx.x == 0 && threadIdx.x == 0) *(unsigned long long*)(ws + ((size_t)64 << 20) + 1 * 8) = __builtin_amdgcn_s_memrealtime();
  const unsigned lds0 = (unsigned)(uintptr_t)smem;
  const int tid = threadIdx.x, lane = tid & 63;
  const int w = (int)rfl(tid >> 6);
  const int g8 = (blockIdx.x & 7) * 32 + (blockIdx.x >> 3);
  const int r32 = lane & 31, h = lane >> 5;
  const u32x4 qown = *(const u32x4*)(ws + WS_Q + (size_t)(g8 * 8 + (r32 & 7)) * 1024 + (w * 4 + (r32 >> 3)) * 32 + h * 16);
  const float km2l = *(const float*)(ws + WS_KMAX + lane * 4);
  bf16x8 kf[8], vf[8][2];
  u32x4 vr[8];
  const char* ksrc = ws + WS_K + (size_t)w * 8192 + lane * 16;
  const char* vsrc = ws + WS_V + (size_t)w * 8192 + (r32 >> 4) * 512 + h * 256 + (lane & 15) * 16;
#define K2_LOADKV(j) do { kf[j] = *(const bf16x8*)(ksrc + (j) * 1024); vr[j] = *(const u32x4*)(vsrc + (j) * 1024); } while (0)
#define K2_SPLITV(j) do { u32x4 a_, b_; _Pragma("unroll") for (int i_ = 0; i_ < 4; ++i_) { \
    const auto r_ = __builtin_amdgcn_permlane16_swap(vr[j][i_], vones, false, false); a_[i_] = r_[0]; b_[i_] = r_[1]; } \
    vf[j][0] = __builtin_bit_cast(bf16x8, a_); vf[j][1] = __builtin_bit_cast(bf16x8, b_); } while (0)
  const unsigned vones = (lane & 11) == 0 ? ONES : 0u;
  K2_LOADKV(0); K2_LOADKV(1);
  if (w < 4) __builtin_amdgcn_s_setprio(2); else __builtin_amdgcn_s_setprio(1);
  if (tid >= 8 && tid < 128) LDS_VU32(lds + K2_FLG + tid * 4) = 0u;
  {
    float n2 = 0.f;
#pragma unroll
    for (int i = 0; i < 4; ++i) n2 = dot2(qown[i], qown[i], n2);
    const float mref = sqrtf(swap32_sum(n2) * wave_max(km2l));
    const f32x4 nm = {-mref, -mref, -mref, -mref};
    LDS_U32X4(lds + K2_Q + w * 1024 + lane * 16) = qown;
    LDS_F32X4(lds + K2_NM + w * 1024 + lane * 16) = nm;
    LDS_VU32(lds + K2_FLG + w * 4) = __any(!(mref <= 40.f)) ? 1u : 0u;
  }
  BAR();
  const lds_ptr qp = lds + K2_Q + lane * 16;
  f32x16 negm = {};
  const bool slow = __any(LDS_VU32(lds + K2_FLG + (lane & 7) * 4) != 0u);
  if (slow) {
    bf16x8 kf[8];
#pragma unroll
    for (int j = 0; j < 8; ++j) kf[j] = *(const bf16x8*)(ksrc + j * 1024);
    const f32x16 zero = {};
    for (int g = 0; g < 8; ++g) {
      const bf16x8 qf = lds_b128(qp + g * 1024);
      float mx = -3.0e38f;
#pragma unroll
      for (int j = 0; j < 8; ++j) {
        const f32x16 s = MFMA32(kf[j], qf, zero);
#pragma unroll
        for (int i = 0; i < 16; ++i) mx = fmaxf(mx, s[i]);
      }
      mx = swap32_max(mx);
      if (h == 0) LDS_F32(lds + K2_MX + (g * 8 + w) * 128 + r32 * 4) = mx;
    }
    BAR();
    float mx = LDS_F32(lds + K2_MX + w * 1024 + r32 * 4);
#pragma unroll
    for (int i = 1; i < 8; ++i) mx = fmaxf(mx, LDS_F32(lds + K2_MX + w * 1024 + i * 128 + r32 * 4));
    const f32x4 nm = {-mx, -mx, -mx, -mx};
    LDS_F32X4(lds + K2_NM + w * 1024 + lane * 16) = nm;
    BAR();
  }
  else {
    float nm = LDS_F32(lds + K2_NM + lane * 16);
#pragma unroll
    for (int g = 1; g < 8; ++g) nm = fminf(nm, LDS_F32(lds + K2_NM + g * 1024 + lane * 16));
#pragma unroll
    for (int i = 0; i < 16; ++i) negm[i] = nm;
  }
#define K2_LOAD_NEGM(g) asm volatile("ds_read_b128 v[216:219], %1\n\tds_read_b128 v[220:223], %1\n\tds_read_b128 v[224:227], %1\n\tds_read_b128 v[228:231], %1\n\ts_waitcnt lgkmcnt(0)" \
    : "+{v[216:231]}"(negm) : "v"(lds0 + K2_NM + (g) * 1024 + lane * 16) : "memory")
  if (slow) K2_LOAD_NEGM(0);
  f32x16 o = {}, sa, sb = {};
  u32x4 p0 = {}, p1 = {};
  bf16x8 qf = lds_b128(qp);
  sa = MFMA32(kf[0], qf, negm);
  K2_LOADKV(2); K2_LOADKV(3); K2_LOADKV(4);
  const lds_ptr redw = lds + K2_RED + w * K2_PSZ + lane * 16;
#define K2_WRITE_PARTIAL(g) do { const lds_ptr rp_ = redw + (g) * (8 * K2_PSZ); f32x4 t0_, t1_; \
    t0_[0] = o[0]; t0_[1] = o[1]; t0_[2] = o[2]; t0_[3] = o[3]; t1_[0] = o[4]; t1_[1] = o[5]; t1_[2] = o[6]; t1_[3] = o[7]; \
    LDS_F32X4(rp_) = t0_; LDS_F32X4(rp_ + 1024) = t1_; LDS_F32(rp_ + 2048 - lane * 16 + r32 * 4) = o[8]; \
    asm volatile("" ::: "memory"); LDS_VU32(lds + K2_FLG + 32 + ((g) * 8 + w) * 4) = 1u; } while (0)
#define VMWAIT0() __builtin_amdgcn_s_waitcnt(0x0F70)
#define SPL(j) do { if (FIRST_) { K2_SPLITV(j); asm volatile("s_nop 1" : "+v"(vf[j][0]), "+v"(vf[j][1])); } } while (0)
#define K2_ITER(g, FIRST) do { constexpr bool FIRST_ = FIRST; \
    const bf16x8 qn = lds_b128(qp + (((g) + 1) & 7) * 1024); \
    if (FIRST) { \
      SPL(0); K2_ASM(TILE_A1 TILE_A2 PV0Z_ TILE_A3, kf[1], qf, vf[0][0], vf[0][0]); \
      K2_LOADKV(5); SPL(1); K2_ASM(TILE_B1 PVPREV_ TILE_B2 PV0_ TILE_B3, kf[2], qf, vf[0][1], vf[1][0]); \
      K2_LOADKV(6); SPL(2); K2_ASM(TILE_A1 PVPREV_ TILE_A2 PV0_ TILE_A3, kf[3], qf, vf[1][1], vf[2][0]); \
      K2_LOADKV(7); SPL(3); K2_ASM(TILE_B1 PVPREV_ TILE_B2 PV0_ TILE_B3, kf[4], qf, vf[2][1], vf[3][0]); \
      SPL(4); K2_ASM(TILE_A1 PVPREV_ TILE_A2 PV0_ TILE_A3, kf[5], qf, vf[3][1], vf[4][0]); \
      SPL(5); K2_ASM(TILE_B1 PVPREV_ TILE_B2 PV0_ TILE_B3, kf[6], qf, vf[4][1], vf[5][0]); \
      SPL(6); K2_ASM(TILE_A1 PVPREV_ TILE_A2 PV0_ TILE_A3, kf[7], qf, vf[5][1], vf[6][0]); \
      SPL(7); \
    } else { K2_ASM(TILE_A1 PVPREV_ TILE_A2, kf[1], qf, vf[7][1], vf[0][0]); K2_WRITE_PARTIAL((g) - 1); K2_ASM_MID(); } \
    if (slow) K2_LOAD_NEGM(((g) + 1) & 7); \
    K2_ASM(TILE_B1 PVPREV_ TILE_B2 PV0_ TILE_B3, kf[0], qn, vf[6][1], vf[7][0]); \
    qf = qn; } while (0)

  asm volatile("s_nop 15" : "+{v[168:183]}"(sa));
  K2_ITER(0, true);
  VMWAIT0();
#pragma unroll
  for (int g = 1; g < 8; ++g) {
    if (g <= 5 && w < 4) {
      const int ln = ((g - 1) * 128 + (blockIdx.x >> 3) * 4 + w) * 64 + lane;
      const char* src = ln < 11008 ? (const char*)wd + ln * 128 : ln < 15104 ? ws + WS_WO + (ln - 11008) * 128 : ws + WS_WGU + (ln < 37120 ? ln - 15104 : 0) * 128;
      glds4(src, lds0 + K2_JUNK);
    }
    if (g == 7 && w == 0 && blockIdx.x < 32) {
      const char *c0 = CODE_ADDR(k4_down), *c1 = CODE_ADDR(k_end_marker);
      const char* src = c0 + ((blockIdx.x >> 3) * 64 + lane) * 128;
      glds4(src < c1 ? src : c0, lds0 + K2_JUNK);
    }
    K2_ITER(g, false);
  }
  K2_ASM("s_nop 1\n\t" PVPREV_ "s_nop 15", kf[0], qf, vf[7][1], vf[7][0]);
  K2_WRITE_PARTIAL(7);
  if (w >= 4) return;
  asm volatile("s_waitcnt vmcnt(0)" ::: "memory");
  VMWAIT0();
  const int g4 = lane >> 4, tok8 = lane & 7;
  const char* wsrc = ws + WS_WO + (size_t)w * 16384 + lane * 16;
  bf16x8 wr[5][8];
#define K2_WLOAD(hh) do { if ((hh) < 16) { _Pragma("unroll") for (int i_ = 0; i_ < 8; ++i_) wr[(hh) % 5][i_] = *(const bf16x8*)(wsrc + (size_t)((hh) >> 1) * 65536 + ((hh) & 1) * 8192 + i_ * 1024); } } while (0)
  f32x4 oacc[8];
  const float piv = __uint_as_float((unsigned)x[(size_t)(g8 * 8 + tok8) * DM + w * 128] << 16);
  {
    u32x4 xr[4];
#pragma unroll
    for (int r = 0; r < 4; ++r) xr[r] = *(const u32x4*)(x + (size_t)(g8 * 8 + tok8) * DM + w * 128 + g4 * 32 + r * 8);
    K2_WLOAD(0); K2_WLOAD(1);
#pragma unroll
    for (int r = 0; r < 4; ++r)
#pragma unroll
      for (int i = 0; i < 4; ++i) { oacc[2 * i][r] = bflo(xr[r][i]); oacc[2 * i + 1][r] = bfhi(xr[r][i]); }
  }
#define K2_GAP(g, f) do { if ((g) < 7 && ((f) & 3) == 3) __builtin_amdgcn_s_sleep(1); } while (0)
#define K2_PROJ(g) do { \
    while (!__all(LDS_VU32(lds + K2_FLG + 32 + ((g) * 8 + tok8) * 4) != 0u)) __builtin_amdgcn_s_sleep((g) < 7 ? 2 : 1); \
    asm volatile("" ::: "memory"); \
    { const lds_ptr rp_ = lds + K2_RED + (g) * (8 * K2_PSZ) + (w >> 1) * 1024 + lane * 16 + (w & 1) * 8, lp_ = lds + K2_RED + (g) * (8 * K2_PSZ) + 2048 + r32 * 4; \
      f32x2 t_[8]; float l_[8]; \
      _Pragma("unroll") for (int i_ = 0; i_ < 8; ++i_) { t_[i_] = LDS_F32X2(rp_ + i_ * K2_PSZ); l_[i_] = LDS_F32(lp_ + i_ * K2_PSZ); } \
      SB(); K2_WLOAD(2 * (g) + 2); SB(); \
      const f32x2 ts_ = ((t_[0] + t_[1]) + (t_[2] + t_[3])) + ((t_[4] + t_[5]) + (t_[6] + t_[7])); \
      const float inv_ = __builtin_amdgcn_rcpf(((l_[0] + l_[1]) + (l_[2] + l_[3])) + ((l_[4] + l_[5]) + (l_[6] + l_[7]))); \
      LDS_VU32(lds + K2_OB + ((g) & 1) * 1152 + (r32 & 7) * 144 + (r32 >> 3) * 32 + h * 8 + (w >> 1) * 16 + (w & 1) * 4) = pk2(ts_[0] * inv_, ts_[1] * inv_); \
      LDS_VU32(lds + K2_FLG + 288 + ((g) * 4 + w) * 4) = 1u; } \
    SB(); K2_WLOAD(2 * (g) + 3); SB(); \
    while (!__all(LDS_VU32(lds + K2_FLG + 288 + ((g) * 4 + (lane & 3)) * 4) != 0u)) __builtin_amdgcn_s_sleep(1); \
    asm volatile("" ::: "memory"); \
    const bf16x8 b0_ = lds_b128(lds + K2_OB + ((g) & 1) * 1152 + tok8 * 144 + g4 * 16), b1_ = lds_b128(lds + K2_OB + ((g) & 1) * 1152 + tok8 * 144 + 64 + g4 * 16); \
    _Pragma("unroll") for (int f_ = 0; f_ < 8; ++f_) { oacc[f_] = MFMA16(wr[(2 * (g)) % 5][f_], b0_, oacc[f_]); PINM(oacc[f_]); K2_GAP(g, f_); } \
    _Pragma("unroll") for (int f_ = 0; f_ < 8; ++f_) { oacc[f_] = MFMA16(wr[(2 * (g) + 1) % 5][f_], b1_, oacc[f_]); PINM(oacc[f_]); K2_GAP(g, f_); } } while (0)
  K2_PROJ(0); K2_PROJ(1); K2_PROJ(2); K2_PROJ(3); K2_PROJ(4); K2_PROJ(5); K2_PROJ(6); K2_PROJ(7);
  {
    const int tok = g8 * 8 + tok8;
    u32x4 hb[4];
#pragma unroll
    for (int r = 0; r < 4; ++r)
#pragma unroll
      for (int i = 0; i < 4; ++i) hb[r][i] = pk2(oacc[2 * i][r], oacc[2 * i + 1][r]);
    if ((lane & 8) == 0) {
#pragma unroll
      for (int r = 0; r < 4; ++r) *(u32x4*)(ws + WS_H + (size_t)(tok >> 5) * 32768 + (w * 16 + g4 * 4 + r) * 512 + (tok & 31) * 16) = hb[r];
    }
    float sx = 0.f, sxx = 0.f;
#pragma unroll
    for (int r = 0; r < 4; ++r)
#pragma unroll
      for (int i = 0; i < 4; ++i) { sx = dot2(hb[r][i], ONES, sx); sxx = dot2(hb[r][i], hb[r][i], sxx); }
    const float d1 = swap32_sum(swap16_sum(sx - 32.f * piv)), d2 = swap32_sum(swap16_sum(__builtin_fmaf(-2.f * piv, sx, sxx) + 32.f * piv * piv));
    f32x2 st; st[0] = piv + d1 * (1.f / 128); st[1] = fmaxf(d2 - d1 * d1 * (1.f / 128), 0.f);
    if (lane < 8) *(f32x2*)(ws + WS_ST + ((size_t)((tok >> 5) * 4 + w) * 32 + (tok & 31)) * 8) = st;
  }
}

constexpr int K4_SLOT = 32768, K4_LDS = 5 * K4_SLOT;

extern "C" __global__ __launch_bounds__(512) void k4_down(const u16* __restrict__ W, char* __restrict__ ws, u16* __restrict__ out) {
  constexpr int NKS = NAT;
  extern __shared__ __attribute__((aligned(16))) char smem[];
  const lds_ptr lds = (lds_ptr)smem;
  if (blockIdx.x == 0 && threadIdx.x == 0) *(unsigned long long*)(ws + ((size_t)64 << 20) + 3 * 8) = __builtin_amdgcn_s_memrealtime();
  const unsigned lds0 = (unsigned)(uintptr_t)smem;
  const int tid = threadIdx.x, lane = tid & 63;
  const int w = (int)rfl(tid >> 6);
  const int bid = blockIdx.x;
  const int tb = (bid & 7) * 4 + ((bid >> 3) & 3), nb = bid >> 5;
  const int r32 = lane & 31, h = lane >> 5;
  if (w >= 4) {
    const int lw = w - 4;
    const int rr = lane >> 3, pos = lane & 7;
    const char* wcol = (const char*)W + nb * 128 + (pos ^ (((rr >> 1) & 1) << 2)) * 16;
    const char* wsrc = wcol + (size_t)(64 * (lw >> 1) + 8 * rr + 4 * (lw & 1)) * 1024;
    const char* wlast = wcol + (size_t)(1344 + 8 * (rr & 3) + (rr >> 2)) * 1024;
    const char* asrc = ws + WS_ACT + ((size_t)(tb * 2 + (lw >> 1)) * NKS + (lw & 1) * 4) * 1024 + lane * 16;
#define K4_ISSUE(st) do { const unsigned sl_ = lds0 + ((st) % 5) * K4_SLOT; _Pragma("unroll") for (int i = 0; i < 4; ++i) { \
    const bool oka_ = (st) < 10 || !((lw & 1) && i >= 2); \
    glds16(asrc + (oka_ ? (st) * 8192 + i * 1024 : 0), sl_ + 16384 + (lw * 4 + i) * 1024); \
    glds16(((st) < 10 || lw < 2) ? wsrc + (size_t)(st) * 131072 + i * 1024 : wlast + i * 2048, sl_ + (lw * 4 + i) * 1024); } } while (0)
    {
      K4_ISSUE(0); K4_ISSUE(1); K4_ISSUE(2);
      WAIT_VM(16); RAWBAR(); K4_ISSUE(3); K4_ISSUE(4);
      WAIT_VM(24); RAWBAR(); K4_ISSUE(5);
      WAIT_VM(24); RAWBAR(); K4_ISSUE(6);
      WAIT_VM(24); RAWBAR(); K4_ISSUE(7);
      WAIT_VM(24); RAWBAR(); K4_ISSUE(8);
      WAIT_VM(24); RAWBAR(); K4_ISSUE(9);
      WAIT_VM(24); RAWBAR(); K4_ISSUE(10);
      WAIT_VM(24); RAWBAR();
      WAIT_VM(16); RAWBAR();
      WAIT_VM(8); RAWBAR();
      WAIT_VM(0); RAWBAR();
    }
    return;
  }
  const int nt = w & 1, tt = w >> 1;
  const int tok = tb * 64 + tt * 32 + r32;
  const int ncol = nb * 64 + nt * 32 + h * 16;
  const int blk = (lane >> 4) & 1, q = (lane & 15) >> 2, p = lane & 3;
  const char* hfrag = ws + WS_H + (size_t)(tok >> 5) * 32768 + (ncol >> 3) * 512 + r32 * 16;
  const u32x4 h0 = *(const u32x4*)(hfrag), h1 = *(const u32x4*)(hfrag + 512);
  f32x16 acc = {};
  const int cbyte = nt * 64 + (16 * (p & 1) + 8 * blk + 4 * (p >> 1)) * 2;
  lds_ptr ap[3], bp[3];
#pragma unroll
  for (int u = 0; u < 3; ++u) { ap[u] = lds + u * 2 * K4_SLOT + (4 * h + q) * 128 + (cbyte ^ ((q >> 1) << 6)); bp[u] = lds + u * 2 * K4_SLOT + 16384 + tt * 8192 + lane * 16; }
  s16x4 fa[2][4][2]; bf16x8 fb_[2][4];
#define K4_U(st) (((st) % 5) >> 1)
#define K4_O(st) ((((st) % 5) & 1) * K4_SLOT)
#define K4_LD1(bu, st, k0, k) do { fa[bu][k][0] = lds_tr(ap[K4_U(st)] + K4_O(st) + ((k0) + (k)) * 2048); fa[bu][k][1] = lds_tr(ap[K4_U(st)] + K4_O(st) + ((k0) + (k)) * 2048 + 1024); \
    fb_[bu][k] = lds_b128(bp[K4_U(st)] + K4_O(st) + ((k0) + (k)) * 1024); } while (0)
#define K4_STEP(bu, NM, NL, st, k0) do { _Pragma("unroll") for (int k = 0; k < 4; ++k) { \
    if (k < (NM)) { acc = MFMA32(cat8(fa[bu][k][0], fa[bu][k][1]), fb_[bu][k], acc); PINM(acc); } \
    if (k < (NL)) K4_LD1(1 - (bu), st, k0, k); SB(); } } while (0)
#define K4_STAGE(st, NK2) do { BAR(); K4_STEP(1, (st) > 0 ? 4 : 0, 4, st, 0); K4_STEP(0, 4, NK2, st, 4); } while (0)
  K4_STAGE(0, 4); K4_STAGE(1, 4); K4_STAGE(2, 4); K4_STAGE(3, 4);
  K4_STAGE(4, 4); K4_STAGE(5, 4); K4_STAGE(6, 4); K4_STAGE(7, 4); K4_STAGE(8, 4); K4_STAGE(9, 4); K4_STAGE(10, 2);
  K4_STEP(1, 2, 0, 0, 0);
  u32x4 o0, o1;
#pragma unroll
  for (int i = 0; i < 4; ++i) {
    o0[i] = pk2(acc[2 * i] + bflo(h0[i]), acc[2 * i + 1] + bfhi(h0[i]));
    o1[i] = pk2(acc[8 + 2 * i] + bflo(h1[i]), acc[9 + 2 * i] + bfhi(h1[i]));
  }
  u16* dst = out + (size_t)tok * DM + ncol;
  *(u32x4*)dst = o0;
  *(u32x4*)(dst + 8) = o1;
}

constexpr int K3_SLOT = 12288, K3_FLG = 13 * K3_SLOT, K3_SC = K3_FLG + 64, K3_LDS = K3_SC + 768;

extern "C" __global__ __launch_bounds__(512) void k3_gateup(const u16* __restrict__ wd, char* __restrict__ ws) {
  extern __shared__ __attribute__((aligned(16))) char smem[];
  const lds_ptr lds = (lds_ptr)smem;
  if (blockIdx.x == 0 && threadIdx.x == 0) *(unsigned long long*)(ws + ((size_t)64 << 20) + 2 * 8) = __builtin_amdgcn_s_memrealtime();
  const unsigned lds0 = (unsigned)(uintptr_t)smem;
  const int tid = threadIdx.x, lane = tid & 63;
  const int w = (int)rfl(tid >> 6);
  const int bid = blockIdx.x, jx = bid >> 3;
  if (jx >= 30) {
    const char* src = (const char*)wd + (size_t)(((jx & 1) * 8 + w) * 64 + lane) * 128;
#pragma unroll
    for (int i = 0; i < 6; ++i) glds4(src + i * 131072, lds0 + w * 256);
    return;
  }
  const int rb = (bid & 7) * 2 + (jx & 1), cb = jx >> 1;
  const int at0 = cb * 6 > 80 ? 80 : cb * 6;
  if (w >= 4) {
    const int lw = w - 4;
    const char* wsrc[3];
#pragma unroll
    for (int i = 0; i < 3; ++i) wsrc[i] = ws + WS_WGU + (size_t)(at0 + ((lw * 3 + i) >> 1)) * 32768 + ((lw * 3 + i) & 1) * 1024 + lane * 16;
#define K3_ISSUE(st) do { if ((st) >= 13) WAIT_CNT(lds + K3_FLG + 16, (st) - 12); \
    _Pragma("unroll") for (int i = 0; i < 3; ++i) glds16(wsrc[i] + (st) * 2048, lds0 + ((st) % 13) * K3_SLOT + (lw * 3 + i) * 1024); } while (0)
#define K3_PUB(n) LDS_VU32(lds + K3_FLG + lw * 4) = (n)
    K3_ISSUE(0); K3_ISSUE(1);
    RAWBAR();

    K3_ISSUE(2); WAIT_VM(6); K3_PUB(1);
    K3_ISSUE(3); WAIT_VM(6); K3_PUB(2);
    K3_ISSUE(4); WAIT_VM(6); K3_PUB(3);
    K3_ISSUE(5); WAIT_VM(6); K3_PUB(4);
    K3_ISSUE(6); WAIT_VM(6); K3_PUB(5);
    K3_ISSUE(7); WAIT_VM(6); K3_PUB(6);
    K3_ISSUE(8); WAIT_VM(6); K3_PUB(7);
    K3_ISSUE(9); WAIT_VM(6); K3_PUB(8);
    K3_ISSUE(10); WAIT_VM(6); K3_PUB(9);
    K3_ISSUE(11); WAIT_VM(6); K3_PUB(10);
    K3_ISSUE(12); WAIT_VM(6); K3_PUB(11);
    K3_ISSUE(13); WAIT_VM(6); K3_PUB(12);
    K3_ISSUE(14); WAIT_VM(6); K3_PUB(13);
    K3_ISSUE(15); WAIT_VM(6); K3_PUB(14);
    WAIT_VM(3); K3_PUB(15);
    WAIT_VM(0); K3_PUB(16);
    if (lw < 3) {
      const int i = lw * 64 + lane;
      const float* cp = (const float*)(ws + WS_CS) + ((i >> 4) & 1) * (8 * 1408) + ffeat(at0 + (i >> 5), i & 15);
      float c[8];
#pragma unroll
      for (int j = 0; j < 8; ++j) c[j] = cp[j * 1408];
      LDS_F32(lds + K3_SC + i * 4) = ((c[0] + c[1]) + (c[2] + c[3])) + ((c[4] + c[5]) + (c[6] + c[7]));
    }
    BAR();
    return;
  }
  const int r32 = lane & 31, h = lane >> 5;
  const char* bsrc = ws + WS_H + (size_t)(rb * 4 + w) * 32768 + lane * 16;
  constexpr int K3_BD = 8;
  bf16x8 fb_[K3_BD];
#define K3_LDB(kk) fb_[(kk) % K3_BD] = *(const bf16x8*)(bsrc + (kk) * 1024)
#pragma unroll
  for (int i = 0; i < 4; ++i) K3_LDB(i);
  if (tid < 8) LDS_VU32(lds + K3_FLG + tid * 4) = 0u;
  BAR();
#pragma unroll
  for (int i = 4; i < K3_BD; ++i) K3_LDB(i);
  f32x16 acc[6] = {};
  lds_ptr ap[3];
#pragma unroll
  for (int u = 0; u < 3; ++u) { ap[u] = lds + u * 5 * K3_SLOT + lane * 16; PIN(ap[u]); }
  bf16x8 fa[2][6];
  u32x4 rdy;
#define K3_A(kk, i) lds_b128(ap[(((kk) >> 1) % 13) / 5] + ((((kk) >> 1) % 13) % 5) * K3_SLOT + (i) * 2048 + ((kk) & 1) * 1024)
#define K3_STEP(kk) do { constexpr int n_ = (kk) + 2; \
    if (n_ < 32 && (n_ & 1) == 0 && __builtin_expect(rfl(min4(rdy)) < n_ / 2u + 1u, 0)) WAIT_CNT(lds + K3_FLG, n_ / 2 + 1); \
    _Pragma("unroll") for (int i = 0; i < 6; ++i) { \
      acc[i] = MFMA32(fa[(kk) & 1][i], fb_[(kk) % K3_BD], acc[i]); PINM(acc[i]); \
      if (i == 0 && (kk) > 0 && (kk) - 1 + K3_BD < 32) K3_LDB((kk) - 1 + K3_BD); \
      if (n_ < 32) fa[(kk) & 1][i] = K3_A(n_, i); \
      if ((n_ & 1) == 1 && i == 2) rdy = LDS_VU32X4(lds + K3_FLG); \
      SB(); } \
    if (((kk) & 1) == 1 && (kk) < 6) LDS_VU32(lds + K3_FLG + 16 + w * 4) = ((kk) + 1) / 2;   } while (0)
#define K3_STEP4(kk) do { K3_STEP(kk); K3_STEP((kk) + 1); K3_STEP((kk) + 2); K3_STEP((kk) + 3); } while (0)
  WAIT_CNT(lds + K3_FLG, 1);
#pragma unroll
  for (int i = 0; i < 6; ++i) { fa[0][i] = K3_A(0, i); fa[1][i] = K3_A(1, i); }
  rdy = LDS_VU32X4(lds + K3_FLG);
  K3_STEP4(0); K3_STEP4(4);
  const f32x2 lp0 = *(const f32x2*)(ws + WS_ST + ((size_t)((rb * 4 + w) * 4 + h * 2) * 32 + r32) * 8), lp1 = *(const f32x2*)(ws + WS_ST + ((size_t)((rb * 4 + w) * 4 + h * 2 + 1) * 32 + r32) * 8);
  K3_STEP4(8); K3_STEP4(12); K3_STEP4(16); K3_STEP4(20); K3_STEP4(24);
  float mean = swap32_sum(lp0[0] + lp1[0]) * 0.25f;
  float rstd = ln_rstd(swap32_sum(lp0[1] + lp1[1] + 128.f * ((lp0[0] - mean) * (lp0[0] - mean) + (lp1[0] - mean) * (lp1[0] - mean))) * (1.f / DM));
  mean = rbf(mean);
  PIN(mean); PIN(rstd);
  BAR();
  K3_STEP4(28);
  const float ke = -1.4426950408889634f * rstd, ir2 = __builtin_amdgcn_rcpf(rstd * rstd);
  const f32x2 nm2 = {-mean, -mean}, ke2 = {ke, ke}, ir22 = {ir2, ir2};
  f32x4 call[6][4];
#pragma unroll
  for (int i = 0; i < 6; ++i)
#pragma unroll
    for (int g = 0; g < 4; ++g) call[i][g] = LDS_F32X4(lds + K3_SC + i * 128 + (8 * g + 4 * h) * 4);
  SB();
#pragma unroll
  for (int i = 0; i < 6; ++i) {
    const f32x4 (&c4)[4] = call[i];
    f32x2 tg[4], tu[4], e[4];
#pragma unroll
    for (int j = 0; j < 4; ++j) {
      const f32x2 ag2 = {acc[i][2 * j], acc[i][2 * j + 1]}, au2 = {acc[i][8 + 2 * j], acc[i][9 + 2 * j]};
      const f32x2 cg2 = {c4[j >> 1][2 * (j & 1)], c4[j >> 1][2 * (j & 1) + 1]}, cu2 = {c4[2 + (j >> 1)][2 * (j & 1)], c4[2 + (j >> 1)][2 * (j & 1) + 1]};
      tg[j] = __builtin_elementwise_fma(nm2, cg2, ag2); tu[j] = __builtin_elementwise_fma(nm2, cu2, au2);
      e[j] = tg[j] * ke2;
    }
#pragma unroll
    for (int j = 0; j < 4; ++j) { e[j][0] = __builtin_amdgcn_exp2f(e[j][0]); e[j][1] = __builtin_amdgcn_exp2f(e[j][1]); tg[j] = tg[j] * tu[j]; }
#pragma unroll
    for (int j = 0; j < 4; ++j) e[j] = __builtin_elementwise_fma(e[j], ir22, ir22);
#pragma unroll
    for (int j = 0; j < 4; ++j) { e[j][0] = __builtin_amdgcn_rcpf(e[j][0]); e[j][1] = __builtin_amdgcn_rcpf(e[j][1]); }
    u32x4 pk;
#pragma unroll
    for (int j = 0; j < 4; ++j) { const f32x2 a2 = tg[j] * e[j]; pk[j] = pk2(a2[0], a2[1]); }
    *(u32x4*)(ws + WS_ACT + ((size_t)(rb * 4 + w) * NAT + at0 + i) * 1024 + lane * 16) = pk;
  }
}

extern "C" __global__ void k_spin(char* ws) {
  const unsigned long long* t = (const unsigned long long*)(ws + ((size_t)64 << 20));
  const unsigned long long now = __builtin_amdgcn_s_memrealtime();
  unsigned long long tt[5] = {t[0], t[1], t[2], t[3], now};
  const unsigned long long d = SEL < 0 ? 0 : tt[SEL + 1] - tt[SEL];
  while (__builtin_amdgcn_s_memrealtime() - now < d) __builtin_amdgcn_s_sleep(1);
}
extern "C" __global__ void k_end_marker() {}

extern "C" void kernel_launch(void* const* d_in, const int* in_sizes, int n_in, void* d_out, int out_size, void* d_ws, size_t ws_size,
                              hipStream_t stream) {
  const u16* x = (const u16*)d_in[0];
  const u16* wq = (const u16*)d_in[1];
  const u16* wk = (const u16*)d_in[2];
  const u16* wv = (const u16*)d_in[3];
  const u16* wo = (const u16*)d_in[4];
  const u16* wg = (const u16*)d_in[6];
  const u16* wu = (const u16*)d_in[7];
  const u16* wd = (const u16*)d_in[8];
  char* ws = (char*)d_ws;
  (void)hipFuncSetAttribute((const void*)k1_qkv, hipFuncAttributeMaxDynamicSharedMemorySize, K1_LDS);
  (void)hipFuncSetAttribute((const void*)k2_attn, hipFuncAttributeMaxDynamicSharedMemorySize, K2_LDS);
  (void)hipFuncSetAttribute((const void*)k3_gateup, hipFuncAttributeMaxDynamicSharedMemorySize, K3_LDS);
  (void)hipFuncSetAttribute((const void*)k4_down, hipFuncAttributeMaxDynamicSharedMemorySize, K4_LDS);
  k1_qkv<<<256, 640, K1_LDS, stream>>>(x, wq, wk, wv, wo, wg, wu, ws);
  k2_attn<<<256, 512, K2_LDS, stream>>>(x, wd, ws);
  k3_gateup<<<256, 512, K3_LDS, stream>>>(wd, ws);
  k4_down<<<256, 512, K4_LDS, stream>>>(wd, ws, (u16*)d_out);
  k_spin<<<1, 64, 0, stream>>>(ws);
}
```

```cpp
#include <hip/hip_runtime.h>
#include <hip/hip_bf16.h>
#include <stdint.h>

typedef __attribute__((ext_vector_type(8))) short bf16x8;
typedef __attribute__((ext_vector_type(4))) short s16x4;
typedef __attribute__((ext_vector_type(2))) __bf16 bf16x2;
typedef __attribute__((ext_vector_type(4))) unsigned u32x4;
typedef __attribute__((ext_vector_type(2))) unsigned u32x2;
typedef __attribute__((ext_vector_type(16))) float f32x16;
typedef __attribute__((ext_vector_type(4))) float f32x4;
typedef __attribute__((ext_vector_type(2))) float f32x2;
typedef __attribute__((address_space(3))) char* lds_ptr;
typedef unsigned short u16;

#define DI __device__ __forceinline__
#define MFMA32(a, b, c) __builtin_amdgcn_mfma_f32_32x32x16_bf16((a), (b), (c), 0, 0, 0)
#define MFMA16(a, b, c) __builtin_amdgcn_mfma_f32_16x16x32_bf16((a), (b), (c), 0, 0, 0)
#define WAIT_VM_BAR(N) asm volatile("s_waitcnt vmcnt(" #N ") lgkmcnt(0)\n\ts_barrier" ::: "memory")
#define BAR() asm volatile("s_waitcnt lgkmcnt(0)\n\ts_barrier" ::: "memory")

constexpr int SEQ = 2048, DM = 512, FF = 1376, NAT = 86;
constexpr float QSCALE = 0.25f * 1.4426950408889634f;
constexpr float LN_EPS = 1e-5f;

constexpr size_t WS_Q = 0;
constexpr size_t WS_K = WS_Q + (size_t)SEQ * DM * 2;
constexpr size_t WS_V = WS_K + 65536;
constexpr size_t WS_KMAX = WS_V + 65536;
constexpr size_t WS_WO = WS_KMAX + 4096;
constexpr size_t WS_H = WS_WO + (size_t)DM * DM * 2;
constexpr size_t WS_ST = WS_H + (size_t)SEQ * DM * 2;
constexpr size_t WS_CS = WS_ST + (size_t)SEQ * 16 * 8;
constexpr size_t WS_WGU = WS_CS + 2 * 8 * 1408 * 4;
constexpr size_t WS_ACT = WS_WGU + (size_t)NAT * 32768;

DI void glds16(const void* g, unsigned lds_base) {
  unsigned sv;
  asm volatile("s_mov_b32 %0, m0\n\ts_mov_b32 m0, %2\n\ts_nop 0\n\tglobal_load_lds_dwordx4 %1, off\n\ts_mov_b32 m0, %0"
               : "=&s"(sv) : "v"(g), "s"(lds_base) : "memory");
}
DI void glds4(const void* g, unsigned lds_base) {
  unsigned sv;
  asm volatile("s_mov_b32 %0, m0\n\ts_mov_b32 m0, %2\n\ts_nop 0\n\tglobal_load_lds_dword %1, off\n\ts_mov_b32 m0, %0"
               : "=&s"(sv) : "v"(g), "s"(lds_base) : "memory");
}
#define CODE_ADDR(sym) ({ unsigned lo_, hi_; asm volatile("s_getpc_b64 s[40:41]\n\ts_add_u32 %0, s40, " #sym "@rel32@lo+4\n\ts_addc_u32 %1, s41, " #sym "@rel32@hi+12" \
    : "=s"(lo_), "=s"(hi_) :: "s40", "s41", "scc"); (const char*)(((unsigned long long)hi_ << 32) | lo_); })
DI unsigned rfl(unsigned v) { return (unsigned)__builtin_amdgcn_readfirstlane((int)v); }
DI unsigned pk2(float lo, float hi) {
  bf16x2 v; v[0] = (__bf16)lo; v[1] = (__bf16)hi;
  return __builtin_bit_cast(unsigned, v);
}
DI int ffeat(int at, int rho) { return at < 84 ? 64 * (at >> 2) + 8 * (rho & 7) + 2 * (at & 3) + (rho >> 3) : 1344 + 8 * (rho & 3) + 4 * (at - 84) + (rho >> 2); }
DI void st16_wt(void* p, u32x4 v) { asm volatile("global_store_dwordx4 %0, %1, off sc1\n\ts_nop 1" :: "v"(p), "v"(v) : "memory"); }
DI float rbf(float v) { return (float)(__bf16)v; }
DI float ln_rstd(float var) { return __builtin_amdgcn_rcpf(rbf(__builtin_amdgcn_sqrtf(rbf(rbf(var) + LN_EPS)))); }
DI float bflo(unsigned u) { return __uint_as_float(u << 16); }
DI float bfhi(unsigned u) { return __uint_as_float(u & 0xffff0000u); }
DI float dot2(unsigned a, unsigned b, float c) {
  return __builtin_amdgcn_fdot2_f32_bf16(__builtin_bit_cast(bf16x2, a), __builtin_bit_cast(bf16x2, b), c, false);
}
DI bf16x8 lds_b128(lds_ptr p) { return *(const __attribute__((address_space(3))) bf16x8*)p; }
DI s16x4 lds_tr(lds_ptr p) {
  typedef short v4i16_t __attribute__((ext_vector_type(4)));
  return __builtin_bit_cast(s16x4, __builtin_amdgcn_ds_read_tr16_b64_v4i16((__attribute__((address_space(3))) v4i16_t*)p));
}
DI float swap32_sum(float v) {
  auto r = __builtin_amdgcn_permlane32_swap(__float_as_uint(v), __float_as_uint(v), false, false);
  return __uint_as_float(r[0]) + __uint_as_float(r[1]);
}
DI float swap16_sum(float v) {
  auto r = __builtin_amdgcn_permlane16_swap(__float_as_uint(v), __float_as_uint(v), false, false);
  return __uint_as_float(r[0]) + __uint_as_float(r[1]);
}
DI float swap32_max(float v) {
  auto r = __builtin_amdgcn_permlane32_swap(__float_as_uint(v), __float_as_uint(v), false, false);
  return fmaxf(__uint_as_float(r[0]), __uint_as_float(r[1]));
}
template <int CTRL, int RM>
DI float dpp(float old, float v) { return __uint_as_float((unsigned)__builtin_amdgcn_update_dpp((int)__float_as_uint(old), (int)__float_as_uint(v), CTRL, RM, 0xf, false)); }
DI float wave_sum(float v) {
  v += dpp<0x111, 0xf>(0.f, v); v += dpp<0x112, 0xf>(0.f, v); v += dpp<0x114, 0xf>(0.f, v); v += dpp<0x118, 0xf>(0.f, v);
  v += dpp<0x142, 0xa>(0.f, v); v += dpp<0x143, 0xc>(0.f, v);
  return __uint_as_float((unsigned)__builtin_amdgcn_readlane((int)__float_as_uint(v), 63));
}
DI float wave_max(float v) {
  v = fmaxf(v, dpp<0x111, 0xf>(0.f, v)); v = fmaxf(v, dpp<0x112, 0xf>(0.f, v)); v = fmaxf(v, dpp<0x114, 0xf>(0.f, v)); v = fmaxf(v, dpp<0x118, 0xf>(0.f, v));
  v = fmaxf(v, dpp<0x142, 0xa>(0.f, v)); v = fmaxf(v, dpp<0x143, 0xc>(0.f, v));
  return __uint_as_float((unsigned)__builtin_amdgcn_readlane((int)__float_as_uint(v), 63));
}
DI float quad_sum(float v) { v += dpp<0xb1, 0xf>(0.f, v); v += dpp<0x4e, 0xf>(0.f, v); return v; }

#define SB() __builtin_amdgcn_sched_barrier(0)
#define PIN(x) asm volatile("" : "+v"(x))
#define PINM(x) asm volatile("" : "+v"(x) :: "memory")
#define LDS_F32X4(p) (*(__attribute__((address_space(3))) f32x4*)(p))
#define LDS_F32X2(p) (*(__attribute__((address_space(3))) f32x2*)(p))
#define LDS_F32(p) (*(__attribute__((address_space(3))) float*)(p))
#define LDS_U32X2(p) (*(__attribute__((address_space(3))) u32x2*)(p))
#define LDS_U32X4(p) (*(__attribute__((address_space(3))) u32x4*)(p))
#define LDS_VU32(p) (*(volatile __attribute__((address_space(3))) unsigned*)(p))
#define WAIT_VM(N) asm volatile("s_waitcnt vmcnt(" #N ")" ::: "memory")
#define RAWBAR() asm volatile("s_barrier" ::: "memory")
#define LGKM0() asm volatile("s_waitcnt lgkmcnt(0)" ::: "memory")
DI bf16x8 cat8(s16x4 a, s16x4 b) { return __builtin_shufflevector(a, b, 0, 1, 2, 3, 4, 5, 6, 7); }
constexpr unsigned ONES = 0x3f803f80u;
#define LDS_VU32X4(p) (*(volatile __attribute__((address_space(3))) u32x4*)(p))
DI unsigned min4(u32x4 v) { return min(min(v[0], v[1]), min(v[2], v[3])); }
#define WAIT_CNT(p, n) do { while (rfl(min4(LDS_VU32X4(p))) < (unsigned)(n)) __builtin_amdgcn_s_sleep(1); asm volatile("" ::: "memory"); } while (0)

constexpr int K1_X = 0, K1_W = 65536, K1_KV = 131072, K1_SC = 147456, K1_FLG = K1_SC + 2048 + 8192, K1_JUNK = K1_FLG + 32, K1_LDS = K1_JUNK + 256;

extern "C" __global__ __launch_bounds__(640) void k1_qkv(const u16* __restrict__ x, const u16* __restrict__ wq, const u16* __restrict__ wk,
                                              const u16* __restrict__ wv, const u16* __restrict__ wo, const u16* __restrict__ wg,
                                              const u16* __restrict__ wu, char* __restrict__ ws) {
  extern __shared__ __attribute__((aligned(16))) char smem[];
  const lds_ptr lds = (lds_ptr)smem;
  const unsigned lds0 = (unsigned)(uintptr_t)smem;
  const int tid = threadIdx.x, lane = tid & 63;
  const int w = (int)rfl(tid >> 6);
  if (w < 3) {
    const char *c0 = CODE_ADDR(k1_qkv), *c1 = CODE_ADDR(k2_attn);
    const char* src = c0 + tid * 128;
    glds4(src < c1 ? src : c0, lds0 + K1_JUNK);
  }
  const int bid = blockIdx.x;
  const int tb = (bid & 7) * 4 + ((bid >> 3) & 3), fb = bid >> 5;
  const bool kvwg = fb < 2;
  if (w >= 8) {
    RAWBAR();
    const int job = (w - 8) * 256 + (255 - bid);
    if (w == 9 && bid < 64) {
      const char *c0 = CODE_ADDR(k2_attn), *c1 = CODE_ADDR(k_end_marker);
      const char* src = c0 + ((bid >> 3) * 64 + lane) * 128;
      glds4(src < c1 ? src : c0, lds0 + K1_JUNK);
    }
    if (job < 416) {
        const bool isw = job < 64;
        const int jg = job - 64, mat = jg >= 176, jj = jg - mat * 176, ksl = jj & 7, cg = jj >> 3;
        const int col = cg * 64 + (lane & 7) * 8, kc = ksl * 8 + (lane >> 3);
        const u16* src = isw ? wo + (size_t)((job >> 3) * 64 + (job & 1) * 32 + 8 * (lane >> 4)) * DM + ((job >> 1) & 3) * 128 + (lane & 15) * 8
                             : (mat ? wu : wg) + (size_t)kc * 8 * FF + (col < FF ? col : 0);
        const int ld = isw ? DM : FF;
        WAIT_CNT(lds + K1_FLG, 2);
        u32x4 R[8];
#pragma unroll
        for (int j = 0; j < 8; ++j) R[j] = *(const u32x4*)(src + (size_t)j * ld);
        char* dst = isw ? ws + WS_WO + (size_t)job * 8192 + lane * 16
                        : ws + WS_WGU + ((size_t)(cg * 128 + (kc >> 1)) * 64 + (kc & 1) * 32 + mat * 16 + (lane & 7)) * 16;
        float c[8];
#pragma unroll
        for (int m = 0; m < 4; ++m) {
          u32x4 lo, hi;
#pragma unroll
          for (int i = 0; i < 4; ++i) { lo[i] = __builtin_amdgcn_perm(R[2 * i + 1][m], R[2 * i][m], 0x05040100u); hi[i] = __builtin_amdgcn_perm(R[2 * i + 1][m], R[2 * i][m], 0x07060302u); }
          const int o0 = isw ? 2 * m * 1024 : cg < 21 ? m * 32768 : (m >> 1) * 32768 + (m & 1) * 128, o1 = o0 + (isw ? 1024 : cg < 21 ? 128 : 64);
          if (isw || col < FF) { st16_wt(dst + o0, lo); st16_wt(dst + o1, hi); }
          c[2 * m] = dot2(lo[3], ONES, dot2(lo[2], ONES, dot2(lo[1], ONES, dot2(lo[0], ONES, 0.f))));
          c[2 * m + 1] = dot2(hi[3], ONES, dot2(hi[2], ONES, dot2(hi[1], ONES, dot2(hi[0], ONES, 0.f))));
        }
        if (!isw) {
#pragma unroll
          for (int i = 0; i < 8; ++i) { c[i] += dpp<0x128, 0xf>(0.f, c[i]); c[i] = swap32_sum(swap16_sum(c[i])); }
          if (lane < 8) {
            float* cd = (float*)(ws + WS_CS) + (mat * 8 + ksl) * 1408 + cg * 64 + lane * 8;
            *(f32x4*)cd = f32x4{c[0], c[1], c[2], c[3]};
            *(f32x4*)(cd + 4) = f32x4{c[4], c[5], c[6], c[7]};
          }
        }
    }
    return;
  }
  if (w >= 4) {
    const int lw = w - 4;
    const u16* xs = x + (size_t)(tb * 64 + lw * 16 + (lane >> 4)) * DM;
    const int xc = (lane & 15) ^ (lane >> 4);
    const int rr = lane >> 3, pos = lane & 7;
    const u16* wsrc = wq + (size_t)(lw * 32 + rr) * DM + fb * 64 + ((pos ^ (((rr >> 1) & 1) << 2)) * 8);
    const u16* kvs = (fb ? wv : wk) + lw * 512 + lane * 8;
#define K1_ISSUE(st, KV) do { _Pragma("unroll") for (int i = 0; i < 4; ++i) { \
      glds16(xs + (size_t)i * 4 * DM + (st) * 128 + ((xc ^ (4 * i)) * 8), lds0 + K1_X + (st) * 16384 + (lw * 4 + i) * 1024); \
      glds16(wsrc + (size_t)((st) * 128 + i * 8) * DM, lds0 + K1_W + (st) * 16384 + (lw * 4 + i) * 1024); } \
    if (KV) glds16(kvs + (st) * 2048, lds0 + K1_KV + (st) * 4096 + lw * 1024); } while (0)
#define K1_PUB(n) LDS_VU32(lds + K1_FLG + lw * 4) = (n)
    if (kvwg) {
      K1_ISSUE(0, 1); RAWBAR(); K1_ISSUE(1, 1);
      WAIT_VM(9); K1_PUB(1); K1_ISSUE(2, 1); WAIT_VM(9); K1_PUB(2); K1_ISSUE(3, 1); WAIT_VM(9); K1_PUB(3);
    } else {
      K1_ISSUE(0, 0); RAWBAR(); K1_ISSUE(1, 0);
      WAIT_VM(8); K1_PUB(1); K1_ISSUE(2, 0); WAIT_VM(8); K1_PUB(2); K1_ISSUE(3, 0); WAIT_VM(8); K1_PUB(3);
    }
    WAIT_VM(0); K1_PUB(4);
    return;
  }
  if (tid < 8) LDS_VU32(lds + K1_FLG + tid * 4) = 0u;
  BAR();
  const int r32 = lane & 31, h = lane >> 5;
  const int blk = (lane >> 4) & 1, q = (lane & 15) >> 2, p = lane & 3;
  const int tt = w & 1, ft = w >> 1;
  const int tok = tt * 32 + r32;
  lds_ptr xb[8];
#pragma unroll
  for (int i = 0; i < 8; ++i) xb[i] = lds + K1_X + tok * 256 + (((2 * i + h) ^ (tok & 15)) * 16);
  const lds_ptr wb = lds + K1_W + (8 * h + q) * 128 + (((ft * 4 + blk * 2 + (p >> 1)) ^ ((q >> 1) << 2)) * 16) + 8 * (p & 1);
  const lds_ptr kb = lds + K1_KV + (8 * h + q) * 32 + 8 * p;
  const float piv = __uint_as_float((unsigned)x[(size_t)(tb * 64 + tok) * DM] << 16);
  f32x16 acc = {}, acc2 = {};
  float cs = 0.f, cs2 = 0.f, sx = 0.f, sxx = 0.f, sxb = 0.f, sxxb = 0.f;
  s16x4 a[2][4][2]; bf16x8 b[2][4];
#define K1_STEP(bu, MM, LD, s0, sc0) do { _Pragma("unroll") for (int i = 0; i < 4; ++i) { const int s = (s0) + i; \
      if (MM) { acc = MFMA32(cat8(a[bu][i][0], a[bu][i][1]), b[bu][i], acc); PINM(acc); } \
      if (LD) { a[1 - (bu)][i][0] = lds_tr(wb + s * 2048); a[1 - (bu)][i][1] = lds_tr(wb + s * 2048 + 512); b[1 - (bu)][i] = lds_b128(xb[s & 7] + (s >> 3) * 16384); } SB(); } \
    if (MM) { \
      if (ft == (bu)) { \
        if (kvwg) { bf16x8 ka[4]; \
          _Pragma("unroll") for (int i = 0; i < 4; ++i) ka[i] = cat8(lds_tr(kb + ((sc0) + i) * 512), lds_tr(kb + ((sc0) + i) * 512 + 128)); \
          if (fb == 0) { _Pragma("unroll") for (int i = 0; i < 4; ++i) acc2 = MFMA32(ka[i], b[bu][i], acc2); } \
          else { _Pragma("unroll") for (int i = 0; i < 4; ++i) acc2 = MFMA32(b[bu][i], ka[i], acc2); } \
          PINM(acc2); SB(); \
          _Pragma("unroll") for (int i = 0; i < 4; ++i) { const u32x4 u = __builtin_bit_cast(u32x4, ka[i]); \
            cs2 = dot2(u[0], ONES, cs2); cs2 = dot2(u[1], ONES, cs2); cs2 = dot2(u[2], ONES, cs2); cs2 = dot2(u[3], ONES, cs2); } PIN(cs2); } \
        _Pragma("unroll") for (int i = 0; i < 4; ++i) { const u32x4 u = __builtin_bit_cast(u32x4, b[bu][i]); \
          sx = dot2(u[0], ONES, sx); sxx = dot2(u[0], u[0], sxx); sxb = dot2(u[1], ONES, sxb); sxxb = dot2(u[1], u[1], sxxb); \
          sx = dot2(u[2], ONES, sx); sxx = dot2(u[2], u[2], sxx); sxb = dot2(u[3], ONES, sxb); sxxb = dot2(u[3], u[3], sxxb); } \
        PIN(sx); PIN(sxx); PIN(sxb); PIN(sxxb); } \
      if (tt == (bu)) { _Pragma("unroll") for (int i = 0; i < 4; ++i) { const u32x4 u = __builtin_bit_cast(u32x4, cat8(a[bu][i][0], a[bu][i][1])); \
          cs = dot2(u[0], ONES, cs); cs = dot2(u[1], ONES, cs); cs = dot2(u[2], ONES, cs); cs = dot2(u[3], ONES, cs); } PIN(cs); } \
      SB(); } } while (0)
#pragma unroll
  for (int st = 0; st < 4; ++st) { WAIT_CNT(lds + K1_FLG, st + 1); K1_STEP(1, st > 0, true, st * 8, st * 8 - 4); K1_STEP(0, true, true, st * 8 + 4, st * 8); }
  K1_STEP(1, true, false, 0, 28);
  const lds_ptr sc = lds + K1_SC;
  {
    sx += sxb; sxx += sxxb;
    f32x2 d; d[0] = swap32_sum(sx - 128.f * piv); d[1] = swap32_sum(__builtin_fmaf(-2.f * piv, sx, sxx) + 128.f * piv * piv);
    cs = swap32_sum(cs); cs2 = swap32_sum(cs2);
    if (h == 0) { LDS_F32(sc + (ft * 2 + tt) * 128 + r32 * 4) = cs; LDS_F32X2(sc + 512 + (tt * 2 + ft) * 256 + r32 * 8) = d; LDS_F32(sc + 1536 + w * 128 + r32 * 4) = cs2; }
    if (kvwg && ft == 1) {
#pragma unroll
      for (int g = 0; g < 4; ++g) { f32x4 t; t[0] = acc2[4 * g]; t[1] = acc2[4 * g + 1]; t[2] = acc2[4 * g + 2]; t[3] = acc2[4 * g + 3]; LDS_F32X4(sc + 2048 + tt * 4096 + g * 1024 + lane * 16) = t; }
    }
  }
  asm volatile("" ::: "memory");
  LDS_VU32(lds + K1_FLG + 16 + w * 4) = 1u;
  WAIT_CNT(lds + K1_FLG + 16, 1);
  float mean, rstd;
  {
    const f32x2 d = LDS_F32X2(sc + 512 + tt * 512 + r32 * 8) + LDS_F32X2(sc + 512 + tt * 512 + 256 + r32 * 8);
    const float dmn = d[0] * (1.f / DM);
    mean = rbf(piv + dmn); rstd = ln_rstd(fmaxf(d[1] * (1.f / DM) - dmn * dmn, 0.f));
  }
  {
    const float scl = rstd * QSCALE;
    float o[16];
#pragma unroll
    for (int g = 0; g < 4; ++g) {
      const f32x4 c4 = LDS_F32X4(sc + ft * 256 + (8 * g + 4 * h) * 4) + LDS_F32X4(sc + ft * 256 + 128 + (8 * g + 4 * h) * 4);
#pragma unroll
      for (int j = 0; j < 4; ++j) o[4 * g + j] = (acc[4 * g + j] - mean * c4[j]) * scl;
    }
    u32x4 p0, p1;
#pragma unroll
    for (int i = 0; i < 4; ++i) { p0[i] = pk2(o[2 * i], o[2 * i + 1]); p1[i] = pk2(o[8 + 2 * i], o[9 + 2 * i]); }
    char* dst = ws + WS_Q + (size_t)(tb * 64 + tok) * 1024 + (fb * 4 + ft * 2) * 32 + h * 16;
    *(u32x4*)dst = p0;
    *(u32x4*)(dst + 32) = p1;
  }
  if (kvwg && ft == 0) {
    const int gt = tb * 2 + tt;
#pragma unroll
    for (int g = 0; g < 4; ++g) { const f32x4 t = LDS_F32X4(sc + 2048 + tt * 4096 + g * 1024 + lane * 16);
#pragma unroll
      for (int j = 0; j < 4; ++j) acc2[4 * g + j] += t[j]; }
    if (fb == 0) {
      float o[8];
#pragma unroll
      for (int g = 0; g < 2; ++g) {
        const f32x4 c4 = LDS_F32X4(sc + 1536 + tt * 128 + (8 * g + 4 * h) * 4) + LDS_F32X4(sc + 1536 + (tt + 2) * 128 + (8 * g + 4 * h) * 4);
#pragma unroll
        for (int j = 0; j < 4; ++j) o[4 * g + j] = (acc2[4 * g + j] - mean * c4[j]) * rstd;
      }
      u32x4 p0;
#pragma unroll
      for (int i = 0; i < 4; ++i) p0[i] = pk2(o[2 * i], o[2 * i + 1]);
      *(u32x4*)(ws + WS_K + (size_t)gt * 1024 + h * 512 + r32 * 16) = p0;
      float n2 = 0.f;
#pragma unroll
      for (int i = 0; i < 4; ++i) n2 = dot2(p0[i], p0[i], n2);
      n2 = wave_max(swap32_sum(n2));
      if (lane == 0) *(float*)(ws + WS_KMAX + gt * 4) = n2;
    } else {
      const float cv = LDS_F32(sc + 1536 + tt * 128 + r32 * 4) + LDS_F32(sc + 1536 + (tt + 2) * 128 + r32 * 4);
      const lds_ptr sp = sc + 2048 + tt * 4096;
      LGKM0();
      if (h == 0) { f32x2 st; st[0] = mean; st[1] = rstd; LDS_F32X2(sp + r32 * 8) = st; }
      float o[16];
#pragma unroll
      for (int g = 0; g < 4; ++g) {
        const f32x4 s01 = LDS_F32X4(sp + (8 * g + 4 * h) * 8), s23 = LDS_F32X4(sp + (8 * g + 4 * h) * 8 + 16);
        o[4 * g + 0] = (acc2[4 * g + 0] - s01[0] * cv) * s01[1];
        o[4 * g + 1] = (acc2[4 * g + 1] - s01[2] * cv) * s01[3];
        o[4 * g + 2] = (acc2[4 * g + 2] - s23[0] * cv) * s23[1];
        o[4 * g + 3] = (acc2[4 * g + 3] - s23[2] * cv) * s23[3];
      }
      u32x4 p0, p1;
#pragma unroll
      for (int i = 0; i < 4; ++i) { p0[i] = pk2(o[2 * i], o[2 * i + 1]); p1[i] = pk2(o[8 + 2 * i], o[9 + 2 * i]); }
      if (r32 < 16) {
        char* dst = ws + WS_V + (size_t)gt * 1024 + h * 256 + r32 * 16;
        *(u32x4*)dst = p0;
        *(u32x4*)(dst + 512) = p1;
      }
    }
  }
}

constexpr int K2_PSZ = 2176;
constexpr int K2_RED = 0;
constexpr int K2_Q = 64 * K2_PSZ;
constexpr int K2_NM = K2_Q + 8192;
constexpr int K2_JUNK = K2_NM + 8192;
constexpr int K2_FLG = K2_JUNK + 256;
constexpr int K2_OB = K2_FLG + 512;
constexpr int K2_LDS = K2_OB + 2 * 1152;
constexpr int K2_MX = 0;

#define E_(r) "v_exp_f32 v" #r ", v" #r "\n\t"
#define C_(d, a, b) "v_cvt_pk_bf16_f32 v" #d ", v" #a ", v" #b "\n\t"
#define PVPREV_ "v_mfma_f32_32x32x16_bf16 v[200:215], %8, v[236:239], v[200:215]\n\t"
#define TILE_A1 E_(168) E_(169)
#define TILE_A2 E_(170) E_(171) E_(172) E_(173) "v_mfma_f32_32x32x16_bf16 v[184:199], %6, %7, v[216:231]\n\t" E_(174) E_(175) \
    C_(232, 168, 169) C_(233, 170, 171) C_(234, 172, 173) C_(235, 174, 175) E_(176) E_(177)
#define TILE_A3 E_(178) E_(179) E_(180) E_(181) E_(182) E_(183) C_(236, 176, 177) C_(237, 178, 179) C_(238, 180, 181) C_(239, 182, 183)
#define TILE_B1 E_(184) E_(185)
#define TILE_B2 E_(186) E_(187) E_(188) E_(189) "v_mfma_f32_32x32x16_bf16 v[168:183], %6, %7, v[216:231]\n\t" E_(190) E_(191) \
    C_(232, 184, 185) C_(233, 186, 187) C_(234, 188, 189) C_(235, 190, 191) E_(192) E_(193)
#define TILE_B3 E_(194) E_(195) E_(196) E_(197) E_(198) E_(199) C_(236, 192, 193) C_(237, 194, 195) C_(238, 196, 197) C_(239, 198, 199)
#define PV0_ "v_mfma_f32_32x32x16_bf16 v[200:215], %9, v[232:235], v[200:215]\n\t"
#define PV0Z_ "v_mfma_f32_32x32x16_bf16 v[200:215], %9, v[232:235], 0\n\t"
#define MF_(D, A, B, C) "v_mfma_f32_32x32x16_bf16 " D ", " A ", " B ", " C "\n\t"
#define XT_A(KN, QN, VP, V0) E_(168) E_(169) MF_("v[200:215]", VP, "v[236:239]", "v[200:215]") E_(170) E_(171) E_(172) E_(173) MF_("v[184:199]", KN, QN, "v[216:231]") E_(174) E_(175) \
    C_(232, 168, 169) C_(233, 170, 171) C_(234, 172, 173) C_(235, 174, 175) E_(176) E_(177) MF_("v[200:215]", V0, "v[232:235]", "v[200:215]") TILE_A3
#define XT_B(KN, QN, VP, V0) E_(184) E_(185) MF_("v[200:215]", VP, "v[236:239]", "v[200:215]") E_(186) E_(187) E_(188) E_(189) MF_("v[168:183]", KN, QN, "v[216:231]") E_(190) E_(191) \
    C_(232, 184, 185) C_(233, 186, 187) C_(234, 188, 189) C_(235, 190, 191) E_(192) E_(193) MF_("v[200:215]", V0, "v[232:235]", "v[200:215]") TILE_B3
#define K2_ASM_MID() asm volatile(MF_("v[200:215]", "%26", "v[232:235]", "0") TILE_A3 \
    XT_B("%7", "%6", "%13", "%14") XT_A("%8", "%6", "%15", "%16") XT_B("%9", "%6", "%17", "%18") XT_A("%10", "%6", "%19", "%20") XT_B("%11", "%6", "%21", "%22") XT_A("%12", "%6", "%23", "%24") \
    : "+{v[168:183]}"(sa), "+{v[184:199]}"(sb), "+{v[200:215]}"(o), "+{v[232:235]}"(p0), "+{v[236:239]}"(p1), "+{v[216:231]}"(negm) \
    : "v"(qf), "v"(kf[2]), "v"(kf[3]), "v"(kf[4]), "v"(kf[5]), "v"(kf[6]), "v"(kf[7]), "v"(vf[0][1]), "v"(vf[1][0]), "v"(vf[1][1]), "v"(vf[2][0]), \
      "v"(vf[2][1]), "v"(vf[3][0]), "v"(vf[3][1]), "v"(vf[4][0]), "v"(vf[4][1]), "v"(vf[5][0]), "v"(vf[5][1]), "v"(vf[6][0]), "v"(vf[6][0]), "v"(vf[0][0]))
#define K2_ASM(TXT, KN, QN, VP, V0) asm volatile(TXT : "+{v[168:183]}"(sa), "+{v[184:199]}"(sb), "+{v[200:215]}"(o), "+{v[232:235]}"(p0), "+{v[236:239]}"(p1), "+{v[216:231]}"(negm) \
    : "v"(KN), "v"(QN), "v"(VP), "v"(V0))

extern "C" __global__ __launch_bounds__(512) void k2_attn(const u16* __restrict__ x, const u16* __restrict__ wd, char* __restrict__ ws) {
  extern __shared__ __attribute__((aligned(16))) char smem[];
  const lds_ptr lds = (lds_ptr)smem;
  const unsigned lds0 = (unsigned)(uintptr_t)smem;
  const int tid = threadIdx.x, lane = tid & 63;
  const int w = (int)rfl(tid >> 6);
  const int g8 = (blockIdx.x & 7) * 32 + (blockIdx.x >> 3);
  const int r32 = lane & 31, h = lane >> 5;
  const u32x4 qown = *(const u32x4*)(ws + WS_Q + (size_t)(g8 * 8 + (r32 & 7)) * 1024 + (w * 4 + (r32 >> 3)) * 32 + h * 16);
  const float km2l = *(const float*)(ws + WS_KMAX + lane * 4);
  bf16x8 kf[8], vf[8][2];
  u32x4 vr[8];
  const char* ksrc = ws + WS_K + (size_t)w * 8192 + lane * 16;
  const char* vsrc = ws + WS_V + (size_t)w * 8192 + (r32 >> 4) * 512 + h * 256 + (lane & 15) * 16;
#define K2_LOADKV(j) do { kf[j] = *(const bf16x8*)(ksrc + (j) * 1024); vr[j] = *(const u32x4*)(vsrc + (j) * 1024); } while (0)
#define K2_SPLITV(j) do { u32x4 a_, b_; _Pragma("unroll") for (int i_ = 0; i_ < 4; ++i_) { \
    const auto r_ = __builtin_amdgcn_permlane16_swap(vr[j][i_], vones, false, false); a_[i_] = r_[0]; b_[i_] = r_[1]; } \
    vf[j][0] = __builtin_bit_cast(bf16x8, a_); vf[j][1] = __builtin_bit_cast(bf16x8, b_); } while (0)
  const unsigned vones = (lane & 11) == 0 ? ONES : 0u;
  K2_LOADKV(0); K2_LOADKV(1);
  if (w < 4) __builtin_amdgcn_s_setprio(2); else __builtin_amdgcn_s_setprio(1);
  if (tid >= 8 && tid < 128) LDS_VU32(lds + K2_FLG + tid * 4) = 0u;
  {
    float n2 = 0.f;
#pragma unroll
    for (int i = 0; i < 4; ++i) n2 = dot2(qown[i], qown[i], n2);
    const float mref = sqrtf(swap32_sum(n2) * wave_max(km2l));
    const f32x4 nm = {-mref, -mref, -mref, -mref};
    LDS_U32X4(lds + K2_Q + w * 1024 + lane * 16) = qown;
    LDS_F32X4(lds + K2_NM + w * 1024 + lane * 16) = nm;
    LDS_VU32(lds + K2_FLG + w * 4) = __any(!(mref <= 40.f)) ? 1u : 0u;
  }
  BAR();
  const lds_ptr qp = lds + K2_Q + lane * 16;
  f32x16 negm = {};
  const bool slow = __any(LDS_VU32(lds + K2_FLG + (lane & 7) * 4) != 0u);
  if (slow) {
    bf16x8 kf[8];
#pragma unroll
    for (int j = 0; j < 8; ++j) kf[j] = *(const bf16x8*)(ksrc + j * 1024);
    const f32x16 zero = {};
    for (int g = 0; g < 8; ++g) {
      const bf16x8 qf = lds_b128(qp + g * 1024);
      float mx = -3.0e38f;
#pragma unroll
      for (int j = 0; j < 8; ++j) {
        const f32x16 s = MFMA32(kf[j], qf, zero);
#pragma unroll
        for (int i = 0; i < 16; ++i) mx = fmaxf(mx, s[i]);
      }
      mx = swap32_max(mx);
      if (h == 0) LDS_F32(lds + K2_MX + (g * 8 + w) * 128 + r32 * 4) = mx;
    }
    BAR();
    float mx = LDS_F32(lds + K2_MX + w * 1024 + r32 * 4);
#pragma unroll
    for (int i = 1; i < 8; ++i) mx = fmaxf(mx, LDS_F32(lds + K2_MX + w * 1024 + i * 128 + r32 * 4));
    const f32x4 nm = {-mx, -mx, -mx, -mx};
    LDS_F32X4(lds + K2_NM + w * 1024 + lane * 16) = nm;
    BAR();
  }
  else {
    float nm = LDS_F32(lds + K2_NM + lane * 16);
#pragma unroll
    for (int g = 1; g < 8; ++g) nm = fminf(nm, LDS_F32(lds + K2_NM + g * 1024 + lane * 16));
#pragma unroll
    for (int i = 0; i < 16; ++i) negm[i] = nm;
  }
#define K2_LOAD_NEGM(g) asm volatile("ds_read_b128 v[216:219], %1\n\tds_read_b128 v[220:223], %1\n\tds_read_b128 v[224:227], %1\n\tds_read_b128 v[228:231], %1\n\ts_waitcnt lgkmcnt(0)" \
    : "+{v[216:231]}"(negm) : "v"(lds0 + K2_NM + (g) * 1024 + lane * 16) : "memory")
  if (slow) K2_LOAD_NEGM(0);
  f32x16 o = {}, sa, sb = {};
  u32x4 p0 = {}, p1 = {};
  bf16x8 qf = lds_b128(qp);
  sa = MFMA32(kf[0], qf, negm);
  K2_LOADKV(2); K2_LOADKV(3); K2_LOADKV(4);
  const lds_ptr redw = lds + K2_RED + w * K2_PSZ + lane * 16;
#define K2_WRITE_PARTIAL(g) do { const lds_ptr rp_ = redw + (g) * (8 * K2_PSZ); f32x4 t0_, t1_; \
    t0_[0] = o[0]; t0_[1] = o[1]; t0_[2] = o[2]; t0_[3] = o[3]; t1_[0] = o[4]; t1_[1] = o[5]; t1_[2] = o[6]; t1_[3] = o[7]; \
    LDS_F32X4(rp_) = t0_; LDS_F32X4(rp_ + 1024) = t1_; LDS_F32(rp_ + 2048 - lane * 16 + r32 * 4) = o[8]; \
    asm volatile("" ::: "memory"); LDS_VU32(lds + K2_FLG + 32 + ((g) * 8 + w) * 4) = 1u; } while (0)
#define VMWAIT0() __builtin_amdgcn_s_waitcnt(0x0F70)
#define SPL(j) do { if (FIRST_) { K2_SPLITV(j); asm volatile("s_nop 1" : "+v"(vf[j][0]), "+v"(vf[j][1])); } } while (0)
#define K2_ITER(g, FIRST) do { constexpr bool FIRST_ = FIRST; \
    const bf16x8 qn = lds_b128(qp + (((g) + 1) & 7) * 1024); \
    if (FIRST) { \
      SPL(0); K2_ASM(TILE_A1 TILE_A2 PV0Z_ TILE_A3, kf[1], qf, vf[0][0], vf[0][0]); \
      K2_LOADKV(5); SPL(1); K2_ASM(TILE_B1 PVPREV_ TILE_B2 PV0_ TILE_B3, kf[2], qf, vf[0][1], vf[1][0]); \
      K2_LOADKV(6); SPL(2); K2_ASM(TILE_A1 PVPREV_ TILE_A2 PV0_ TILE_A3, kf[3], qf, vf[1][1], vf[2][0]); \
      K2_LOADKV(7); SPL(3); K2_ASM(TILE_B1 PVPREV_ TILE_B2 PV0_ TILE_B3, kf[4], qf, vf[2][1], vf[3][0]); \
      SPL(4); K2_ASM(TILE_A1 PVPREV_ TILE_A2 PV0_ TILE_A3, kf[5], qf, vf[3][1], vf[4][0]); \
      SPL(5); K2_ASM(TILE_B1 PVPREV_ TILE_B2 PV0_ TILE_B3, kf[6], qf, vf[4][1], vf[5][0]); \
      SPL(6); K2_ASM(TILE_A1 PVPREV_ TILE_A2 PV0_ TILE_A3, kf[7], qf, vf[5][1], vf[6][0]); \
      SPL(7); \
    } else { K2_ASM(TILE_A1 PVPREV_ TILE_A2, kf[1], qf, vf[7][1], vf[0][0]); K2_WRITE_PARTIAL((g) - 1); K2_ASM_MID(); } \
    if (slow) K2_LOAD_NEGM(((g) + 1) & 7); \
    K2_ASM(TILE_B1 PVPREV_ TILE_B2 PV0_ TILE_B3, kf[0], qn, vf[6][1], vf[7][0]); \
    qf = qn; } while (0)

  asm volatile("s_nop 15" : "+{v[168:183]}"(sa));
  K2_ITER(0, true);
  VMWAIT0();
#pragma unroll
  for (int g = 1; g < 8; ++g) {
    if (g <= 5 && w < 4) {
      const int ln = ((g - 1) * 128 + (blockIdx.x >> 3) * 4 + w) * 64 + lane;
      const char* src = ln < 11008 ? (const char*)wd + ln * 128 : ln < 15104 ? ws + WS_WO + (ln - 11008) * 128 : ws + WS_WGU + (ln < 37120 ? ln - 15104 : 0) * 128;
      glds4(src, lds0 + K2_JUNK);
    }
    if (g == 7 && w == 0 && blockIdx.x < 32) {
      const char *c0 = CODE_ADDR(k4_down), *c1 = CODE_ADDR(k_end_marker);
      const char* src = c0 + ((blockIdx.x >> 3) * 64 + lane) * 128;
      glds4(src < c1 ? src : c0, lds0 + K2_JUNK);
    }
    K2_ITER(g, false);
  }
  K2_ASM("s_nop 1\n\t" PVPREV_ "s_nop 15", kf[0], qf, vf[7][1], vf[7][0]);
  K2_WRITE_PARTIAL(7);
  if (w >= 4) return;
  asm volatile("s_waitcnt vmcnt(0)" ::: "memory");
  VMWAIT0();
  const int g4 = lane >> 4, tok8 = lane & 7;
  const char* wsrc = ws + WS_WO + (size_t)w * 16384 + lane * 16;
  bf16x8 wr[5][8];
#define K2_WLOAD(hh) do { if ((hh) < 16) { _Pragma("unroll") for (int i_ = 0; i_ < 8; ++i_) wr[(hh) % 5][i_] = *(const bf16x8*)(wsrc + (size_t)((hh) >> 1) * 65536 + ((hh) & 1) * 8192 + i_ * 1024); } } while (0)
  f32x4 oacc[8];
  const float piv = __uint_as_float((unsigned)x[(size_t)(g8 * 8 + tok8) * DM + w * 128] << 16);
  {
    u32x4 xr[4];
#pragma unroll
    for (int r = 0; r < 4; ++r) xr[r] = *(const u32x4*)(x + (size_t)(g8 * 8 + tok8) * DM + w * 128 + g4 * 32 + r * 8);
    K2_WLOAD(0); K2_WLOAD(1);
#pragma unroll
    for (int r = 0; r < 4; ++r)
#pragma unroll
      for (int i = 0; i < 4; ++i) { oacc[2 * i][r] = bflo(xr[r][i]); oacc[2 * i + 1][r] = bfhi(xr[r][i]); }
  }
#define K2_GAP(g, f) do { if ((g) < 7 && ((f) & 3) == 3) __builtin_amdgcn_s_sleep(1); } while (0)
#define K2_PROJ(g) do { \
    while (!__all(LDS_VU32(lds + K2_FLG + 32 + ((g) * 8 + tok8) * 4) != 0u)) __builtin_amdgcn_s_sleep((g) < 7 ? 2 : 1); \
    asm volatile("" ::: "memory"); \
    { const lds_ptr rp_ = lds + K2_RED + (g) * (8 * K2_PSZ) + (w >> 1) * 1024 + lane * 16 + (w & 1) * 8, lp_ = lds + K2_RED + (g) * (8 * K2_PSZ) + 2048 + r32 * 4; \
      f32x2 t_[8]; float l_[8]; \
      _Pragma("unroll") for (int i_ = 0; i_ < 8; ++i_) { t_[i_] = LDS_F32X2(rp_ + i_ * K2_PSZ); l_[i_] = LDS_F32(lp_ + i_ * K2_PSZ); } \
      SB(); K2_WLOAD(2 * (g) + 2); SB(); \
      const f32x2 ts_ = ((t_[0] + t_[1]) + (t_[2] + t_[3])) + ((t_[4] + t_[5]) + (t_[6] + t_[7])); \
      const float inv_ = __builtin_amdgcn_rcpf(((l_[0] + l_[1]) + (l_[2] + l_[3])) + ((l_[4] + l_[5]) + (l_[6] + l_[7]))); \
      LDS_VU32(lds + K2_OB + ((g) & 1) * 1152 + (r32 & 7) * 144 + (r32 >> 3) * 32 + h * 8 + (w >> 1) * 16 + (w & 1) * 4) = pk2(ts_[0] * inv_, ts_[1] * inv_); \
      LDS_VU32(lds + K2_FLG + 288 + ((g) * 4 + w) * 4) = 1u; } \
    SB(); K2_WLOAD(2 * (g) + 3); SB(); \
    while (!__all(LDS_VU32(lds + K2_FLG + 288 + ((g) * 4 + (lane & 3)) * 4) != 0u)) __builtin_amdgcn_s_sleep(1); \
    asm volatile("" ::: "memory"); \
    const bf16x8 b0_ = lds_b128(lds + K2_OB + ((g) & 1) * 1152 + tok8 * 144 + g4 * 16), b1_ = lds_b128(lds + K2_OB + ((g) & 1) * 1152 + tok8 * 144 + 64 + g4 * 16); \
    _Pragma("unroll") for (int f_ = 0; f_ < 8; ++f_) { oacc[f_] = MFMA16(wr[(2 * (g)) % 5][f_], b0_, oacc[f_]); PINM(oacc[f_]); K2_GAP(g, f_); } \
    _Pragma("unroll") for (int f_ = 0; f_ < 8; ++f_) { oacc[f_] = MFMA16(wr[(2 * (g) + 1) % 5][f_], b1_, oacc[f_]); PINM(oacc[f_]); K2_GAP(g, f_); } } while (0)
  K2_PROJ(0); K2_PROJ(1); K2_PROJ(2); K2_PROJ(3); K2_PROJ(4); K2_PROJ(5); K2_PROJ(6); K2_PROJ(7);
  {
    const int tok = g8 * 8 + tok8;
    u32x4 hb[4];
#pragma unroll
    for (int r = 0; r < 4; ++r)
#pragma unroll
      for (int i = 0; i < 4; ++i) hb[r][i] = pk2(oacc[2 * i][r], oacc[2 * i + 1][r]);
    if ((lane & 8) == 0) {
#pragma unroll
      for (int r = 0; r < 4; ++r) *(u32x4*)(ws + WS_H + (size_t)(tok >> 5) * 32768 + (w * 16 + g4 * 4 + r) * 512 + (tok & 31) * 16) = hb[r];
    }
    float sx = 0.f, sxx = 0.f;
#pragma unroll
    for (int r = 0; r < 4; ++r)
#pragma unroll
      for (int i = 0; i < 4; ++i) { sx = dot2(hb[r][i], ONES, sx); sxx = dot2(hb[r][i], hb[r][i], sxx); }
    const float d1 = swap32_sum(swap16_sum(sx - 32.f * piv)), d2 = swap32_sum(swap16_sum(__builtin_fmaf(-2.f * piv, sx, sxx) + 32.f * piv * piv));
    f32x2 st; st[0] = piv + d1 * (1.f / 128); st[1] = fmaxf(d2 - d1 * d1 * (1.f / 128), 0.f);
    if (lane < 8) *(f32x2*)(ws + WS_ST + ((size_t)((tok >> 5) * 4 + w) * 32 + (tok & 31)) * 8) = st;
  }
}

constexpr int K4_SLOT = 32768, K4_LDS = 5 * K4_SLOT;

extern "C" __global__ __launch_bounds__(512) void k4_down(const u16* __restrict__ W, char* __restrict__ ws, u16* __restrict__ out) {
  constexpr int NKS = NAT;
  extern __shared__ __attribute__((aligned(16))) char smem[];
  const lds_ptr lds = (lds_ptr)smem;
  const unsigned lds0 = (unsigned)(uintptr_t)smem;
  const int tid = threadIdx.x, lane = tid & 63;
  const int w = (int)rfl(tid >> 6);
  const int bid = blockIdx.x;
  const int tb = (bid & 7) * 4 + ((bid >> 3) & 3), nb = bid >> 5;
  const int r32 = lane & 31, h = lane >> 5;
  if (w >= 4) {
    const int lw = w - 4;
    const int rr = lane >> 3, pos = lane & 7;
    const char* wcol = (const char*)W + nb * 128 + (pos ^ (((rr >> 1) & 1) << 2)) * 16;
    const char* wsrc = wcol + (size_t)(64 * (lw >> 1) + 8 * rr + 4 * (lw & 1)) * 1024;
    const char* wlast = wcol + (size_t)(1344 + 8 * (rr & 3) + (rr >> 2)) * 1024;
    const char* asrc = ws + WS_ACT + ((size_t)(tb * 2 + (lw >> 1)) * NKS + (lw & 1) * 4) * 1024 + lane * 16;
#define K4_ISSUE(st) do { const unsigned sl_ = lds0 + ((st) % 5) * K4_SLOT; _Pragma("unroll") for (int i = 0; i < 4; ++i) { \
    const bool oka_ = (st) < 10 || !((lw & 1) && i >= 2); \
    glds16(asrc + (oka_ ? (st) * 8192 + i * 1024 : 0), sl_ + 16384 + (lw * 4 + i) * 1024); \
    glds16(((st) < 10 || lw < 2) ? wsrc + (size_t)(st) * 131072 + i * 1024 : wlast + i * 2048, sl_ + (lw * 4 + i) * 1024); } } while (0)
    {
      K4_ISSUE(0); K4_ISSUE(1); K4_ISSUE(2);
      WAIT_VM(16); RAWBAR(); K4_ISSUE(3); K4_ISSUE(4);
      WAIT_VM(24); RAWBAR(); K4_ISSUE(5);
      WAIT_VM(24); RAWBAR(); K4_ISSUE(6);
      WAIT_VM(24); RAWBAR(); K4_ISSUE(7);
      WAIT_VM(24); RAWBAR(); K4_ISSUE(8);
      WAIT_VM(24); RAWBAR(); K4_ISSUE(9);
      WAIT_VM(24); RAWBAR(); K4_ISSUE(10);
      WAIT_VM(24); RAWBAR();
      WAIT_VM(16); RAWBAR();
      WAIT_VM(8); RAWBAR();
      WAIT_VM(0); RAWBAR();
    }
    return;
  }
  const int nt = w & 1, tt = w >> 1;
  const int tok = tb * 64 + tt * 32 + r32;
  const int ncol = nb * 64 + nt * 32 + h * 16;
  const int blk = (lane >> 4) & 1, q = (lane & 15) >> 2, p = lane & 3;
  const char* hfrag = ws + WS_H + (size_t)(tok >> 5) * 32768 + (ncol >> 3) * 512 + r32 * 16;
  const u32x4 h0 = *(const u32x4*)(hfrag), h1 = *(const u32x4*)(hfrag + 512);
  f32x16 acc = {};
  const int cbyte = nt * 64 + (16 * (p & 1) + 8 * blk + 4 * (p >> 1)) * 2;
  lds_ptr ap[3], bp[3];
#pragma unroll
  for (int u = 0; u < 3; ++u) { ap[u] = lds + u * 2 * K4_SLOT + (4 * h + q) * 128 + (cbyte ^ ((q >> 1) << 6)); bp[u] = lds + u * 2 * K4_SLOT + 16384 + tt * 8192 + lane * 16; }
  s16x4 fa[2][4][2]; bf16x8 fb_[2][4];
#define K4_U(st) (((st) % 5) >> 1)
#define K4_O(st) ((((st) % 5) & 1) * K4_SLOT)
#define K4_LD1(bu, st, k0, k) do { fa[bu][k][0] = lds_tr(ap[K4_U(st)] + K4_O(st) + ((k0) + (k)) * 2048); fa[bu][k][1] = lds_tr(ap[K4_U(st)] + K4_O(st) + ((k0) + (k)) * 2048 + 1024); \
    fb_[bu][k] = lds_b128(bp[K4_U(st)] + K4_O(st) + ((k0) + (k)) * 1024); } while (0)
#define K4_STEP(bu, NM, NL, st, k0) do { _Pragma("unroll") for (int k = 0; k < 4; ++k) { \
    if (k < (NM)) { acc = MFMA32(cat8(fa[bu][k][0], fa[bu][k][1]), fb_[bu][k], acc); PINM(acc); } \
    if (k < (NL)) K4_LD1(1 - (bu), st, k0, k); SB(); } } while (0)
#define K4_STAGE(st, NK2) do { BAR(); K4_STEP(1, (st) > 0 ? 4 : 0, 4, st, 0); K4_STEP(0, 4, NK2, st, 4); } while (0)
  K4_STAGE(0, 4); K4_STAGE(1, 4); K4_STAGE(2, 4); K4_STAGE(3, 4);
  K4_STAGE(4, 4); K4_STAGE(5, 4); K4_STAGE(6, 4); K4_STAGE(7, 4); K4_STAGE(8, 4); K4_STAGE(9, 4); K4_STAGE(10, 2);
  K4_STEP(1, 2, 0, 0, 0);
  u32x4 o0, o1;
#pragma unroll
  for (int i = 0; i < 4; ++i) {
    o0[i] = pk2(acc[2 * i] + bflo(h0[i]), acc[2 * i + 1] + bfhi(h0[i]));
    o1[i] = pk2(acc[8 + 2 * i] + bflo(h1[i]), acc[9 + 2 * i] + bfhi(h1[i]));
  }
  u16* dst = out + (size_t)tok * DM + ncol;
  *(u32x4*)dst = o0;
  *(u32x4*)(dst + 8) = o1;
}

constexpr int K3_SLOT = 12288, K3_FLG = 13 * K3_SLOT, K3_SC = K3_FLG + 64, K3_LDS = K3_SC + 768;

extern "C" __global__ __launch_bounds__(512) void k3_gateup(const u16* __restrict__ wd, char* __restrict__ ws) {
  extern __shared__ __attribute__((aligned(16))) char smem[];
  const lds_ptr lds = (lds_ptr)smem;
  const unsigned lds0 = (unsigned)(uintptr_t)smem;
  const int tid = threadIdx.x, lane = tid & 63;
  const int w = (int)rfl(tid >> 6);
  const int bid = blockIdx.x, jx = bid >> 3;
  if (jx >= 30) {
    const char* src = (const char*)wd + (size_t)(((jx & 1) * 8 + w) * 64 + lane) * 128;
#pragma unroll
    for (int i = 0; i < 6; ++i) glds4(src + i * 131072, lds0 + w * 256);
    return;
  }
  const int rb = (bid & 7) * 2 + (jx & 1), cb = jx >> 1;
  const int at0 = cb * 6 > 80 ? 80 : cb * 6;
  if (w >= 4) {
    const int lw = w - 4;
    const char* wsrc[3];
#pragma unroll
    for (int i = 0; i < 3; ++i) wsrc[i] = ws + WS_WGU + (size_t)(at0 + ((lw * 3 + i) >> 1)) * 32768 + ((lw * 3 + i) & 1) * 1024 + lane * 16;
#define K3_ISSUE(st) do { if ((st) >= 13) WAIT_CNT(lds + K3_FLG + 16, (st) - 12); \
    _Pragma("unroll") for (int i = 0; i < 3; ++i) glds16(wsrc[i] + (st) * 2048, lds0 + ((st) % 13) * K3_SLOT + (lw * 3 + i) * 1024); } while (0)
#define K3_PUB(n) LDS_VU32(lds + K3_FLG + lw * 4) = (n)
    K3_ISSUE(0); K3_ISSUE(1);
    RAWBAR();

    K3_ISSUE(2); WAIT_VM(6); K3_PUB(1);
    K3_ISSUE(3); WAIT_VM(6); K3_PUB(2);
    K3_ISSUE(4); WAIT_VM(6); K3_PUB(3);
    K3_ISSUE(5); WAIT_VM(6); K3_PUB(4);
    K3_ISSUE(6); WAIT_VM(6); K3_PUB(5);
    K3_ISSUE(7); WAIT_VM(6); K3_PUB(6);
    K3_ISSUE(8); WAIT_VM(6); K3_PUB(7);
    K3_ISSUE(9); WAIT_VM(6); K3_PUB(8);
    K3_ISSUE(10); WAIT_VM(6); K3_PUB(9);
    K3_ISSUE(11); WAIT_VM(6); K3_PUB(10);
    K3_ISSUE(12); WAIT_VM(6); K3_PUB(11);
    K3_ISSUE(13); WAIT_VM(6); K3_PUB(12);
    K3_ISSUE(14); WAIT_VM(6); K3_PUB(13);
    K3_ISSUE(15); WAIT_VM(6); K3_PUB(14);
    WAIT_VM(3); K3_PUB(15);
    WAIT_VM(0); K3_PUB(16);
    if (lw < 3) {
      const int i = lw * 64 + lane;
      const float* cp = (const float*)(ws + WS_CS) + ((i >> 4) & 1) * (8 * 1408) + ffeat(at0 + (i >> 5), i & 15);
      float c[8];
#pragma unroll
      for (int j = 0; j < 8; ++j) c[j] = cp[j * 1408];
      LDS_F32(lds + K3_SC + i * 4) = ((c[0] + c[1]) + (c[2] + c[3])) + ((c[4] + c[5]) + (c[6] + c[7]));
    }
    BAR();
    return;
  }
  const int r32 = lane & 31, h = lane >> 5;
  const char* bsrc = ws + WS_H + (size_t)(rb * 4 + w) * 32768 + lane * 16;
  constexpr int K3_BD = 8;
  bf16x8 fb_[K3_BD];
#define K3_LDB(kk) fb_[(kk) % K3_BD] = *(const bf16x8*)(bsrc + (kk) * 1024)
#pragma unroll
  for (int i = 0; i < 4; ++i) K3_LDB(i);
  if (tid < 8) LDS_VU32(lds + K3_FLG + tid * 4) = 0u;
  BAR();
#pragma unroll
  for (int i = 4; i < K3_BD; ++i) K3_LDB(i);
  f32x16 acc[6] = {};
  lds_ptr ap[3];
#pragma unroll
  for (int u = 0; u < 3; ++u) { ap[u] = lds + u * 5 * K3_SLOT + lane * 16; PIN(ap[u]); }
  bf16x8 fa[2][6];
  u32x4 rdy;
#define K3_A(kk, i) lds_b128(ap[(((kk) >> 1) % 13) / 5] + ((((kk) >> 1) % 13) % 5) * K3_SLOT + (i) * 2048 + ((kk) & 1) * 1024)
#define K3_STEP(kk) do { constexpr int n_ = (kk) + 2; \
    if (n_ < 32 && (n_ & 1) == 0 && __builtin_expect(rfl(min4(rdy)) < n_ / 2u + 1u, 0)) WAIT_CNT(lds + K3_FLG, n_ / 2 + 1); \
    _Pragma("unroll") for (int i = 0; i < 6; ++i) { \
      acc[i] = MFMA32(fa[(kk) & 1][i], fb_[(kk) % K3_BD], acc[i]); PINM(acc[i]); \
      if (i == 0 && (kk) > 0 && (kk) - 1 + K3_BD < 32) K3_LDB((kk) - 1 + K3_BD); \
      if (n_ < 32) fa[(kk) & 1][i] = K3_A(n_, i); \
      if ((n_ & 1) == 1 && i == 2) rdy = LDS_VU32X4(lds + K3_FLG); \
      SB(); } \
    if (((kk) & 1) == 1 && (kk) < 6) LDS_VU32(lds + K3_FLG + 16 + w * 4) = ((kk) + 1) / 2;   } while (0)
#define K3_STEP4(kk) do { K3_STEP(kk); K3_STEP((kk) + 1); K3_STEP((kk) + 2); K3_STEP((kk) + 3); } while (0)
  WAIT_CNT(lds + K3_FLG, 1);
#pragma unroll
  for (int i = 0; i < 6; ++i) { fa[0][i] = K3_A(0, i); fa[1][i] = K3_A(1, i); }
  rdy = LDS_VU32X4(lds + K3_FLG);
  K3_STEP4(0); K3_STEP4(4);
  const f32x2 lp0 = *(const f32x2*)(ws + WS_ST + ((size_t)((rb * 4 + w) * 4 + h * 2) * 32 + r32) * 8), lp1 = *(const f32x2*)(ws + WS_ST + ((size_t)((rb * 4 + w) * 4 + h * 2 + 1) * 32 + r32) * 8);
  K3_STEP4(8); K3_STEP4(12); K3_STEP4(16); K3_STEP4(20); K3_STEP4(24);
  float mean = swap32_sum(lp0[0] + lp1[0]) * 0.25f;
  float rstd = ln_rstd(swap32_sum(lp0[1] + lp1[1] + 128.f * ((lp0[0] - mean) * (lp0[0] - mean) + (lp1[0] - mean) * (lp1[0] - mean))) * (1.f / DM));
  mean = rbf(mean);
  PIN(mean); PIN(rstd);
  BAR();
  K3_STEP4(28);
  const float ke = -1.4426950408889634f * rstd, ir2 = __builtin_amdgcn_rcpf(rstd * rstd);
  const f32x2 nm2 = {-mean, -mean}, ke2 = {ke, ke}, ir22 = {ir2, ir2};
  f32x4 call[6][4];
#pragma unroll
  for (int i = 0; i < 6; ++i)
#pragma unroll
    for (int g = 0; g < 4; ++g) call[i][g] = LDS_F32X4(lds + K3_SC + i * 128 + (8 * g + 4 * h) * 4);
  SB();
#pragma unroll
  for (int i = 0; i < 6; ++i) {
    const f32x4 (&c4)[4] = call[i];
    f32x2 tg[4], tu[4], e[4];
#pragma unroll
    for (int j = 0; j < 4; ++j) {
      const f32x2 ag2 = {acc[i][2 * j], acc[i][2 * j + 1]}, au2 = {acc[i][8 + 2 * j], acc[i][9 + 2 * j]};
      const f32x2 cg2 = {c4[j >> 1][2 * (j & 1)], c4[j >> 1][2 * (j & 1) + 1]}, cu2 = {c4[2 + (j >> 1)][2 * (j & 1)], c4[2 + (j >> 1)][2 * (j & 1) + 1]};
      tg[j] = __builtin_elementwise_fma(nm2, cg2, ag2); tu[j] = __builtin_elementwise_fma(nm2, cu2, au2);
      e[j] = tg[j] * ke2;
    }
#pragma unroll
    for (int j = 0; j < 4; ++j) { e[j][0] = __builtin_amdgcn_exp2f(e[j][0]); e[j][1] = __builtin_amdgcn_exp2f(e[j][1]); tg[j] = tg[j] * tu[j]; }
#pragma unroll
    for (int j = 0; j < 4; ++j) e[j] = __builtin_elementwise_fma(e[j], ir22, ir22);
#pragma unroll
    for (int j = 0; j < 4; ++j) { e[j][0] = __builtin_amdgcn_rcpf(e[j][0]); e[j][1] = __builtin_amdgcn_rcpf(e[j][1]); }
    u32x4 pk;
#pragma unroll
    for (int j = 0; j < 4; ++j) { const f32x2 a2 = tg[j] * e[j]; pk[j] = pk2(a2[0], a2[1]); }
    *(u32x4*)(ws + WS_ACT + ((size_t)(rb * 4 + w) * NAT + at0 + i) * 1024 + lane * 16) = pk;
  }
}

extern "C" __global__ void k_end_marker() {}

extern "C" void kernel_launch(void* const* d_in, const int* in_sizes, int n_in, void* d_out, int out_size, void* d_ws, size_t ws_size,
                              hipStream_t stream) {
  const u16* x = (const u16*)d_in[0];
  const u16* wq = (const u16*)d_in[1];
  const u16* wk = (const u16*)d_in[2];
  const u16* wv = (const u16*)d_in[3];
  const u16* wo = (const u16*)d_in[4];
  const u16* wg = (const u16*)d_in[6];
  const u16* wu = (const u16*)d_in[7];
  const u16* wd = (const u16*)d_in[8];
  char* ws = (char*)d_ws;
  (void)hipFuncSetAttribute((const void*)k1_qkv, hipFuncAttributeMaxDynamicSharedMemorySize, K1_LDS);
  (void)hipFuncSetAttribute((const void*)k2_attn, hipFuncAttributeMaxDynamicSharedMemorySize, K2_LDS);
  (void)hipFuncSetAttribute((const void*)k3_gateup, hipFuncAttributeMaxDynamicSharedMemorySize, K3_LDS);
  (void)hipFuncSetAttribute((const void*)k4_down, hipFuncAttributeMaxDynamicSharedMemorySize, K4_LDS);
  k1_qkv<<<256, 640, K1_LDS, stream>>>(x, wq, wk, wv, wo, wg, wu, ws);
  k2_attn<<<256, 512, K2_LDS, stream>>>(x, wd, ws);
  k3_gateup<<<256, 512, K3_LDS, stream>>>(wd, ws);
  k4_down<<<256, 512, K4_LDS, stream>>>(wd, ws, (u16*)d_out);
}
```
